```python
import torch

def _softmax(x, axis=-1):
    return torch.softmax(x, dim=axis)

def _gelu(x):
    return torch.nn.functional.gelu(x, approximate='tanh')

def _silu(x):
    return torch.nn.functional.silu(x)

def _sigmoid(x):
    return torch.sigmoid(x)

def _layernorm(x, axis=-1, eps=1e-05):
    m = x.mean(dim=axis, keepdim=True)
    v = x.var(dim=axis, keepdim=True, unbiased=False)
    return (x - m) / torch.sqrt(v + eps)

def _scatter_add(tgt, idx, src, axis=0):
    return tgt.clone().index_add_(axis, idx, src)

def _segment_sum(src, seg, n):
    out = torch.zeros((n,) + src.shape[1:], dtype=src.dtype)
    return out.index_add_(0, seg, src)

def _pad(x, pw):
    flat = tuple((v for pair in reversed(pw) for v in pair))
    return torch.nn.functional.pad(x, flat)
kernel_name = 'adaLN_cond__mha_block_cached__moe_arch_mod__prompt'

def setup_inputs():

    _w = lambda a: (lambda t: t.to(torch.bfloat16) if t.is_floating_point() else t)(torch.as_tensor(a))

    def _A_setup():
        B, Sq, D = (1, 2048, 512)
        H, Sk, Dh = (32, 2048, 128)
        return {'x': _w((lambda *s: torch.randn(*s, dtype=torch.bfloat16) * 0.1)(B, Sq, D)), 'k_cache': _w((lambda *s: torch.randn(*s, dtype=torch.bfloat16) * 0.8)(B, H, Sk, Dh)), 'v_cache': _w((lambda *s: torch.randn(*s, dtype=torch.bfloat16) * 1.6)(B, H, Sk, Dh)), 'wq': _w((lambda *s: torch.randn(*s, dtype=torch.bfloat16) * 0.8)(D, H * Dh) * D ** (-0.5)), 'wo': _w((lambda *s: torch.randn(*s, dtype=torch.bfloat16) * 1.6)(H * Dh, D) * (H * Dh) ** (-0.5))}
    _A_in = _A_setup()

    def _B_setup():
        B, S, D, F, C = (1, 2048, 512, 1792, 8)
        return {'x': _w((lambda *s: torch.randn(*s, dtype=torch.bfloat16) * 0.1)(B, S, D)), 'w_r': _w((lambda *s: torch.randn(*s, dtype=torch.bfloat16) * 3.2)(D)), 'w_gate': _w((lambda *s: torch.randn(*s, dtype=torch.bfloat16) * 3.2)(D, F)), 'w_up': _w((lambda *s: torch.randn(*s, dtype=torch.bfloat16) * 3.2)(D, F)), 'w_down': _w((lambda *s: torch.randn(*s, dtype=torch.bfloat16) * 3.2)(F, D))}
    _B_in = _B_setup()
    return {'A_x': _A_in['x'], 'A_k_cache': _A_in['k_cache'], 'A_v_cache': _A_in['v_cache'], 'A_wq': _A_in['wq'], 'A_wo': _A_in['wo'], 'B_x': _B_in['x'], 'B_w_r': _B_in['w_r'], 'B_w_gate': _B_in['w_gate'], 'B_w_up': _B_in['w_up'], 'B_w_down': _B_in['w_down'], 'c': _w((lambda *s: torch.randn(*s, dtype=torch.bfloat16) * 0.2)(1, 512) * 8.0), 'w_mod': _w((lambda *s: torch.randn(*s, dtype=torch.bfloat16) * 0.2)(512, 6 * 512) * 512 ** (-0.5) * 8.0)}

def reference(A_x, A_k_cache, A_v_cache, A_wq, A_wo, B_x, B_w_r, B_w_gate, B_w_up, B_w_down, c, w_mod):

    def _A_ref(x, k_cache, v_cache, wq, wo):
        B, Sq, D = x.shape
        H, Dh = (32, 128)
        q = (lambda x, *d: x.permute(*d))((x @ wq).reshape(B, Sq, H, Dh), 0, 2, 1, 3)
        scores = torch.einsum('bhqd,bhkd->bhqk', q, k_cache) * Dh ** (-0.5)
        p = _softmax(scores, axis=-1)
        o = torch.einsum('bhqk,bhkd->bhqd', p, v_cache)
        return (lambda x, *d: x.permute(*d))(o, 0, 2, 1, 3).reshape(B, Sq, H * Dh) @ wo

    def _B_ref(x, w_r, w_gate, w_up, w_down):
        B, S, D = x.shape
        C = 8
        r = x @ w_r
        idx = torch.argsort(-r, axis=-1)[:, :C]
        idx_e = (lambda x, n, axis: torch.repeat_interleave(x, n, dim=axis))(idx.reshape(B, C, 1), D, axis=2)
        x_sel = (lambda x, i, axis: torch.take_along_dim(x, i, dim=axis))(x, idx_e, axis=1)
        g = _silu(x_sel @ w_gate)
        u = x_sel @ w_up
        y = g * u @ w_down
        r_sel = _sigmoid((lambda x, i, axis: torch.take_along_dim(x, i, dim=axis))(r, idx, axis=1))
        y = y * r_sel.reshape(B, C, 1)
        b_off = (torch.arange(B) * S).reshape(B, 1)
        flat_idx = (idx + b_off).reshape(B * C)
        zero = (x * 0.0).reshape(B * S, D)
        upd = _scatter_add(zero, flat_idx, y.reshape(B * C, D), axis=0)
        return x + upd.reshape(B, S, D)
    x = A_x
    mod = (_silu(c) @ w_mod).reshape(c.shape[0], 6, 1, -1)
    sa, ca, ga, sb, cb, gb = (mod[:, 0].clone(), mod[:, 1].clone(), mod[:, 2].clone(), mod[:, 3].clone(), mod[:, 4].clone(), mod[:, 5].clone())
    h = _layernorm(x, axis=-1) * (1 + ca) + sa
    x = x + ga * _A_ref(h, A_k_cache, A_v_cache, A_wq, A_wo)
    h = _layernorm(x, axis=-1) * (1 + cb) + sb
    x = x + gb * _B_ref(h, B_w_r, B_w_gate, B_w_up, B_w_down)
    return x
```

```cpp
#include <hip/hip_runtime.h>
#include <hip/hip_bf16.h>

typedef unsigned short u16;
typedef float    f32x16 __attribute__((ext_vector_type(16)));
typedef float    f32x4  __attribute__((ext_vector_type(4)));
typedef float    f32x2  __attribute__((ext_vector_type(2)));
typedef __bf16   bf16x8 __attribute__((ext_vector_type(8)));
typedef __bf16   bf16x2 __attribute__((ext_vector_type(2)));
typedef unsigned u32x4  __attribute__((ext_vector_type(4)));
typedef unsigned u32x2  __attribute__((ext_vector_type(2)));
typedef u16      u16x8  __attribute__((ext_vector_type(8)));
typedef _Float16 f16x2  __attribute__((ext_vector_type(2)));
typedef _Float16 f16x8  __attribute__((ext_vector_type(8)));
#define DI __device__ __forceinline__
#define GAS __attribute__((address_space(1)))

constexpr int SEQ = 2048, DM = 512, NH = 32, DH = 128, FF = 1792, TOPC = 8, NEB = FF / 16;

DI float bf2f(u16 v) { return __uint_as_float((unsigned)v << 16); }
DI float bflo(unsigned w) { return __uint_as_float(w << 16); }
DI float bfhi(unsigned w) { return __uint_as_float(w & 0xffff0000u); }
DI unsigned cvtpk(float lo, float hi) { f32x2 v = {lo, hi}; bf16x2 b = __builtin_convertvector(v, bf16x2); return __builtin_bit_cast(unsigned, b); }
DI float rbf(float x) { return bflo(cvtpk(x, 0.f)); }
DI unsigned rfl(unsigned x) { return __builtin_amdgcn_readfirstlane(x); }
DI float silu_f(float x) { return x * __builtin_amdgcn_rcpf(1.f + __expf(-x)); }

#define KS_ARG
#define KS_PASS(i)
#define KS_BEGIN
#define KS_END
#define KS_MID(i)
#define KS_TARG
#define KS_TPASS
#define CODE_ADDR(name, sym) DI const char* name() { unsigned long long p; \
  asm volatile("s_getpc_b64 s[40:41]\n\ts_add_u32 s40, s40, " #sym "@rel32@lo+4\n\ts_addc_u32 s41, s41, " #sym "@rel32@hi+12\n\ts_mov_b64 %0, s[40:41]" : "=s"(p) :: "scc", "s40", "s41"); return (const char*)p; }
CODE_ADDR(code_addr_attn, k_attn) CODE_ADDR(code_addr_fin, k_end)
constexpr size_t WS_MOD   = 0;
constexpr size_t WS_R     = 16384;
constexpr size_t WS_HFRAG = 10u << 20;
constexpr size_t WS_YP    = 8u << 20;
constexpr size_t WS_XSEL  = WS_YP + (size_t)NEB * 8 * DM * 4;
constexpr size_t WS_OFRAG = 16u << 20;
constexpr size_t WS_PART  = 32u << 20;

template <int CTRL> DI float dpp(float v) { return __int_as_float(__builtin_amdgcn_update_dpp(0, __float_as_int(v), CTRL, 0xf, 0xf, false)); }
DI float xor8(float v)  { return dpp<0x128>(v); }
DI void pair16(float v, float& a, float& b) { auto r = __builtin_amdgcn_permlane16_swap(__float_as_uint(v), __float_as_uint(v), false, false); a = __uint_as_float(r[0]); b = __uint_as_float(r[1]); }
DI void pair32(float v, float& a, float& b) { auto r = __builtin_amdgcn_permlane32_swap(__float_as_uint(v), __float_as_uint(v), false, false); a = __uint_as_float(r[0]); b = __uint_as_float(r[1]); }
DI float sum_4_up(float v) {
  v += dpp<0x124>(v); v += dpp<0x128>(v);
  float a, b; pair16(v, a, b); v = a + b; pair32(v, a, b); return a + b;
}
DI float wave_sum(float v) { v += dpp<0xB1>(v); v += dpp<0x4E>(v); return sum_4_up(v); }
DI void chan(float ma, float qa, float mb, float qb, float halfn, float& m, float& q) { const float d = mb - ma; m = 0.5f * (ma + mb); q = qa + qb + d * d * halfn; }
#define PF_DELAY 10
#define XREP 1
#define TOUCH_STEP 65536u
constexpr float QSCALE = 0.12751743082f;

extern "C" __global__ __launch_bounds__(256) void k_pre(const u16* __restrict__ c, const u16* __restrict__ wmod, float* mod,
                                             const u16* __restrict__ x, u16* __restrict__ nfrag, const u16* __restrict__ wq, const u16* __restrict__ kc, const u16* __restrict__ vc,
                                             unsigned* __restrict__ keys, float* __restrict__ yp, const u16* wo, const u16* out, const u16* wg, const u16* wu, const u16* wd, const u16* wr KS_ARG) {
  KS_BEGIN
  __shared__ float red[4][32];
  const int tid = threadIdx.x, lane = tid & 63;
  if (__builtin_expect(blockIdx.x < 96, 1)) {
    const int mb = (blockIdx.x & 7) * 12 + (blockIdx.x >> 3);
    const int cg = tid & 3, rg = tid >> 2, col0 = mb * 32 + cg * 8;
    u32x4 w[8]; float sc[8];
#pragma unroll
    for (int i = 0; i < 8; ++i) w[i] = *(const u32x4*)(wmod + (size_t)(rg + 64 * i) * 3072 + col0);
#pragma unroll
    for (int i = 0; i < 8; ++i) sc[i] = rbf(silu_f(bf2f(c[rg + 64 * i])));
    float acc[8] = {0, 0, 0, 0, 0, 0, 0, 0};
#pragma unroll
    for (int i = 0; i < 8; ++i)
#pragma unroll
      for (int j = 0; j < 4; ++j) { acc[2 * j] += sc[i] * bflo(w[i][j]); acc[2 * j + 1] += sc[i] * bfhi(w[i][j]); }
#pragma unroll
    for (int j = 0; j < 8; ++j) acc[j] = sum_4_up(acc[j]);
    if (lane < 4) {
#pragma unroll
      for (int j = 0; j < 8; ++j) red[tid >> 6][cg * 8 + j] = acc[j];
    }
    __syncthreads();
    if (tid < 32) {
      const float v = rbf((red[0][tid] + red[1][tid]) + (red[2][tid] + red[3][tid]));
      mod[mb * 32 + tid] = v;
      if (mb < 32) {
        float* modA = (float*)(nfrag + SEQ * DM);
        const int ks = mb & 15, a = tid >> 4, g = (tid >> 2) & 3, e = tid & 3;
        if (mb < 16) modA[(ks * 4 + g) * 16 + 8 + 4 * a + e] = v * QSCALE;
        else                 modA[(ks * 4 + g) * 16 + 4 * a + e] = rbf(1.f + v) * QSCALE;
      }
    }
  } else if (blockIdx.x < 352) {
    const int row0 = (blockIdx.x - 96) * 8 + (tid >> 6) * 2;
    u32x4 xv[2];
#pragma unroll
    for (int r = 0; r < 2; ++r) xv[r] = *(const u32x4*)(x + (size_t)(row0 + r) * DM + lane * 8);
#pragma unroll
    for (int r = 0; r < 2; ++r) {
      float v[8];
#pragma unroll
      for (int j = 0; j < 4; ++j) { v[2 * j] = bflo(xv[r][j]); v[2 * j + 1] = bfhi(xv[r][j]); }
      const float mean = wave_sum(((v[0] + v[1]) + (v[2] + v[3])) + ((v[4] + v[5]) + (v[6] + v[7]))) * (1.f / DM);
      float q = 0;
#pragma unroll
      for (int j = 0; j < 8; ++j) { const float d = v[j] - mean; q += d * d; }
      const float var = rbf(wave_sum(q) * (1.f / DM)), m = rbf(mean);
      const float rsd = __builtin_amdgcn_rcpf(rbf(__builtin_amdgcn_sqrtf(rbf(var + 1e-5f))));
      unsigned w[4];
#pragma unroll
      for (int j = 0; j < 4; ++j) {
        const f32x2 nn = {rbf(rbf(v[2 * j] - m) * rsd), rbf(rbf(v[2 * j + 1] - m) * rsd)};
        w[j] = __builtin_bit_cast(unsigned, __builtin_convertvector(nn, f16x2));
      }
      const bool a1 = lane & 2;
      const unsigned s0 = a1 ? w[0] : w[2], s1 = a1 ? w[1] : w[3];
      const unsigned r0 = (unsigned)__builtin_amdgcn_update_dpp(0, (int)s0, 0x4E, 0xf, 0xf, false), r1 = (unsigned)__builtin_amdgcn_update_dpp(0, (int)s1, 0x4E, 0xf, 0xf, false);
      const u32x4 o = a1 ? u32x4{r0, r1, w[2], w[3]} : u32x4{w[0], w[1], r0, r1};
      const int row = row0 + r, rb = row >> 4, qq = row & 15, ks = lane >> 2, g = 2 * (lane & 1) + ((lane >> 1) & 1);
      *(u32x4*)(nfrag + ((size_t)(rb * 16 + ks) * 64 + g * 16 + qq) * 8) = o;
    }
  } else {
    const unsigned pb = blockIdx.x - 352, xl = pb & 7u, j = pb >> 3;
    if (tid < 40) *(u32x4*)(keys + (pb * 40u + tid) * 4u) = u32x4{0, 0, 0, 0};
    { const __amdgpu_buffer_rsrc_t rY = __builtin_amdgcn_make_buffer_rsrc((void*)yp, (short)0, NEB * 8 * DM * 4 + 73728, 0x00020000);
#pragma unroll
      for (int i = 0; i < 4; ++i) if (i * 256 + tid < 932) __builtin_amdgcn_raw_buffer_store_b128(u32x4{~0u, ~0u, ~0u, ~0u}, rY, (pb * 932u + i * 256 + tid) * 16u, 0, 16); }
    u32x4 sink = {0, 0, 0, 0};
    { const char* ca = code_addr_attn() + j * 4096u + tid * 16u;
      if (ca + 16 <= code_addr_fin()) sink ^= *(const u32x4*)ca; }
    asm volatile("" :: "v"(sink));
  }
  KS_END
}

DI void attn_slow(const u16* __restrict__ hfrag, const u16* __restrict__ wq, const u16* __restrict__ kc, const u16* __restrict__ vc,
                  u16* __restrict__ ofrag, unsigned head, unsigned rb0) {
  unsigned lane; asm volatile("v_mbcnt_lo_u32_b32 %0, -1, 0\n\tv_mbcnt_hi_u32_b32 %0, -1, %0" : "=v"(lane));
  const unsigned i = lane & 15, g = lane >> 4;
  const u16* K = kc + (size_t)head * SEQ * DH; const u16* V = vc + (size_t)head * SEQ * DH;
#pragma unroll 1
  for (int qq = 0; qq < 4; ++qq) {
    const unsigned rb = rb0 + qq;
    f32x4 acc[8];
#pragma unroll
    for (int db = 0; db < 8; ++db) acc[db] = f32x4{};
#pragma unroll 1
    for (int ks = 0; ks < 16; ++ks) {
      const f16x8 nf = *(const f16x8*)(hfrag + ((size_t)(rb * 16 + ks) * 64 + lane) * 8);
      const float* ma = (const float*)(hfrag + SEQ * DM) + (ks * 4 + g) * 16;
      u32x4 hw;
#pragma unroll
      for (int e = 0; e < 4; ++e) hw[e] = cvtpk(fmaf((float)nf[2 * e], ma[2 * e], ma[8 + 2 * e]), fmaf((float)nf[2 * e + 1], ma[2 * e + 1], ma[8 + 2 * e + 1]));
      const bf16x8 hf = __builtin_bit_cast(bf16x8, hw);
#pragma unroll
      for (int db = 0; db < 8; ++db) {
        u16x8 w;
#pragma unroll
        for (int j = 0; j < 8; ++j) w[j] = wq[(size_t)(32 * ks + 16 * (j >> 2) + 4 * g + (j & 3)) * (NH * DH) + head * DH + 32 * (db >> 1) + 8 * (i >> 2) + 4 * (db & 1) + (i & 3)];
        acc[db] = __builtin_amdgcn_mfma_f32_16x16x32_bf16(__builtin_bit_cast(bf16x8, w), hf, acc[db], 0, 0, 0);
      }
    }
    bf16x8 qf[4];
#pragma unroll
    for (int dd = 0; dd < 4; ++dd) {
      u32x4 w;
#pragma unroll
      for (int e = 0; e < 4; ++e) w[e] = cvtpk(acc[2 * dd + (e >> 1)][2 * (e & 1)], acc[2 * dd + (e >> 1)][2 * (e & 1) + 1]);
      qf[dd] = __builtin_bit_cast(bf16x8, w);
    }
    float m = -__builtin_inff(), l = 0.f;
#pragma unroll
    for (int db = 0; db < 8; ++db) acc[db] = f32x4{};
#pragma unroll 1
    for (int k0 = 0; k0 < SEQ; k0 += 32) {
      f32x4 S[2] = {f32x4{}, f32x4{}};
#pragma unroll
      for (int kb = 0; kb < 2; ++kb)
#pragma unroll
        for (int dd = 0; dd < 4; ++dd) {
          const bf16x8 kf = *(const bf16x8*)(K + (size_t)(k0 + 16 * kb + i) * DH + 32 * dd + 8 * g);
          S[kb] = __builtin_amdgcn_mfma_f32_16x16x32_bf16(kf, qf[dd], S[kb], 0, 0, 0);
        }
      float mx = fmaxf(fmaxf(fmaxf(S[0][0], S[0][1]), fmaxf(S[0][2], S[0][3])), fmaxf(fmaxf(S[1][0], S[1][1]), fmaxf(S[1][2], S[1][3])));
      mx = fmaxf(mx, __shfl_xor(mx, 16)); mx = fmaxf(mx, __shfl_xor(mx, 32));
      const float mn = fmaxf(m, mx), alpha = __builtin_amdgcn_exp2f(m - mn);
      m = mn; l *= alpha;
#pragma unroll
      for (int db = 0; db < 8; ++db) acc[db] *= alpha;
#pragma unroll
      for (int r = 0; r < 8; ++r) { S[r >> 2][r & 3] = __builtin_amdgcn_exp2f(S[r >> 2][r & 3] - mn); l += S[r >> 2][r & 3]; }
      u32x4 pw;
#pragma unroll
      for (int e = 0; e < 4; ++e) pw[e] = cvtpk(S[e >> 1][2 * (e & 1)], S[e >> 1][2 * (e & 1) + 1]);
#pragma unroll
      for (int db = 0; db < 8; ++db) {
        u16x8 w;
#pragma unroll
        for (int j = 0; j < 8; ++j) w[j] = V[(size_t)(k0 + 16 * (j >> 2) + 4 * g + (j & 3)) * DH + 16 * db + i];
        acc[db] = __builtin_amdgcn_mfma_f32_16x16x32_bf16(__builtin_bit_cast(bf16x8, w), __builtin_bit_cast(bf16x8, pw), acc[db], 0, 0, 0);
      }
    }
    l += __shfl_xor(l, 16); l += __shfl_xor(l, 32);
    const float il = 1.f / l;
#pragma unroll
    for (int dp = 0; dp < 4; ++dp) {
      u32x4 w;
#pragma unroll
      for (int e = 0; e < 4; ++e) w[e] = cvtpk(acc[2 * dp + (e >> 1)][2 * (e & 1)] * il, acc[2 * dp + (e >> 1)][2 * (e & 1) + 1] * il);
      __builtin_amdgcn_raw_buffer_store_b128(w, __builtin_amdgcn_make_buffer_rsrc((void*)ofrag, (short)0, SEQ * NH * DH * 2, 0x00020000), ((rb * 128 + head * 4 + dp) * 64 + lane) * 16, 0, 16);
    }
  }
}

typedef __bf16 bf16x4 __attribute__((ext_vector_type(4)));
typedef short  s16x4  __attribute__((ext_vector_type(4)));
#define RSRC(p, n) __builtin_amdgcn_make_buffer_rsrc((void*)(p), (short)0, (n), 0x00020000)
DI u32x4 ld128_sc1(__amdgpu_buffer_rsrc_t r, unsigned off) { return __builtin_amdgcn_raw_buffer_load_b128(r, off, 0, 16); }
DI void  st128_sc1(__amdgpu_buffer_rsrc_t r, unsigned off, u32x4 v) { __builtin_amdgcn_raw_buffer_store_b128(v, r, off, 0, 16); }
DI unsigned rkey(float r, int idx) {
  unsigned u = __float_as_uint(r);
  u = (u & 0x80000000u) ? ~u : (u | 0x80000000u);
  return (u & 0xfffff000u) | (unsigned)(0xfff - idx);
}
DI float rkey_val(unsigned k) { const unsigned u = k & 0xfffff000u; return rbf(__uint_as_float((u & 0x80000000u) ? ((u & 0x7fffffffu) | 0x800u) : (~u & 0xfffff800u))); }
template <int CTRL> DI unsigned dppu(unsigned v) { return (unsigned)__builtin_amdgcn_update_dpp(0, (int)v, CTRL, 0xf, 0xf, false); }
DI unsigned wave_allmax(unsigned m) {
  m = max(m, dppu<0x121>(m)); m = max(m, dppu<0x122>(m)); m = max(m, dppu<0x124>(m)); m = max(m, dppu<0x128>(m));
  { auto r = __builtin_amdgcn_permlane16_swap(m, m, false, false); m = max(r[0], r[1]); }
  { auto r = __builtin_amdgcn_permlane32_swap(m, m, false, false); m = max(r[0], r[1]); }
  return m;
}
constexpr unsigned SPIN_CAP = 1u << 20;
#define F_DELAY 50
#define E_DELAY 24
#define SWEEP_DELAY 50
#define SWEEP_SLEEP 4
DI void sweep_top8(const unsigned* keys, unsigned* cand, unsigned t256) {
  const __amdgpu_buffer_rsrc_t rK = RSRC(keys, SEQ * 4);
  unsigned k[8];
#pragma unroll 1
  for (unsigned spins = 0; spins < SPIN_CAP; ++spins) {
    const u32x4 a = ld128_sc1(rK, t256 * 32u), b = ld128_sc1(rK, t256 * 32u + 16u);
#pragma unroll
    for (int j = 0; j < 4; ++j) { k[j] = a[j]; k[4 + j] = b[j]; }
    const unsigned mn = min(min(min(k[0], k[1]), min(k[2], k[3])), min(min(k[4], k[5]), min(k[6], k[7])));
    if (__all(mn != 0u)) break;
    __builtin_amdgcn_s_sleep(SWEEP_SLEEP);
    asm volatile("" ::: "memory");
  }
  asm volatile("" ::: "memory");
#define CE(i, j) { const unsigned hi = max(k[i], k[j]), lo = min(k[i], k[j]); k[i] = hi; k[j] = lo; }
  CE(0, 1) CE(2, 3) CE(4, 5) CE(6, 7) CE(0, 2) CE(1, 3) CE(4, 6) CE(5, 7) CE(1, 2) CE(5, 6) CE(0, 4) CE(3, 7) CE(1, 5) CE(2, 6) CE(1, 4) CE(3, 6) CE(2, 4) CE(3, 5) CE(3, 4)
#undef CE
  unsigned mine = 0;
#pragma unroll
  for (int it = 0; it < 8; ++it) {
    const unsigned m = wave_allmax(k[0]);
    const bool win = k[0] == m;
#pragma unroll
    for (int j = 0; j < 7 - it; ++j) k[j] = win ? k[j + 1] : k[j];
    mine = ((t256 & 63u) == (unsigned)it) ? m : mine;
  }
  if ((t256 & 63u) < 8u) cand[(t256 >> 6) * 8u + (t256 & 63u)] = mine;
}
DI unsigned rank_top8(const unsigned* cand, unsigned* selw, unsigned lane) {
  const unsigned me = cand[lane & 31u]; int rank = 0;
#pragma unroll
  for (int j = 0; j < 8; ++j) { const u32x4 c4 = *(const u32x4*)(cand + 4 * j); rank += (c4[0] > me) + (c4[1] > me) + (c4[2] > me) + (c4[3] > me); }
  if (rank < 8) selw[rank] = me;
  __builtin_amdgcn_fence(__ATOMIC_RELEASE, "wavefront"); __builtin_amdgcn_wave_barrier(); __builtin_amdgcn_fence(__ATOMIC_ACQUIRE, "wavefront");
  return selw[lane & 7u];
}

DI void tail(char* lds, unsigned blk, unsigned wid, const u16* part, const u16* __restrict__ x, const float* __restrict__ mod, const u16* __restrict__ wr,
             u16* xsel, unsigned* keys, const unsigned* cnt2, float* yp, u16* out KS_TARG) {
  unsigned lane; asm volatile("v_mbcnt_lo_u32_b32 %0, -1, 0\n\tv_mbcnt_hi_u32_b32 %0, -1, %0" : "=v"(lane));
  const unsigned tid = wid * 64u + lane, g = lane >> 4, ldsb = (unsigned)(size_t)lds;
  float* redm = (float*)(lds + 155648); float* redq = redm + 32; float* redr = redm + 64;
  unsigned* cand = (unsigned*)(lds + 155648 + 512); unsigned* selw = cand + 32;
  const __amdgpu_buffer_rsrc_t rS = RSRC(xsel, (1 + XREP) * 8 * DM * 2), rO = RSRC(out, SEQ * DM * 2), rP = RSRC(part, 4 * SEQ * DM * 2), rY = RSRC(yp, NEB * 8 * DM * 4);
  const bool roleE = blk < (unsigned)NEB;
  const unsigned slice = (blk & 7u) * 14u + (blk >> 3);
  u32x4 hpk[2], xpk[2];
  {
    const unsigned q8 = tid & 7u, c0 = tid >> 3, row = blk * 8u + q8;
    u32x2 xv[2][2], wv[2][2]; f32x4 ga[2][2], sb[2][2], cb[2][2], gb[2][2];
#pragma unroll
    for (int i = 0; i < 2; ++i)
#pragma unroll
      for (int a = 0; a < 2; ++a) {
        const unsigned c = c0 + 32u * i, n = 32u * (c >> 2) + 4u * (c & 3u) + 16u * a;
        xv[i][a] = *(const u32x2*)(x + (size_t)row * DM + n); wv[i][a] = *(const u32x2*)(wr + n);
        ga[i][a] = *(const f32x4*)(mod + 1024 + n); sb[i][a] = *(const f32x4*)(mod + 1536 + n); cb[i][a] = *(const f32x4*)(mod + 2048 + n); gb[i][a] = *(const f32x4*)(mod + 2560 + n);
      }
    if (wid == 0u) {
#pragma unroll 1
      for (unsigned spins = 0; spins < SPIN_CAP; ++spins) {
        if (rfl(__hip_atomic_load((GAS const unsigned*)cnt2 + (blk >> 2) * 32u, __ATOMIC_RELAXED, __HIP_MEMORY_SCOPE_AGENT)) >= 16u) break;
        __builtin_amdgcn_s_sleep(1);
      }
    }
    __syncthreads();
    KS_MID(0)
    u32x4 pv[2][4];
#pragma unroll
    for (int i = 0; i < 2; ++i)
#pragma unroll
      for (int sp = 0; sp < 4; ++sp) { const unsigned c = c0 + 32u * i; pv[i][sp] = ld128_sc1(rP, (((sp * 128u + (row >> 4)) * 16u + (c >> 2)) * 64u + (c & 3u) * 16u + (row & 15u)) * 16u); }
    float x1[2][8];
#pragma unroll
    for (int i = 0; i < 2; ++i) {
#pragma unroll
      for (int w = 0; w < 4; ++w) {
        const float lo = (bflo(pv[i][0][w]) + bflo(pv[i][1][w])) + (bflo(pv[i][2][w]) + bflo(pv[i][3][w]));
        const float hi = (bfhi(pv[i][0][w]) + bfhi(pv[i][1][w])) + (bfhi(pv[i][2][w]) + bfhi(pv[i][3][w]));
        x1[i][2 * w]     = rbf(bflo(xv[i][w >> 1][w & 1]) + rbf(ga[i][w >> 1][2 * (w & 1)] * rbf(lo)));
        x1[i][2 * w + 1] = rbf(bfhi(xv[i][w >> 1][w & 1]) + rbf(ga[i][w >> 1][2 * (w & 1) + 1] * rbf(hi)));
      }
      xpk[i] = u32x4{cvtpk(x1[i][0], x1[i][1]), cvtpk(x1[i][2], x1[i][3]), cvtpk(x1[i][4], x1[i][5]), cvtpk(x1[i][6], x1[i][7])};
    }
    float m = 0, q = 0;
#pragma unroll
    for (int i = 0; i < 2; ++i) m += ((x1[i][0] + x1[i][1]) + (x1[i][2] + x1[i][3])) + ((x1[i][4] + x1[i][5]) + (x1[i][6] + x1[i][7]));
    m *= (1.f / 16);
#pragma unroll
    for (int e = 0; e < 16; ++e) { const float d = x1[e >> 3][e & 7] - m; q += d * d; }
    { float ma, mb, qa, qb;
      chan(m, q, xor8(m), xor8(q), 8.f, m, q);
      pair16(m, ma, mb); pair16(q, qa, qb); chan(ma, qa, mb, qb, 16.f, m, q);
      pair32(m, ma, mb); pair32(q, qa, qb); chan(ma, qa, mb, qb, 32.f, m, q); }
    if (lane < 8u) { redm[q8 * 4u + wid] = m; redq[q8 * 4u + wid] = q; }
    __syncthreads();
    { const f32x4 m0 = *(const f32x4*)&redm[q8 * 4u]; f32x4 qs = *(const f32x4*)&redq[q8 * 4u];
      m = ((m0[0] + m0[1]) + (m0[2] + m0[3])) * 0.25f;
      const f32x4 d0 = m0 - m;
      qs += 128.f * (d0 * d0);
      q = (qs[0] + qs[1]) + (qs[2] + qs[3]); }
    const float var = rbf(q * (1.f / DM)), mr = rbf(m);
    const float rsd = __builtin_amdgcn_rcpf(rbf(__builtin_amdgcn_sqrtf(rbf(var + 1e-5f))));
    float h[2][8], rr = 0;
#pragma unroll
    for (int i = 0; i < 2; ++i) {
#pragma unroll
      for (int e = 0; e < 8; ++e) {
        const float nn = rbf(rbf(x1[i][e] - mr) * rsd);
        h[i][e] = rbf(rbf(nn * rbf(1.f + cb[i][e >> 2][e & 3])) + sb[i][e >> 2][e & 3]);
        rr += h[i][e] * ((e & 1) ? bfhi(wv[i][e >> 2][(e >> 1) & 1]) : bflo(wv[i][e >> 2][(e >> 1) & 1]));
      }
    }
    { float u, v; rr += xor8(rr); pair16(rr, u, v); rr = u + v; pair32(rr, u, v); rr = u + v; }
    if (lane < 8u) redr[q8 * 4u + wid] = rr;
    __syncthreads();
    if (tid < 64u) {
      const f32x4 s = *(const f32x4*)&redr[q8 * 4u];
      __hip_atomic_store((GAS unsigned*)keys + c0 * SEQ + row, rkey((s[0] + s[1]) + (s[2] + s[3]), (int)row), __ATOMIC_RELAXED, __HIP_MEMORY_SCOPE_AGENT);
    }
    KS_MID(1)
#pragma unroll
    for (int i = 0; i < 2; ++i) {
      const unsigned c = c0 + 32u * i, n0 = 32u * (c >> 2) + 4u * (c & 3u);
      hpk[i] = u32x4{cvtpk(h[i][0], h[i][1]), cvtpk(h[i][2], h[i][3]), cvtpk(h[i][4], h[i][5]), cvtpk(h[i][6], h[i][7])};
      float o[8];
#pragma unroll
      for (int e = 0; e < 8; ++e) o[e] = x1[i][e] + rbf(gb[i][e >> 2][e & 3] * (h[i][e] + h[i][e] * 0.f));
      const unsigned a0 = cvtpk(o[0], o[1]), a1 = cvtpk(o[2], o[3]), b0 = cvtpk(o[4], o[5]), b1 = cvtpk(o[6], o[7]);
      const bool odd = c & 1u;
      const unsigned r0 = dppu<0x128>(odd ? a0 : b0), r1 = dppu<0x128>(odd ? a1 : b1);
      st128_sc1(rO, (row * DM + (odd ? n0 + 12u : n0)) * 2u, odd ? u32x4{r0, r1, b0, b1} : u32x4{a0, a1, r0, r1});
    }
  }
  s16x4 wf[2][4][2], df[8];
  if (roleE) {
    const unsigned a = ldsb + 98304u + 4096u * wid + (g >> 1) * 256u + ((g & 1u) * 4u + ((lane >> 2) & 3u)) * 32u + (lane & 3u) * 8u;
#define TRR(dst, off) "ds_read_b64_tr_b16 %" #dst ", %16 offset:" #off "\n\t"
    asm volatile(TRR(0, 0) TRR(1, 512) TRR(2, 1024) TRR(3, 1536) TRR(4, 2048) TRR(5, 2560) TRR(6, 3072) TRR(7, 3584)
                 TRR(8, 16384) TRR(9, 16896) TRR(10, 17408) TRR(11, 17920) TRR(12, 18432) TRR(13, 18944) TRR(14, 19456) TRR(15, 19968) "s_waitcnt lgkmcnt(0)"
                 : "=&v"(wf[0][0][0]), "=&v"(wf[0][0][1]), "=&v"(wf[0][1][0]), "=&v"(wf[0][1][1]), "=&v"(wf[0][2][0]), "=&v"(wf[0][2][1]), "=&v"(wf[0][3][0]), "=&v"(wf[0][3][1]),
                   "=&v"(wf[1][0][0]), "=&v"(wf[1][0][1]), "=&v"(wf[1][1][0]), "=&v"(wf[1][1][1]), "=&v"(wf[1][2][0]), "=&v"(wf[1][2][1]), "=&v"(wf[1][3][0]), "=&v"(wf[1][3][1])
                 : "v"(a) : "memory");
#undef TRR
#define TRR(dst, off) "ds_read_b64_tr_b16 %" #dst ", %8 offset:" #off "\n\t"
    asm volatile(TRR(0, 32768) TRR(1, 33280) TRR(2, 33792) TRR(3, 34304) TRR(4, 34816) TRR(5, 35328) TRR(6, 35840) TRR(7, 36352) "s_waitcnt lgkmcnt(0)"
                 : "=&v"(df[0]), "=&v"(df[1]), "=&v"(df[2]), "=&v"(df[3]), "=&v"(df[4]), "=&v"(df[5]), "=&v"(df[6]), "=&v"(df[7]) : "v"(a) : "memory");
#undef TRR
  }
  sweep_top8(keys + (blk & 7u) * SEQ, cand, tid);
  __syncthreads();
  KS_MID(2)
  const unsigned key = rank_top8(cand, selw + wid * 8u, lane);
  {
    const unsigned long long own = __ballot(((0xfffu - (key & 0xfffu)) >> 3) == blk) & 0xffull;
    if (own != 0ull) {
      asm volatile("s_waitcnt vmcnt(0)" ::: "memory");
#pragma unroll
      for (int e = 0; e < 8; ++e) { hpk[e >> 2][e & 3] = min(hpk[e >> 2][e & 3], 0xfffffffeu); xpk[e >> 2][e & 3] = min(xpk[e >> 2][e & 3], 0xfffffffeu); }
      for (unsigned tt = 0; tt < 8u; ++tt) {
        if (!((own >> tt) & 1ull)) continue;
        const unsigned rw = 0xfffu - ((unsigned)__builtin_amdgcn_readlane((int)key, (int)tt) & 0xfffu);
        if ((rw & 7u) == (tid & 7u)) {
#pragma unroll
          for (int i = 0; i < 2; ++i) {
            const unsigned o = (tt * DM + 8u * ((tid >> 3) + 32u * i)) * 2u;
#pragma unroll
            for (int rp = 0; rp < XREP; ++rp) st128_sc1(rS, o + (1u + rp) * (8u * DM * 2u), hpk[i]);
            st128_sc1(rS, o, xpk[i]);
          }
        }
      }
    }
  }
  KS_MID(3)
  if (roleE) {
    f32x4 ag = f32x4{}, au = f32x4{};
    u32x4 xf[4];
    __builtin_amdgcn_s_sleep(E_DELAY);
#pragma unroll 1
    for (unsigned spins = 0; spins < SPIN_CAP; ++spins) {
      unsigned mx = 0;
#pragma unroll
      for (int i = 0; i < 4; ++i) xf[i] = ld128_sc1(rS, ((8u + (blk % XREP) * 8u + (lane & 7u)) * DM + (4u * wid + i) * 32u + g * 8u) * 2u);
      asm volatile("" : "+v"(xf[0]), "+v"(xf[1]), "+v"(xf[2]), "+v"(xf[3]) :: "memory");
#pragma unroll
      for (int i = 0; i < 4; ++i) mx = max(max(mx, max(xf[i][0], xf[i][1])), max(xf[i][2], xf[i][3]));
      if (__all(mx != 0xffffffffu)) break;
      __builtin_amdgcn_s_sleep(1);
    }
#pragma unroll
    for (int i = 0; i < 4; ++i) {
      const bf16x8 a0 = __builtin_bit_cast(bf16x8, __builtin_shufflevector(wf[0][i][0], wf[0][i][1], 0, 1, 2, 3, 4, 5, 6, 7));
      const bf16x8 a1 = __builtin_bit_cast(bf16x8, __builtin_shufflevector(wf[1][i][0], wf[1][i][1], 0, 1, 2, 3, 4, 5, 6, 7));
      ag = __builtin_amdgcn_mfma_f32_16x16x32_bf16(a0, __builtin_bit_cast(bf16x8, xf[i]), ag, 0, 0, 0);
      au = __builtin_amdgcn_mfma_f32_16x16x32_bf16(a1, __builtin_bit_cast(bf16x8, xf[i]), au, 0, 0, 0);
    }
    f32x4* red = (f32x4*)(lds + 147456);
    red[(wid * 2 + 0) * 64 + lane] = ag; red[(wid * 2 + 1) * 64 + lane] = au;
    __syncthreads();
    KS_MID(4)
    ag = (red[0 * 64 + lane] + red[2 * 64 + lane]) + (red[4 * 64 + lane] + red[6 * 64 + lane]);
    au = (red[1 * 64 + lane] + red[3 * 64 + lane]) + (red[5 * 64 + lane] + red[7 * 64 + lane]);
    float act[4];
#pragma unroll
    for (int e = 0; e < 4; ++e) act[e] = rbf(silu_f(rbf(ag[e]))) * rbf(au[e]);
    const u32x2 aw = {cvtpk(act[0], act[1]), cvtpk(act[2], act[3])};
#pragma unroll
    for (int j = 0; j < 8; ++j) {
      const f32x4 y = __builtin_amdgcn_mfma_f32_16x16x16bf16_1k(df[j], __builtin_bit_cast(s16x4, aw), f32x4{}, 0, 0, 0);
      u32x4 yb = __builtin_bit_cast(u32x4, y);
#pragma unroll
      for (int e = 0; e < 4; ++e) yb[e] = min(yb[e], 0xfffffffeu);
      if ((lane & 15u) < 8u) st128_sc1(rY, ((slice * 8u + (lane & 7u)) * DM + 16u * (8u * wid + j) + 4u * g) * 4u, yb);
    }
    KS_MID(5)
  } else if (blk < (unsigned)NEB + 128u) {
    const unsigned fb = blk - NEB, t = fb >> 4, nq = tid & 7u, pg = tid >> 3, n = (fb & 15u) * 32u + nq * 4u;
    const unsigned tkey = (unsigned)__builtin_amdgcn_readlane((int)key, (int)t), row = 0xfffu - (tkey & 0xfffu);
    const f32x4 gb = *(const f32x4*)(mod + 2560 + n);
    const unsigned fo = (t * DM + (fb & 15u) * 32u + (nq & 3u) * 8u + (nq >> 2) * 4u) * 2u;
    const float rs = rbf(__builtin_amdgcn_rcpf(1.f + __expf(-rkey_val(tkey))));
    u32x2 hv, xv; u32x4 w[4];
    const unsigned yo = ((pg * 4u * 8u + t) * DM + n) * 4u;
    __builtin_amdgcn_s_sleep(F_DELAY);
#pragma unroll 1
    for (unsigned spins = 0; spins < SPIN_CAP; ++spins) {
#pragma unroll
      for (int i = 0; i < 4; ++i) w[i] = ld128_sc1(rY, yo + i * (8u * DM * 4u));
      xv = __builtin_amdgcn_raw_buffer_load_b64(rS, fo, 0, 16); hv = __builtin_amdgcn_raw_buffer_load_b64(rS, fo + 8u * DM * 2u, 0, 16);
      unsigned mx = max(max(hv[0], hv[1]), max(xv[0], xv[1]));
#pragma unroll
      for (int i = 0; i < 4; ++i) mx = max(max(mx, max(w[i][0], w[i][1])), max(w[i][2], w[i][3]));
      if (__all(mx != 0xffffffffu)) break;
      __builtin_amdgcn_s_sleep(1);
      asm volatile("" ::: "memory");
    }
    f32x4 y = (__builtin_bit_cast(f32x4, w[0]) + __builtin_bit_cast(f32x4, w[1])) + (__builtin_bit_cast(f32x4, w[2]) + __builtin_bit_cast(f32x4, w[3]));
#pragma unroll
    for (int e = 0; e < 4; ++e) { float a, b; y[e] += xor8(y[e]); pair16(y[e], a, b); y[e] = a + b; pair32(y[e], a, b); y[e] = a + b; }
    f32x4* red = (f32x4*)(lds + 147456);
    if (lane < 8u) red[wid * 8u + nq] = y;
    __syncthreads();
    if (tid < 8u) {
      y = (red[nq] + red[8 + nq]) + (red[16 + nq] + red[24 + nq]);
      float o[4];
#pragma unroll
      for (int e = 0; e < 4; ++e) {
        const float h = (e & 1) ? bfhi(hv[e >> 1]) : bflo(hv[e >> 1]);
        const float upd = rbf(h * 0.f + rbf(rbf(y[e]) * rs));
        o[e] = ((e & 1) ? bfhi(xv[e >> 1]) : bflo(xv[e >> 1])) + rbf(gb[e] * rbf(h + upd));
      }
      __builtin_amdgcn_raw_buffer_store_b64(u32x2{cvtpk(o[0], o[1]), cvtpk(o[2], o[3])}, rO, (row * DM + n) * 2u, 0, 16);
    }
  }
}

#define CLOB8(p, b) p #b "0", p #b "1", p #b "2", p #b "3", p #b "4", p #b "5", p #b "6", p #b "7", p #b "8", p #b "9"
#define CLOB_0_99(p) p "0", p "1", p "2", p "3", p "4", p "5", p "6", p "7", p "8", p "9", CLOB8(p, 1), CLOB8(p, 2), CLOB8(p, 3), CLOB8(p, 4), CLOB8(p, 5), CLOB8(p, 6), CLOB8(p, 7), CLOB8(p, 8), CLOB8(p, 9)
#define CLOB_100_239(p) CLOB8(p, 10), CLOB8(p, 11), CLOB8(p, 12), CLOB8(p, 13), CLOB8(p, 14), CLOB8(p, 15), CLOB8(p, 16), CLOB8(p, 17), CLOB8(p, 18), CLOB8(p, 19), CLOB8(p, 20), CLOB8(p, 21), CLOB8(p, 22), CLOB8(p, 23)
#define CLOB_240_255(p) CLOB8(p, 24), p "250", p "251", p "252", p "253", p "254", p "255"

extern "C" __global__ __launch_bounds__(256, 1) void k_attn(const u16* __restrict__ hfrag, const u16* __restrict__ wq, const u16* __restrict__ kc,
                                                 const u16* __restrict__ vc, u16* __restrict__ ofrag, const u16* __restrict__ wo,
                                                 const u16* __restrict__ wg, const u16* __restrict__ wu, const u16* __restrict__ wd, u16* out,
                                                 unsigned* cnt, u16* part, float* yp, const u16* __restrict__ x, const float* __restrict__ mod,
                                                 const u16* __restrict__ wr, u16* xsel KS_ARG) {
  KS_BEGIN
  __shared__ __attribute__((aligned(1024))) char lds[163840];
  const unsigned lane = threadIdx.x & 63u, wid = rfl(threadIdx.x >> 6);
    const unsigned b = blockIdx.x, xcd = b & 7u, r = b >> 3, head = (xcd >> 1) * 8u + (r >> 2), qblk = (xcd & 1u) * 4u + (r & 3u);
  const unsigned rb0 = qblk * 16u + wid * 4u;
  const unsigned ldsb = (unsigned)(size_t)lds;
  const unsigned l16 = lane & 15u, g = lane >> 4, q4 = (lane >> 2) & 3u, p4 = lane & 3u;
  const unsigned vA0 = ldsb + 256u * l16 + 16u * (g ^ l16);
  const unsigned vB0 = 256u * g + 16u * (l16 ^ g);
  const unsigned trb = (g >> 1) * 2048u + ((g & 1u) * 4u + q4) * 64u;
  const unsigned vVb = ldsb + trb + 32u * (g & 1u) + p4 * 8u;
  const unsigned vWb = ldsb + trb + ((16u * p4) ^ (32u * (g & 1u)));
  const unsigned dcol = 64u * (lane >> 5) + 32u * (((lane >> 1) ^ (lane >> 4)) & 1u) + 16u * (lane & 1u);
  const unsigned vVd = 256u * ((lane >> 2) & 7u) + dcol;
  const unsigned vWd = 8192u * ((lane >> 2) & 7u) + dcol;
  const unsigned vCd = 1024u * ((lane >> 2) & 7u) + dcol;
  const unsigned vL16 = lane * 16u;
  const __amdgpu_buffer_rsrc_t srdH = __builtin_amdgcn_make_buffer_rsrc((void*)hfrag, (short)0, (2 << 20) + 4096, 0x00020000);
  const __amdgpu_buffer_rsrc_t srdW = __builtin_amdgcn_make_buffer_rsrc((void*)wq, (short)0, DM * NH * DH * 2, 0x00020000);
  const __amdgpu_buffer_rsrc_t srdK = __builtin_amdgcn_make_buffer_rsrc((void*)((const char*)kc + (size_t)head * 524288), (short)0, 524288, 0x00020000);
  const __amdgpu_buffer_rsrc_t srdV = __builtin_amdgcn_make_buffer_rsrc((void*)((const char*)vc + (size_t)head * 524288 - 1024), (short)0, 524288 + 1024, 0x00020000);
  const __amdgpu_buffer_rsrc_t srdO = __builtin_amdgcn_make_buffer_rsrc((void*)ofrag, (short)0, SEQ * NH * DH * 2, 0x00020000);
  const unsigned sHoff = rb0 * 16384u, sWoff = head * 256u + wid * 131072u, sKV = wid * 4096u, sM0 = ldsb + wid * 4096u;
  const unsigned sOoff = rb0 * 131072u + head * 4096u;
  const unsigned gw = b * 4u + wid, lw = r * 4u + wid;
  const char* pP0 = (gw < 448u ? (const char*)wg + gw * 4096u : (gw < 896u ? (const char*)wu + (gw - 448u) * 4096u : (const char*)wg));
  const char* pP1 = gw < 448u ? (const char*)wd + gw * 4096u : (const char*)out + (size_t)(lw & 1u) * 1048576;
  const char* pP2 = (const char*)wo + (size_t)(xcd >> 1) * 1048576 + (size_t)lw * 8192;
  const char* pP3 = pP2 + 4096;
  const char* pP4 = pP3;
  if (lw < 7u) pP4 = code_addr_fin() - 4096 * (int)(lw + 1u);
  unsigned long long bad;
  asm volatile(
  "s_nop 7\n"
  "s_lshr_b32 s60, %[sKV], 2\n"
  "s_sub_u32 s61, %[sM0], %[sKV]\n"
  "s_add_u32 s61, s61, s60\n"
  "s_add_u32 m0, s61, 147456\n"
  "s_add_u32 s60, s60, 2097152\n"
  "buffer_load_dwordx4 %[vL16], %[srdH], s60 offen lds\n"
  "s_add_u32 m0, %[sM0], 16384\n"
  "s_add_u32 s60, %[sWoff], 0\n"
  "buffer_load_dwordx4 %[vWd], %[srdW], s60 offen lds\n"
  "s_add_u32 m0, %[sM0], 17408\n"
  "s_add_u32 s61, %[sWoff], 128\n"
  "buffer_load_dwordx4 %[vWd], %[srdW], s61 offen lds\n"
  "s_add_u32 m0, %[sM0], 18432\n"
  "s_add_u32 s60, %[sWoff], 65536\n"
  "buffer_load_dwordx4 %[vWd], %[srdW], s60 offen lds\n"
  "s_add_u32 m0, %[sM0], 19456\n"
  "s_add_u32 s61, %[sWoff], 65664\n"
  "buffer_load_dwordx4 %[vWd], %[srdW], s61 offen lds\n"
  "s_add_u32 s66, %[sHoff], 0\n"
  "buffer_load_dwordx4 v[0:3], %[vL16], %[srdH], s66 offen\n"
  "s_add_u32 s67, %[sHoff], 16384\n"
  "buffer_load_dwordx4 v[4:7], %[vL16], %[srdH], s67 offen\n"
  "s_add_u32 s66, %[sHoff], 32768\n"
  "buffer_load_dwordx4 v[8:11], %[vL16], %[srdH], s66 offen\n"
  "s_add_u32 s67, %[sHoff], 49152\n"
  "buffer_load_dwordx4 v[12:15], %[vL16], %[srdH], s67 offen\n"
  "s_add_u32 s66, %[sHoff], 1024\n"
  "buffer_load_dwordx4 v[16:19], %[vL16], %[srdH], s66 offen\n"
  "s_add_u32 s67, %[sHoff], 17408\n"
  "buffer_load_dwordx4 v[20:23], %[vL16], %[srdH], s67 offen\n"
  "s_add_u32 s66, %[sHoff], 33792\n"
  "buffer_load_dwordx4 v[24:27], %[vL16], %[srdH], s66 offen\n"
  "s_add_u32 s67, %[sHoff], 50176\n"
  "buffer_load_dwordx4 v[28:31], %[vL16], %[srdH], s67 offen\n"
  "s_add_u32 m0, %[sM0], 81920\n"
  "s_add_u32 s60, %[sWoff], 524288\n"
  "buffer_load_dwordx4 %[vWd], %[srdW], s60 offen lds\n"
  "s_add_u32 m0, %[sM0], 82944\n"
  "s_add_u32 s61, %[sWoff], 524416\n"
  "buffer_load_dwordx4 %[vWd], %[srdW], s61 offen lds\n"
  "s_add_u32 m0, %[sM0], 83968\n"
  "s_add_u32 s60, %[sWoff], 589824\n"
  "buffer_load_dwordx4 %[vWd], %[srdW], s60 offen lds\n"
  "s_add_u32 m0, %[sM0], 84992\n"
  "s_add_u32 s61, %[sWoff], 589952\n"
  "buffer_load_dwordx4 %[vWd], %[srdW], s61 offen lds\n"
  "v_xor_b32 v192, 0, %[vA0]\n"
  "v_xor_b32 v193, 64, %[vA0]\n"
  "v_xor_b32 v194, 128, %[vA0]\n"
  "v_xor_b32 v195, 192, %[vA0]\n"
  "v_add_u32 v196, 65536, %[vVb]\n"
  "v_xor_b32 v197, 32, v196\n"
  "v_xor_b32 v198, 0, %[vB0]\n"
  "v_xor_b32 v199, 64, %[vB0]\n"
  "v_xor_b32 v200, 128, %[vB0]\n"
  "v_xor_b32 v201, 192, %[vB0]\n"
  "v_add_u32 v202, 1024, %[vVd]\n"
  "v_add_u32 v203, 128, %[vVd]\n"
  "v_add_u32 v204, 0, %[vWb]\n"
  "v_add_u32 v205, 65536, %[vWb]\n"
  "v_add_u32 v206, 131072, %[vWb]\n"
  "s_sub_u32 s64, %[sM0], %[sKV]\n"
  "s_add_u32 s64, s64, 147456\n"
  "v_lshrrev_b32 v176, 8, %[vL16]\n"
  "v_lshl_add_u32 v176, v176, 6, s64\n"
  "s_mov_b32 s63, 0\n"
  "v_mov_b32 v212, 0\n"
  "v_mov_b32 v213, 0\n"
  "s_waitcnt vmcnt(12)\n"
  "s_barrier\n"
  "ds_read_b128 v[128:131], v176 offset:0\n"
  "ds_read_b128 v[132:135], v176 offset:16\n"
  "ds_read_b128 v[136:139], v176 offset:32\n"
  "ds_read_b128 v[140:143], v176 offset:48\n"
  "ds_read_b128 v[144:147], v176 offset:256\n"
  "ds_read_b128 v[148:151], v176 offset:272\n"
  "ds_read_b128 v[152:155], v176 offset:288\n"
  "ds_read_b128 v[156:159], v176 offset:304\n"
  "ds_read_b64_tr_b16 a[128:129], v204 offset:16384\n"
  "ds_read_b64_tr_b16 a[130:131], v204 offset:20480\n"
  "ds_read_b64_tr_b16 a[132:133], v204 offset:16392\n"
  "ds_read_b64_tr_b16 a[134:135], v204 offset:20488\n"
  "ds_read_b64_tr_b16 a[136:137], v204 offset:16896\n"
  "ds_read_b64_tr_b16 a[138:139], v204 offset:20992\n"
  "ds_read_b64_tr_b16 a[140:141], v204 offset:16904\n"
  "ds_read_b64_tr_b16 a[142:143], v204 offset:21000\n"
  "ds_read_b64_tr_b16 a[144:145], v204 offset:17408\n"
  "ds_read_b64_tr_b16 a[146:147], v204 offset:21504\n"
  "ds_read_b64_tr_b16 a[148:149], v204 offset:17416\n"
  "ds_read_b64_tr_b16 a[150:151], v204 offset:21512\n"
  "ds_read_b64_tr_b16 a[152:153], v204 offset:17920\n"
  "ds_read_b64_tr_b16 a[154:155], v204 offset:22016\n"
  "ds_read_b64_tr_b16 a[156:157], v204 offset:17928\n"
  "ds_read_b64_tr_b16 a[158:159], v204 offset:22024\n"
  "s_waitcnt vmcnt(8)\n"
  "s_waitcnt lgkmcnt(15)\n"
  "v_fma_mix_f32 v160, v0, v128, v136 op_sel:[0,0,0] op_sel_hi:[1,0,0]\n"
  "v_fma_mix_f32 v161, v0, v129, v137 op_sel:[1,0,0] op_sel_hi:[1,0,0]\n"
  "v_fma_mix_f32 v162, v1, v130, v138 op_sel:[0,0,0] op_sel_hi:[1,0,0]\n"
  "v_fma_mix_f32 v163, v1, v131, v139 op_sel:[1,0,0] op_sel_hi:[1,0,0]\n"
  "v_fma_mix_f32 v164, v2, v132, v140 op_sel:[0,0,0] op_sel_hi:[1,0,0]\n"
  "v_fma_mix_f32 v165, v2, v133, v141 op_sel:[1,0,0] op_sel_hi:[1,0,0]\n"
  "v_fma_mix_f32 v166, v3, v134, v142 op_sel:[0,0,0] op_sel_hi:[1,0,0]\n"
  "v_fma_mix_f32 v167, v3, v135, v143 op_sel:[1,0,0] op_sel_hi:[1,0,0]\n"
  "v_cvt_pk_bf16_f32 v0, v160, v161\n"
  "v_cvt_pk_bf16_f32 v1, v162, v163\n"
  "v_cvt_pk_bf16_f32 v2, v164, v165\n"
  "v_cvt_pk_bf16_f32 v3, v166, v167\n"
  "v_fma_mix_f32 v168, v4, v128, v136 op_sel:[0,0,0] op_sel_hi:[1,0,0]\n"
  "v_fma_mix_f32 v169, v4, v129, v137 op_sel:[1,0,0] op_sel_hi:[1,0,0]\n"
  "v_fma_mix_f32 v170, v5, v130, v138 op_sel:[0,0,0] op_sel_hi:[1,0,0]\n"
  "v_fma_mix_f32 v171, v5, v131, v139 op_sel:[1,0,0] op_sel_hi:[1,0,0]\n"
  "v_fma_mix_f32 v172, v6, v132, v140 op_sel:[0,0,0] op_sel_hi:[1,0,0]\n"
  "v_fma_mix_f32 v173, v6, v133, v141 op_sel:[1,0,0] op_sel_hi:[1,0,0]\n"
  "v_fma_mix_f32 v174, v7, v134, v142 op_sel:[0,0,0] op_sel_hi:[1,0,0]\n"
  "v_fma_mix_f32 v175, v7, v135, v143 op_sel:[1,0,0] op_sel_hi:[1,0,0]\n"
  "v_cvt_pk_bf16_f32 v4, v168, v169\n"
  "v_cvt_pk_bf16_f32 v5, v170, v171\n"
  "v_cvt_pk_bf16_f32 v6, v172, v173\n"
  "v_cvt_pk_bf16_f32 v7, v174, v175\n"
  "v_fma_mix_f32 v160, v8, v128, v136 op_sel:[0,0,0] op_sel_hi:[1,0,0]\n"
  "v_fma_mix_f32 v161, v8, v129, v137 op_sel:[1,0,0] op_sel_hi:[1,0,0]\n"
  "v_fma_mix_f32 v162, v9, v130, v138 op_sel:[0,0,0] op_sel_hi:[1,0,0]\n"
  "v_fma_mix_f32 v163, v9, v131, v139 op_sel:[1,0,0] op_sel_hi:[1,0,0]\n"
  "v_fma_mix_f32 v164, v10, v132, v140 op_sel:[0,0,0] op_sel_hi:[1,0,0]\n"
  "v_fma_mix_f32 v165, v10, v133, v141 op_sel:[1,0,0] op_sel_hi:[1,0,0]\n"
  "v_fma_mix_f32 v166, v11, v134, v142 op_sel:[0,0,0] op_sel_hi:[1,0,0]\n"
  "v_fma_mix_f32 v167, v11, v135, v143 op_sel:[1,0,0] op_sel_hi:[1,0,0]\n"
  "v_cvt_pk_bf16_f32 v8, v160, v161\n"
  "v_cvt_pk_bf16_f32 v9, v162, v163\n"
  "v_cvt_pk_bf16_f32 v10, v164, v165\n"
  "v_cvt_pk_bf16_f32 v11, v166, v167\n"
  "v_fma_mix_f32 v168, v12, v128, v136 op_sel:[0,0,0] op_sel_hi:[1,0,0]\n"
  "v_fma_mix_f32 v169, v12, v129, v137 op_sel:[1,0,0] op_sel_hi:[1,0,0]\n"
  "v_fma_mix_f32 v170, v13, v130, v138 op_sel:[0,0,0] op_sel_hi:[1,0,0]\n"
  "v_fma_mix_f32 v171, v13, v131, v139 op_sel:[1,0,0] op_sel_hi:[1,0,0]\n"
  "v_fma_mix_f32 v172, v14, v132, v140 op_sel:[0,0,0] op_sel_hi:[1,0,0]\n"
  "v_fma_mix_f32 v173, v14, v133, v141 op_sel:[1,0,0] op_sel_hi:[1,0,0]\n"
  "v_fma_mix_f32 v174, v15, v134, v142 op_sel:[0,0,0] op_sel_hi:[1,0,0]\n"
  "v_fma_mix_f32 v175, v15, v135, v143 op_sel:[1,0,0] op_sel_hi:[1,0,0]\n"
  "v_cvt_pk_bf16_f32 v12, v168, v169\n"
  "v_cvt_pk_bf16_f32 v13, v170, v171\n"
  "v_cvt_pk_bf16_f32 v14, v172, v173\n"
  "v_cvt_pk_bf16_f32 v15, v174, v175\n"
  "s_waitcnt lgkmcnt(14)\n"
  "v_mfma_f32_16x16x32_bf16 a[0:3], a[128:131], v[0:3], 0\n"
  "s_waitcnt vmcnt(4)\n"
  "s_waitcnt lgkmcnt(15)\n"
  "ds_read_b64_tr_b16 a[160:161], v204 offset:24576\n"
  "v_fma_mix_f32 v160, v16, v144, v152 op_sel:[0,0,0] op_sel_hi:[1,0,0]\n"
  "v_fma_mix_f32 v161, v16, v145, v153 op_sel:[1,0,0] op_sel_hi:[1,0,0]\n"
  "v_mfma_f32_16x16x32_bf16 a[32:35], a[128:131], v[4:7], 0\n"
  "v_fma_mix_f32 v162, v17, v146, v154 op_sel:[0,0,0] op_sel_hi:[1,0,0]\n"
  "v_fma_mix_f32 v163, v17, v147, v155 op_sel:[1,0,0] op_sel_hi:[1,0,0]\n"
  "v_mfma_f32_16x16x32_bf16 a[64:67], a[128:131], v[8:11], 0\n"
  "ds_read_b64_tr_b16 a[162:163], v204 offset:28672\n"
  "v_fma_mix_f32 v164, v18, v148, v156 op_sel:[0,0,0] op_sel_hi:[1,0,0]\n"
  "v_fma_mix_f32 v165, v18, v149, v157 op_sel:[1,0,0] op_sel_hi:[1,0,0]\n"
  "v_mfma_f32_16x16x32_bf16 a[96:99], a[128:131], v[12:15], 0\n"
  "s_waitcnt lgkmcnt(14)\n"
  "ds_read_b128 v[128:131], v176 offset:512\n"
  "v_fma_mix_f32 v166, v19, v150, v158 op_sel:[0,0,0] op_sel_hi:[1,0,0]\n"
  "v_mfma_f32_16x16x32_bf16 a[4:7], a[132:135], v[0:3], 0\n"
  "ds_read_b64_tr_b16 a[164:165], v204 offset:24584\n"
  "v_fma_mix_f32 v167, v19, v151, v159 op_sel:[1,0,0] op_sel_hi:[1,0,0]\n"
  "v_mfma_f32_16x16x32_bf16 a[36:39], a[132:135], v[4:7], 0\n"
  "s_add_u32 s66, %[sHoff], 2048\n"
  "buffer_load_dwordx4 v[32:35], %[vL16], %[srdH], s66 offen\n"
  "v_cvt_pk_bf16_f32 v16, v160, v161\n"
  "v_cvt_pk_bf16_f32 v17, v162, v163\n"
  "v_mfma_f32_16x16x32_bf16 a[68:71], a[132:135], v[8:11], 0\n"
  "ds_read_b64_tr_b16 a[166:167], v204 offset:28680\n"
  "v_cvt_pk_bf16_f32 v18, v164, v165\n"
  "v_mfma_f32_16x16x32_bf16 a[100:103], a[132:135], v[12:15], 0\n"
  "s_waitcnt lgkmcnt(15)\n"
  "ds_read_b128 v[132:135], v176 offset:528\n"
  "v_cvt_pk_bf16_f32 v19, v166, v167\n"
  "v_mfma_f32_16x16x32_bf16 a[8:11], a[136:139], v[0:3], 0\n"
  "ds_read_b64_tr_b16 a[168:169], v204 offset:25088\n"
  "v_fma_mix_f32 v168, v20, v144, v152 op_sel:[0,0,0] op_sel_hi:[1,0,0]\n"
  "v_fma_mix_f32 v169, v20, v145, v153 op_sel:[1,0,0] op_sel_hi:[1,0,0]\n"
  "v_mfma_f32_16x16x32_bf16 a[40:43], a[136:139], v[4:7], 0\n"
  "s_add_u32 s67, %[sHoff], 18432\n"
  "buffer_load_dwordx4 v[36:39], %[vL16], %[srdH], s67 offen\n"
  "v_fma_mix_f32 v170, v21, v146, v154 op_sel:[0,0,0] op_sel_hi:[1,0,0]\n"
  "v_mfma_f32_16x16x32_bf16 a[72:75], a[136:139], v[8:11], 0\n"
  "ds_read_b64_tr_b16 a[170:171], v204 offset:29184\n"
  "v_fma_mix_f32 v171, v21, v147, v155 op_sel:[1,0,0] op_sel_hi:[1,0,0]\n"
  "v_mfma_f32_16x16x32_bf16 a[104:107], a[136:139], v[12:15], 0\n"
  "s_waitcnt lgkmcnt(15)\n"
  "ds_read_b128 v[136:139], v176 offset:544\n"
  "v_fma_mix_f32 v172, v22, v148, v156 op_sel:[0,0,0] op_sel_hi:[1,0,0]\n"
  "v_fma_mix_f32 v173, v22, v149, v157 op_sel:[1,0,0] op_sel_hi:[1,0,0]\n"
  "v_mfma_f32_16x16x32_bf16 a[12:15], a[140:143], v[0:3], 0\n"
  "ds_read_b64_tr_b16 a[172:173], v204 offset:25096\n"
  "v_fma_mix_f32 v174, v23, v150, v158 op_sel:[0,0,0] op_sel_hi:[1,0,0]\n"
  "v_mfma_f32_16x16x32_bf16 a[44:47], a[140:143], v[4:7], 0\n"
  "s_add_u32 s66, %[sHoff], 34816\n"
  "buffer_load_dwordx4 v[40:43], %[vL16], %[srdH], s66 offen\n"
  "v_fma_mix_f32 v175, v23, v151, v159 op_sel:[1,0,0] op_sel_hi:[1,0,0]\n"
  "v_mfma_f32_16x16x32_bf16 a[76:79], a[140:143], v[8:11], 0\n"
  "ds_read_b64_tr_b16 a[174:175], v204 offset:29192\n"
  "v_cvt_pk_bf16_f32 v20, v168, v169\n"
  "v_cvt_pk_bf16_f32 v21, v170, v171\n"
  "v_mfma_f32_16x16x32_bf16 a[108:111], a[140:143], v[12:15], 0\n"
  "s_waitcnt lgkmcnt(15)\n"
  "ds_read_b128 v[140:143], v176 offset:560\n"
  "v_cvt_pk_bf16_f32 v22, v172, v173\n"
  "v_mfma_f32_16x16x32_bf16 a[16:19], a[144:147], v[0:3], 0\n"
  "ds_read_b64_tr_b16 a[176:177], v204 offset:25600\n"
  "v_cvt_pk_bf16_f32 v23, v174, v175\n"
  "v_mfma_f32_16x16x32_bf16 a[48:51], a[144:147], v[4:7], 0\n"
  "s_add_u32 s67, %[sHoff], 51200\n"
  "buffer_load_dwordx4 v[44:47], %[vL16], %[srdH], s67 offen\n"
  "v_fma_mix_f32 v160, v24, v144, v152 op_sel:[0,0,0] op_sel_hi:[1,0,0]\n"
  "v_fma_mix_f32 v161, v24, v145, v153 op_sel:[1,0,0] op_sel_hi:[1,0,0]\n"
  "v_mfma_f32_16x16x32_bf16 a[80:83], a[144:147], v[8:11], 0\n"
  "ds_read_b64_tr_b16 a[178:179], v204 offset:29696\n"
  "v_fma_mix_f32 v162, v25, v146, v154 op_sel:[0,0,0] op_sel_hi:[1,0,0]\n"
  "v_mfma_f32_16x16x32_bf16 a[112:115], a[144:147], v[12:15], 0\n"
  "s_waitcnt lgkmcnt(15)\n"
  "v_fma_mix_f32 v163, v25, v147, v155 op_sel:[1,0,0] op_sel_hi:[1,0,0]\n"
  "v_fma_mix_f32 v164, v26, v148, v156 op_sel:[0,0,0] op_sel_hi:[1,0,0]\n"
  "v_mfma_f32_16x16x32_bf16 a[20:23], a[148:151], v[0:3], 0\n"
  "ds_read_b64_tr_b16 a[180:181], v204 offset:25608\n"
  "v_fma_mix_f32 v165, v26, v149, v157 op_sel:[1,0,0] op_sel_hi:[1,0,0]\n"
  "v_fma_mix_f32 v166, v27, v150, v158 op_sel:[0,0,0] op_sel_hi:[1,0,0]\n"
  "v_mfma_f32_16x16x32_bf16 a[52:55], a[148:151], v[4:7], 0\n"
  "s_add_u32 s66, %[sHoff], 3072\n"
  "buffer_load_dwordx4 v[48:51], %[vL16], %[srdH], s66 offen\n"
  "v_fma_mix_f32 v167, v27, v151, v159 op_sel:[1,0,0] op_sel_hi:[1,0,0]\n"
  "v_mfma_f32_16x16x32_bf16 a[84:87], a[148:151], v[8:11], 0\n"
  "ds_read_b64_tr_b16 a[182:183], v204 offset:29704\n"
  "v_cvt_pk_bf16_f32 v24, v160, v161\n"
  "v_mfma_f32_16x16x32_bf16 a[116:119], a[148:151], v[12:15], 0\n"
  "s_waitcnt lgkmcnt(15)\n"
  "v_cvt_pk_bf16_f32 v25, v162, v163\n"
  "v_cvt_pk_bf16_f32 v26, v164, v165\n"
  "v_cvt_pk_bf16_f32 v27, v166, v167\n"
  "v_mfma_f32_16x16x32_bf16 a[24:27], a[152:155], v[0:3], 0\n"
  "ds_read_b64_tr_b16 a[184:185], v204 offset:26112\n"
  "v_fma_mix_f32 v168, v28, v144, v152 op_sel:[0,0,0] op_sel_hi:[1,0,0]\n"
  "v_mfma_f32_16x16x32_bf16 a[56:59], a[152:155], v[4:7], 0\n"
  "s_add_u32 s67, %[sHoff], 19456\n"
  "buffer_load_dwordx4 v[52:55], %[vL16], %[srdH], s67 offen\n"
  "v_fma_mix_f32 v169, v28, v145, v153 op_sel:[1,0,0] op_sel_hi:[1,0,0]\n"
  "v_mfma_f32_16x16x32_bf16 a[88:91], a[152:155], v[8:11], 0\n"
  "ds_read_b64_tr_b16 a[186:187], v204 offset:30208\n"
  "v_fma_mix_f32 v170, v29, v146, v154 op_sel:[0,0,0] op_sel_hi:[1,0,0]\n"
  "v_fma_mix_f32 v171, v29, v147, v155 op_sel:[1,0,0] op_sel_hi:[1,0,0]\n"
  "v_mfma_f32_16x16x32_bf16 a[120:123], a[152:155], v[12:15], 0\n"
  "s_waitcnt lgkmcnt(15)\n"
  "v_fma_mix_f32 v172, v30, v148, v156 op_sel:[0,0,0] op_sel_hi:[1,0,0]\n"
  "v_fma_mix_f32 v173, v30, v149, v157 op_sel:[1,0,0] op_sel_hi:[1,0,0]\n"
  "v_mfma_f32_16x16x32_bf16 a[28:31], a[156:159], v[0:3], 0\n"
  "ds_read_b64_tr_b16 a[188:189], v204 offset:26120\n"
  "v_fma_mix_f32 v174, v31, v150, v158 op_sel:[0,0,0] op_sel_hi:[1,0,0]\n"
  "v_mfma_f32_16x16x32_bf16 a[60:63], a[156:159], v[4:7], 0\n"
  "s_add_u32 s66, %[sHoff], 35840\n"
  "buffer_load_dwordx4 v[56:59], %[vL16], %[srdH], s66 offen\n"
  "v_fma_mix_f32 v175, v31, v151, v159 op_sel:[1,0,0] op_sel_hi:[1,0,0]\n"
  "v_cvt_pk_bf16_f32 v28, v168, v169\n"
  "v_mfma_f32_16x16x32_bf16 a[92:95], a[156:159], v[8:11], 0\n"
  "ds_read_b64_tr_b16 a[190:191], v204 offset:30216\n"
  "v_cvt_pk_bf16_f32 v29, v170, v171\n"
  "v_mfma_f32_16x16x32_bf16 a[124:127], a[156:159], v[12:15], 0\n"
  "s_waitcnt lgkmcnt(15)\n"
  "v_cvt_pk_bf16_f32 v30, v172, v173\n"
  "v_cvt_pk_bf16_f32 v31, v174, v175\n"
  "s_waitcnt vmcnt(7)\n"
  "s_barrier\n"
  "v_mfma_f32_16x16x32_bf16 a[0:3], a[160:163], v[16:19], a[0:3]\n"
  "s_waitcnt vmcnt(3)\n"
  "s_waitcnt lgkmcnt(8)\n"
  "ds_read_b64_tr_b16 a[128:129], v205 offset:16384\n"
  "v_fma_mix_f32 v160, v32, v128, v136 op_sel:[0,0,0] op_sel_hi:[1,0,0]\n"
  "v_fma_mix_f32 v161, v32, v129, v137 op_sel:[1,0,0] op_sel_hi:[1,0,0]\n"
  "v_mfma_f32_16x16x32_bf16 a[32:35], a[160:163], v[20:23], a[32:35]\n"
  "v_fma_mix_f32 v162, v33, v130, v138 op_sel:[0,0,0] op_sel_hi:[1,0,0]\n"
  "v_fma_mix_f32 v163, v33, v131, v139 op_sel:[1,0,0] op_sel_hi:[1,0,0]\n"
  "v_mfma_f32_16x16x32_bf16 a[64:67], a[160:163], v[24:27], a[64:67]\n"
  "ds_read_b64_tr_b16 a[130:131], v205 offset:20480\n"
  "v_fma_mix_f32 v164, v34, v132, v140 op_sel:[0,0,0] op_sel_hi:[1,0,0]\n"
  "v_fma_mix_f32 v165, v34, v133, v141 op_sel:[1,0,0] op_sel_hi:[1,0,0]\n"
  "v_mfma_f32_16x16x32_bf16 a[96:99], a[160:163], v[28:31], a[96:99]\n"
  "s_waitcnt lgkmcnt(15)\n"
  "ds_read_b128 v[144:147], v176 offset:768\n"
  "v_fma_mix_f32 v166, v35, v134, v142 op_sel:[0,0,0] op_sel_hi:[1,0,0]\n"
  "v_mfma_f32_16x16x32_bf16 a[4:7], a[164:167], v[16:19], a[4:7]\n"
  "ds_read_b64_tr_b16 a[132:133], v205 offset:16392\n"
  "v_fma_mix_f32 v167, v35, v135, v143 op_sel:[1,0,0] op_sel_hi:[1,0,0]\n"
  "v_mfma_f32_16x16x32_bf16 a[36:39], a[164:167], v[20:23], a[36:39]\n"
  "s_add_u32 s67, %[sHoff], 52224\n"
  "buffer_load_dwordx4 v[60:63], %[vL16], %[srdH], s67 offen\n"
  "v_cvt_pk_bf16_f32 v32, v160, v161\n"
  "v_cvt_pk_bf16_f32 v33, v162, v163\n"
  "v_mfma_f32_16x16x32_bf16 a[68:71], a[164:167], v[24:27], a[68:71]\n"
  "ds_read_b64_tr_b16 a[134:135], v205 offset:20488\n"
  "v_cvt_pk_bf16_f32 v34, v164, v165\n"
  "v_mfma_f32_16x16x32_bf16 a[100:103], a[164:167], v[28:31], a[100:103]\n"
  "s_waitcnt lgkmcnt(15)\n"
  "ds_read_b128 v[148:151], v176 offset:784\n"
  "v_cvt_pk_bf16_f32 v35, v166, v167\n"
  "v_mfma_f32_16x16x32_bf16 a[8:11], a[168:171], v[16:19], a[8:11]\n"
  "s_add_u32 m0, %[sM0], 32768\n"
  "ds_read_b64_tr_b16 a[136:137], v205 offset:16896\n"
  "v_fma_mix_f32 v168, v36, v128, v136 op_sel:[0,0,0] op_sel_hi:[1,0,0]\n"
  "v_fma_mix_f32 v169, v36, v129, v137 op_sel:[1,0,0] op_sel_hi:[1,0,0]\n"
  "v_mfma_f32_16x16x32_bf16 a[40:43], a[168:171], v[20:23], a[40:43]\n"
  "s_add_u32 s60, %[sWoff], 1048576\n"
  "buffer_load_dwordx4 %[vWd], %[srdW], s60 offen lds\n"
  "v_fma_mix_f32 v170, v37, v130, v138 op_sel:[0,0,0] op_sel_hi:[1,0,0]\n"
  "v_mfma_f32_16x16x32_bf16 a[72:75], a[168:171], v[24:27], a[72:75]\n"
  "ds_read_b64_tr_b16 a[138:139], v205 offset:20992\n"
  "v_fma_mix_f32 v171, v37, v131, v139 op_sel:[1,0,0] op_sel_hi:[1,0,0]\n"
  "v_mfma_f32_16x16x32_bf16 a[104:107], a[168:171], v[28:31], a[104:107]\n"
  "s_waitcnt lgkmcnt(15)\n"
  "ds_read_b128 v[152:155], v176 offset:800\n"
  "v_fma_mix_f32 v172, v38, v132, v140 op_sel:[0,0,0] op_sel_hi:[1,0,0]\n"
  "v_fma_mix_f32 v173, v38, v133, v141 op_sel:[1,0,0] op_sel_hi:[1,0,0]\n"
  "v_mfma_f32_16x16x32_bf16 a[12:15], a[172:175], v[16:19], a[12:15]\n"
  "s_add_u32 m0, %[sM0], 33792\n"
  "ds_read_b64_tr_b16 a[140:141], v205 offset:16904\n"
  "v_fma_mix_f32 v174, v39, v134, v142 op_sel:[0,0,0] op_sel_hi:[1,0,0]\n"
  "v_mfma_f32_16x16x32_bf16 a[44:47], a[172:175], v[20:23], a[44:47]\n"
  "s_add_u32 s61, %[sWoff], 1048704\n"
  "buffer_load_dwordx4 %[vWd], %[srdW], s61 offen lds\n"
  "v_fma_mix_f32 v175, v39, v135, v143 op_sel:[1,0,0] op_sel_hi:[1,0,0]\n"
  "v_mfma_f32_16x16x32_bf16 a[76:79], a[172:175], v[24:27], a[76:79]\n"
  "ds_read_b64_tr_b16 a[142:143], v205 offset:21000\n"
  "v_cvt_pk_bf16_f32 v36, v168, v169\n"
  "v_cvt_pk_bf16_f32 v37, v170, v171\n"
  "v_mfma_f32_16x16x32_bf16 a[108:111], a[172:175], v[28:31], a[108:111]\n"
  "s_waitcnt lgkmcnt(15)\n"
  "ds_read_b128 v[156:159], v176 offset:816\n"
  "v_cvt_pk_bf16_f32 v38, v172, v173\n"
  "v_mfma_f32_16x16x32_bf16 a[16:19], a[176:179], v[16:19], a[16:19]\n"
  "s_add_u32 m0, %[sM0], 34816\n"
  "ds_read_b64_tr_b16 a[144:145], v205 offset:17408\n"
  "v_cvt_pk_bf16_f32 v39, v174, v175\n"
  "v_mfma_f32_16x16x32_bf16 a[48:51], a[176:179], v[20:23], a[48:51]\n"
  "s_add_u32 s60, %[sWoff], 1114112\n"
  "buffer_load_dwordx4 %[vWd], %[srdW], s60 offen lds\n"
  "v_fma_mix_f32 v160, v40, v128, v136 op_sel:[0,0,0] op_sel_hi:[1,0,0]\n"
  "v_fma_mix_f32 v161, v40, v129, v137 op_sel:[1,0,0] op_sel_hi:[1,0,0]\n"
  "v_mfma_f32_16x16x32_bf16 a[80:83], a[176:179], v[24:27], a[80:83]\n"
  "ds_read_b64_tr_b16 a[146:147], v205 offset:21504\n"
  "v_fma_mix_f32 v162, v41, v130, v138 op_sel:[0,0,0] op_sel_hi:[1,0,0]\n"
  "v_mfma_f32_16x16x32_bf16 a[112:115], a[176:179], v[28:31], a[112:115]\n"
  "s_waitcnt lgkmcnt(15)\n"
  "v_fma_mix_f32 v163, v41, v131, v139 op_sel:[1,0,0] op_sel_hi:[1,0,0]\n"
  "v_fma_mix_f32 v164, v42, v132, v140 op_sel:[0,0,0] op_sel_hi:[1,0,0]\n"
  "v_mfma_f32_16x16x32_bf16 a[20:23], a[180:183], v[16:19], a[20:23]\n"
  "s_add_u32 m0, %[sM0], 35840\n"
  "ds_read_b64_tr_b16 a[148:149], v205 offset:17416\n"
  "v_fma_mix_f32 v165, v42, v133, v141 op_sel:[1,0,0] op_sel_hi:[1,0,0]\n"
  "v_fma_mix_f32 v166, v43, v134, v142 op_sel:[0,0,0] op_sel_hi:[1,0,0]\n"
  "v_mfma_f32_16x16x32_bf16 a[52:55], a[180:183], v[20:23], a[52:55]\n"
  "s_add_u32 s61, %[sWoff], 1114240\n"
  "buffer_load_dwordx4 %[vWd], %[srdW], s61 offen lds\n"
  "v_fma_mix_f32 v167, v43, v135, v143 op_sel:[1,0,0] op_sel_hi:[1,0,0]\n"
  "v_mfma_f32_16x16x32_bf16 a[84:87], a[180:183], v[24:27], a[84:87]\n"
  "ds_read_b64_tr_b16 a[150:151], v205 offset:21512\n"
  "v_cvt_pk_bf16_f32 v40, v160, v161\n"
  "v_mfma_f32_16x16x32_bf16 a[116:119], a[180:183], v[28:31], a[116:119]\n"
  "s_waitcnt lgkmcnt(15)\n"
  "v_cvt_pk_bf16_f32 v41, v162, v163\n"
  "v_cvt_pk_bf16_f32 v42, v164, v165\n"
  "v_cvt_pk_bf16_f32 v43, v166, v167\n"
  "v_mfma_f32_16x16x32_bf16 a[24:27], a[184:187], v[16:19], a[24:27]\n"
  "ds_read_b64_tr_b16 a[152:153], v205 offset:17920\n"
  "v_fma_mix_f32 v168, v44, v128, v136 op_sel:[0,0,0] op_sel_hi:[1,0,0]\n"
  "v_mfma_f32_16x16x32_bf16 a[56:59], a[184:187], v[20:23], a[56:59]\n"
  "s_add_u32 s66, %[sHoff], 4096\n"
  "buffer_load_dwordx4 v[64:67], %[vL16], %[srdH], s66 offen\n"
  "v_fma_mix_f32 v169, v44, v129, v137 op_sel:[1,0,0] op_sel_hi:[1,0,0]\n"
  "v_mfma_f32_16x16x32_bf16 a[88:91], a[184:187], v[24:27], a[88:91]\n"
  "ds_read_b64_tr_b16 a[154:155], v205 offset:22016\n"
  "v_fma_mix_f32 v170, v45, v130, v138 op_sel:[0,0,0] op_sel_hi:[1,0,0]\n"
  "v_fma_mix_f32 v171, v45, v131, v139 op_sel:[1,0,0] op_sel_hi:[1,0,0]\n"
  "v_mfma_f32_16x16x32_bf16 a[120:123], a[184:187], v[28:31], a[120:123]\n"
  "s_waitcnt lgkmcnt(15)\n"
  "v_fma_mix_f32 v172, v46, v132, v140 op_sel:[0,0,0] op_sel_hi:[1,0,0]\n"
  "v_fma_mix_f32 v173, v46, v133, v141 op_sel:[1,0,0] op_sel_hi:[1,0,0]\n"
  "v_mfma_f32_16x16x32_bf16 a[28:31], a[188:191], v[16:19], a[28:31]\n"
  "ds_read_b64_tr_b16 a[156:157], v205 offset:17928\n"
  "v_fma_mix_f32 v174, v47, v134, v142 op_sel:[0,0,0] op_sel_hi:[1,0,0]\n"
  "v_mfma_f32_16x16x32_bf16 a[60:63], a[188:191], v[20:23], a[60:63]\n"
  "s_add_u32 s67, %[sHoff], 20480\n"
  "buffer_load_dwordx4 v[68:71], %[vL16], %[srdH], s67 offen\n"
  "v_fma_mix_f32 v175, v47, v135, v143 op_sel:[1,0,0] op_sel_hi:[1,0,0]\n"
  "v_cvt_pk_bf16_f32 v44, v168, v169\n"
  "v_mfma_f32_16x16x32_bf16 a[92:95], a[188:191], v[24:27], a[92:95]\n"
  "ds_read_b64_tr_b16 a[158:159], v205 offset:22024\n"
  "v_cvt_pk_bf16_f32 v45, v170, v171\n"
  "v_mfma_f32_16x16x32_bf16 a[124:127], a[188:191], v[28:31], a[124:127]\n"
  "s_waitcnt lgkmcnt(15)\n"
  "v_cvt_pk_bf16_f32 v46, v172, v173\n"
  "v_cvt_pk_bf16_f32 v47, v174, v175\n"
  "v_mfma_f32_16x16x32_bf16 a[0:3], a[128:131], v[32:35], a[0:3]\n"
  "s_waitcnt vmcnt(6)\n"
  "s_waitcnt lgkmcnt(8)\n"
  "ds_read_b64_tr_b16 a[160:161], v205 offset:24576\n"
  "v_fma_mix_f32 v160, v48, v144, v152 op_sel:[0,0,0] op_sel_hi:[1,0,0]\n"
  "v_fma_mix_f32 v161, v48, v145, v153 op_sel:[1,0,0] op_sel_hi:[1,0,0]\n"
  "v_mfma_f32_16x16x32_bf16 a[32:35], a[128:131], v[36:39], a[32:35]\n"
  "v_fma_mix_f32 v162, v49, v146, v154 op_sel:[0,0,0] op_sel_hi:[1,0,0]\n"
  "v_fma_mix_f32 v163, v49, v147, v155 op_sel:[1,0,0] op_sel_hi:[1,0,0]\n"
  "v_mfma_f32_16x16x32_bf16 a[64:67], a[128:131], v[40:43], a[64:67]\n"
  "ds_read_b64_tr_b16 a[162:163], v205 offset:28672\n"
  "v_fma_mix_f32 v164, v50, v148, v156 op_sel:[0,0,0] op_sel_hi:[1,0,0]\n"
  "v_fma_mix_f32 v165, v50, v149, v157 op_sel:[1,0,0] op_sel_hi:[1,0,0]\n"
  "v_mfma_f32_16x16x32_bf16 a[96:99], a[128:131], v[44:47], a[96:99]\n"
  "s_waitcnt lgkmcnt(15)\n"
  "ds_read_b128 v[128:131], v176 offset:1024\n"
  "v_fma_mix_f32 v166, v51, v150, v158 op_sel:[0,0,0] op_sel_hi:[1,0,0]\n"
  "v_mfma_f32_16x16x32_bf16 a[4:7], a[132:135], v[32:35], a[4:7]\n"
  "ds_read_b64_tr_b16 a[164:165], v205 offset:24584\n"
  "v_fma_mix_f32 v167, v51, v151, v159 op_sel:[1,0,0] op_sel_hi:[1,0,0]\n"
  "v_mfma_f32_16x16x32_bf16 a[36:39], a[132:135], v[36:39], a[36:39]\n"
  "s_add_u32 s66, %[sHoff], 36864\n"
  "buffer_load_dwordx4 v[72:75], %[vL16], %[srdH], s66 offen\n"
  "v_cvt_pk_bf16_f32 v48, v160, v161\n"
  "v_cvt_pk_bf16_f32 v49, v162, v163\n"
  "v_mfma_f32_16x16x32_bf16 a[68:71], a[132:135], v[40:43], a[68:71]\n"
  "ds_read_b64_tr_b16 a[166:167], v205 offset:28680\n"
  "v_cvt_pk_bf16_f32 v50, v164, v165\n"
  "v_mfma_f32_16x16x32_bf16 a[100:103], a[132:135], v[44:47], a[100:103]\n"
  "s_waitcnt lgkmcnt(15)\n"
  "ds_read_b128 v[132:135], v176 offset:1040\n"
  "v_cvt_pk_bf16_f32 v51, v166, v167\n"
  "v_mfma_f32_16x16x32_bf16 a[8:11], a[136:139], v[32:35], a[8:11]\n"
  "ds_read_b64_tr_b16 a[168:169], v205 offset:25088\n"
  "v_fma_mix_f32 v168, v52, v144, v152 op_sel:[0,0,0] op_sel_hi:[1,0,0]\n"
  "v_fma_mix_f32 v169, v52, v145, v153 op_sel:[1,0,0] op_sel_hi:[1,0,0]\n"
  "v_mfma_f32_16x16x32_bf16 a[40:43], a[136:139], v[36:39], a[40:43]\n"
  "s_add_u32 s67, %[sHoff], 53248\n"
  "buffer_load_dwordx4 v[76:79], %[vL16], %[srdH], s67 offen\n"
  "v_fma_mix_f32 v170, v53, v146, v154 op_sel:[0,0,0] op_sel_hi:[1,0,0]\n"
  "v_mfma_f32_16x16x32_bf16 a[72:75], a[136:139], v[40:43], a[72:75]\n"
  "ds_read_b64_tr_b16 a[170:171], v205 offset:29184\n"
  "v_fma_mix_f32 v171, v53, v147, v155 op_sel:[1,0,0] op_sel_hi:[1,0,0]\n"
  "v_mfma_f32_16x16x32_bf16 a[104:107], a[136:139], v[44:47], a[104:107]\n"
  "s_waitcnt lgkmcnt(15)\n"
  "ds_read_b128 v[136:139], v176 offset:1056\n"
  "v_fma_mix_f32 v172, v54, v148, v156 op_sel:[0,0,0] op_sel_hi:[1,0,0]\n"
  "v_fma_mix_f32 v173, v54, v149, v157 op_sel:[1,0,0] op_sel_hi:[1,0,0]\n"
  "v_mfma_f32_16x16x32_bf16 a[12:15], a[140:143], v[32:35], a[12:15]\n"
  "ds_read_b64_tr_b16 a[172:173], v205 offset:25096\n"
  "v_fma_mix_f32 v174, v55, v150, v158 op_sel:[0,0,0] op_sel_hi:[1,0,0]\n"
  "v_mfma_f32_16x16x32_bf16 a[44:47], a[140:143], v[36:39], a[44:47]\n"
  "s_add_u32 s66, %[sHoff], 5120\n"
  "buffer_load_dwordx4 v[80:83], %[vL16], %[srdH], s66 offen\n"
  "v_fma_mix_f32 v175, v55, v151, v159 op_sel:[1,0,0] op_sel_hi:[1,0,0]\n"
  "v_mfma_f32_16x16x32_bf16 a[76:79], a[140:143], v[40:43], a[76:79]\n"
  "ds_read_b64_tr_b16 a[174:175], v205 offset:29192\n"
  "v_cvt_pk_bf16_f32 v52, v168, v169\n"
  "v_cvt_pk_bf16_f32 v53, v170, v171\n"
  "v_mfma_f32_16x16x32_bf16 a[108:111], a[140:143], v[44:47], a[108:111]\n"
  "s_waitcnt lgkmcnt(15)\n"
  "ds_read_b128 v[140:143], v176 offset:1072\n"
  "v_cvt_pk_bf16_f32 v54, v172, v173\n"
  "v_mfma_f32_16x16x32_bf16 a[16:19], a[144:147], v[32:35], a[16:19]\n"
  "ds_read_b64_tr_b16 a[176:177], v205 offset:25600\n"
  "v_cvt_pk_bf16_f32 v55, v174, v175\n"
  "v_mfma_f32_16x16x32_bf16 a[48:51], a[144:147], v[36:39], a[48:51]\n"
  "s_add_u32 s67, %[sHoff], 21504\n"
  "buffer_load_dwordx4 v[84:87], %[vL16], %[srdH], s67 offen\n"
  "v_fma_mix_f32 v160, v56, v144, v152 op_sel:[0,0,0] op_sel_hi:[1,0,0]\n"
  "v_fma_mix_f32 v161, v56, v145, v153 op_sel:[1,0,0] op_sel_hi:[1,0,0]\n"
  "v_mfma_f32_16x16x32_bf16 a[80:83], a[144:147], v[40:43], a[80:83]\n"
  "ds_read_b64_tr_b16 a[178:179], v205 offset:29696\n"
  "v_fma_mix_f32 v162, v57, v146, v154 op_sel:[0,0,0] op_sel_hi:[1,0,0]\n"
  "v_mfma_f32_16x16x32_bf16 a[112:115], a[144:147], v[44:47], a[112:115]\n"
  "s_waitcnt lgkmcnt(15)\n"
  "v_fma_mix_f32 v163, v57, v147, v155 op_sel:[1,0,0] op_sel_hi:[1,0,0]\n"
  "v_fma_mix_f32 v164, v58, v148, v156 op_sel:[0,0,0] op_sel_hi:[1,0,0]\n"
  "v_mfma_f32_16x16x32_bf16 a[20:23], a[148:151], v[32:35], a[20:23]\n"
  "ds_read_b64_tr_b16 a[180:181], v205 offset:25608\n"
  "v_fma_mix_f32 v165, v58, v149, v157 op_sel:[1,0,0] op_sel_hi:[1,0,0]\n"
  "v_fma_mix_f32 v166, v59, v150, v158 op_sel:[0,0,0] op_sel_hi:[1,0,0]\n"
  "v_mfma_f32_16x16x32_bf16 a[52:55], a[148:151], v[36:39], a[52:55]\n"
  "s_add_u32 s66, %[sHoff], 37888\n"
  "buffer_load_dwordx4 v[88:91], %[vL16], %[srdH], s66 offen\n"
  "v_fma_mix_f32 v167, v59, v151, v159 op_sel:[1,0,0] op_sel_hi:[1,0,0]\n"
  "v_mfma_f32_16x16x32_bf16 a[84:87], a[148:151], v[40:43], a[84:87]\n"
  "ds_read_b64_tr_b16 a[182:183], v205 offset:29704\n"
  "v_cvt_pk_bf16_f32 v56, v160, v161\n"
  "v_mfma_f32_16x16x32_bf16 a[116:119], a[148:151], v[44:47], a[116:119]\n"
  "s_waitcnt lgkmcnt(15)\n"
  "v_cvt_pk_bf16_f32 v57, v162, v163\n"
  "v_cvt_pk_bf16_f32 v58, v164, v165\n"
  "v_cvt_pk_bf16_f32 v59, v166, v167\n"
  "v_mfma_f32_16x16x32_bf16 a[24:27], a[152:155], v[32:35], a[24:27]\n"
  "ds_read_b64_tr_b16 a[184:185], v205 offset:26112\n"
  "v_fma_mix_f32 v168, v60, v144, v152 op_sel:[0,0,0] op_sel_hi:[1,0,0]\n"
  "v_mfma_f32_16x16x32_bf16 a[56:59], a[152:155], v[36:39], a[56:59]\n"
  "s_add_u32 s67, %[sHoff], 54272\n"
  "buffer_load_dwordx4 v[92:95], %[vL16], %[srdH], s67 offen\n"
  "v_fma_mix_f32 v169, v60, v145, v153 op_sel:[1,0,0] op_sel_hi:[1,0,0]\n"
  "v_mfma_f32_16x16x32_bf16 a[88:91], a[152:155], v[40:43], a[88:91]\n"
  "ds_read_b64_tr_b16 a[186:187], v205 offset:30208\n"
  "v_fma_mix_f32 v170, v61, v146, v154 op_sel:[0,0,0] op_sel_hi:[1,0,0]\n"
  "v_fma_mix_f32 v171, v61, v147, v155 op_sel:[1,0,0] op_sel_hi:[1,0,0]\n"
  "v_mfma_f32_16x16x32_bf16 a[120:123], a[152:155], v[44:47], a[120:123]\n"
  "s_waitcnt lgkmcnt(15)\n"
  "v_fma_mix_f32 v172, v62, v148, v156 op_sel:[0,0,0] op_sel_hi:[1,0,0]\n"
  "v_fma_mix_f32 v173, v62, v149, v157 op_sel:[1,0,0] op_sel_hi:[1,0,0]\n"
  "v_mfma_f32_16x16x32_bf16 a[28:31], a[156:159], v[32:35], a[28:31]\n"
  "s_add_u32 m0, %[sM0], 98304\n"
  "ds_read_b64_tr_b16 a[188:189], v205 offset:26120\n"
  "v_fma_mix_f32 v174, v63, v150, v158 op_sel:[0,0,0] op_sel_hi:[1,0,0]\n"
  "v_mfma_f32_16x16x32_bf16 a[60:63], a[156:159], v[36:39], a[60:63]\n"
  "s_add_u32 s60, %[sWoff], 1572864\n"
  "buffer_load_dwordx4 %[vWd], %[srdW], s60 offen lds\n"
  "v_fma_mix_f32 v175, v63, v151, v159 op_sel:[1,0,0] op_sel_hi:[1,0,0]\n"
  "v_cvt_pk_bf16_f32 v60, v168, v169\n"
  "v_mfma_f32_16x16x32_bf16 a[92:95], a[156:159], v[40:43], a[92:95]\n"
  "ds_read_b64_tr_b16 a[190:191], v205 offset:30216\n"
  "v_cvt_pk_bf16_f32 v61, v170, v171\n"
  "v_mfma_f32_16x16x32_bf16 a[124:127], a[156:159], v[44:47], a[124:127]\n"
  "s_waitcnt lgkmcnt(15)\n"
  "v_cvt_pk_bf16_f32 v62, v172, v173\n"
  "v_cvt_pk_bf16_f32 v63, v174, v175\n"
  "s_waitcnt vmcnt(9)\n"
  "s_barrier\n"
  "v_mfma_f32_16x16x32_bf16 a[0:3], a[160:163], v[48:51], a[0:3]\n"
  "s_waitcnt vmcnt(5)\n"
  "s_waitcnt lgkmcnt(8)\n"
  "ds_read_b64_tr_b16 a[128:129], v204 offset:32768\n"
  "v_fma_mix_f32 v160, v64, v128, v136 op_sel:[0,0,0] op_sel_hi:[1,0,0]\n"
  "v_fma_mix_f32 v161, v64, v129, v137 op_sel:[1,0,0] op_sel_hi:[1,0,0]\n"
  "v_mfma_f32_16x16x32_bf16 a[32:35], a[160:163], v[52:55], a[32:35]\n"
  "v_fma_mix_f32 v162, v65, v130, v138 op_sel:[0,0,0] op_sel_hi:[1,0,0]\n"
  "v_fma_mix_f32 v163, v65, v131, v139 op_sel:[1,0,0] op_sel_hi:[1,0,0]\n"
  "v_mfma_f32_16x16x32_bf16 a[64:67], a[160:163], v[56:59], a[64:67]\n"
  "ds_read_b64_tr_b16 a[130:131], v204 offset:36864\n"
  "v_fma_mix_f32 v164, v66, v132, v140 op_sel:[0,0,0] op_sel_hi:[1,0,0]\n"
  "v_fma_mix_f32 v165, v66, v133, v141 op_sel:[1,0,0] op_sel_hi:[1,0,0]\n"
  "v_mfma_f32_16x16x32_bf16 a[96:99], a[160:163], v[60:63], a[96:99]\n"
  "s_waitcnt lgkmcnt(15)\n"
  "ds_read_b128 v[144:147], v176 offset:1280\n"
  "v_fma_mix_f32 v166, v67, v134, v142 op_sel:[0,0,0] op_sel_hi:[1,0,0]\n"
  "v_mfma_f32_16x16x32_bf16 a[4:7], a[164:167], v[48:51], a[4:7]\n"
  "s_add_u32 m0, %[sM0], 99328\n"
  "ds_read_b64_tr_b16 a[132:133], v204 offset:32776\n"
  "v_fma_mix_f32 v167, v67, v135, v143 op_sel:[1,0,0] op_sel_hi:[1,0,0]\n"
  "v_mfma_f32_16x16x32_bf16 a[36:39], a[164:167], v[52:55], a[36:39]\n"
  "s_add_u32 s61, %[sWoff], 1572992\n"
  "buffer_load_dwordx4 %[vWd], %[srdW], s61 offen lds\n"
  "v_cvt_pk_bf16_f32 v64, v160, v161\n"
  "v_cvt_pk_bf16_f32 v65, v162, v163\n"
  "v_mfma_f32_16x16x32_bf16 a[68:71], a[164:167], v[56:59], a[68:71]\n"
  "ds_read_b64_tr_b16 a[134:135], v204 offset:36872\n"
  "v_cvt_pk_bf16_f32 v66, v164, v165\n"
  "v_mfma_f32_16x16x32_bf16 a[100:103], a[164:167], v[60:63], a[100:103]\n"
  "s_waitcnt lgkmcnt(15)\n"
  "ds_read_b128 v[148:151], v176 offset:1296\n"
  "v_cvt_pk_bf16_f32 v67, v166, v167\n"
  "v_mfma_f32_16x16x32_bf16 a[8:11], a[168:171], v[48:51], a[8:11]\n"
  "s_add_u32 m0, %[sM0], 100352\n"
  "ds_read_b64_tr_b16 a[136:137], v204 offset:33280\n"
  "v_fma_mix_f32 v168, v68, v128, v136 op_sel:[0,0,0] op_sel_hi:[1,0,0]\n"
  "v_fma_mix_f32 v169, v68, v129, v137 op_sel:[1,0,0] op_sel_hi:[1,0,0]\n"
  "v_mfma_f32_16x16x32_bf16 a[40:43], a[168:171], v[52:55], a[40:43]\n"
  "s_add_u32 s60, %[sWoff], 1638400\n"
  "buffer_load_dwordx4 %[vWd], %[srdW], s60 offen lds\n"
  "v_fma_mix_f32 v170, v69, v130, v138 op_sel:[0,0,0] op_sel_hi:[1,0,0]\n"
  "v_mfma_f32_16x16x32_bf16 a[72:75], a[168:171], v[56:59], a[72:75]\n"
  "ds_read_b64_tr_b16 a[138:139], v204 offset:37376\n"
  "v_fma_mix_f32 v171, v69, v131, v139 op_sel:[1,0,0] op_sel_hi:[1,0,0]\n"
  "v_mfma_f32_16x16x32_bf16 a[104:107], a[168:171], v[60:63], a[104:107]\n"
  "s_waitcnt lgkmcnt(15)\n"
  "ds_read_b128 v[152:155], v176 offset:1312\n"
  "v_fma_mix_f32 v172, v70, v132, v140 op_sel:[0,0,0] op_sel_hi:[1,0,0]\n"
  "v_fma_mix_f32 v173, v70, v133, v141 op_sel:[1,0,0] op_sel_hi:[1,0,0]\n"
  "v_mfma_f32_16x16x32_bf16 a[12:15], a[172:175], v[48:51], a[12:15]\n"
  "s_add_u32 m0, %[sM0], 101376\n"
  "ds_read_b64_tr_b16 a[140:141], v204 offset:33288\n"
  "v_fma_mix_f32 v174, v71, v134, v142 op_sel:[0,0,0] op_sel_hi:[1,0,0]\n"
  "v_mfma_f32_16x16x32_bf16 a[44:47], a[172:175], v[52:55], a[44:47]\n"
  "s_add_u32 s61, %[sWoff], 1638528\n"
  "buffer_load_dwordx4 %[vWd], %[srdW], s61 offen lds\n"
  "v_fma_mix_f32 v175, v71, v135, v143 op_sel:[1,0,0] op_sel_hi:[1,0,0]\n"
  "v_mfma_f32_16x16x32_bf16 a[76:79], a[172:175], v[56:59], a[76:79]\n"
  "ds_read_b64_tr_b16 a[142:143], v204 offset:37384\n"
  "v_cvt_pk_bf16_f32 v68, v168, v169\n"
  "v_cvt_pk_bf16_f32 v69, v170, v171\n"
  "v_mfma_f32_16x16x32_bf16 a[108:111], a[172:175], v[60:63], a[108:111]\n"
  "s_waitcnt lgkmcnt(15)\n"
  "ds_read_b128 v[156:159], v176 offset:1328\n"
  "v_cvt_pk_bf16_f32 v70, v172, v173\n"
  "v_mfma_f32_16x16x32_bf16 a[16:19], a[176:179], v[48:51], a[16:19]\n"
  "ds_read_b64_tr_b16 a[144:145], v204 offset:33792\n"
  "v_cvt_pk_bf16_f32 v71, v174, v175\n"
  "v_mfma_f32_16x16x32_bf16 a[48:51], a[176:179], v[52:55], a[48:51]\n"
  "s_add_u32 s66, %[sHoff], 6144\n"
  "buffer_load_dwordx4 v[96:99], %[vL16], %[srdH], s66 offen\n"
  "v_fma_mix_f32 v160, v72, v128, v136 op_sel:[0,0,0] op_sel_hi:[1,0,0]\n"
  "v_fma_mix_f32 v161, v72, v129, v137 op_sel:[1,0,0] op_sel_hi:[1,0,0]\n"
  "v_mfma_f32_16x16x32_bf16 a[80:83], a[176:179], v[56:59], a[80:83]\n"
  "ds_read_b64_tr_b16 a[146:147], v204 offset:37888\n"
  "v_fma_mix_f32 v162, v73, v130, v138 op_sel:[0,0,0] op_sel_hi:[1,0,0]\n"
  "v_mfma_f32_16x16x32_bf16 a[112:115], a[176:179], v[60:63], a[112:115]\n"
  "s_waitcnt lgkmcnt(15)\n"
  "v_fma_mix_f32 v163, v73, v131, v139 op_sel:[1,0,0] op_sel_hi:[1,0,0]\n"
  "v_fma_mix_f32 v164, v74, v132, v140 op_sel:[0,0,0] op_sel_hi:[1,0,0]\n"
  "v_mfma_f32_16x16x32_bf16 a[20:23], a[180:183], v[48:51], a[20:23]\n"
  "ds_read_b64_tr_b16 a[148:149], v204 offset:33800\n"
  "v_fma_mix_f32 v165, v74, v133, v141 op_sel:[1,0,0] op_sel_hi:[1,0,0]\n"
  "v_fma_mix_f32 v166, v75, v134, v142 op_sel:[0,0,0] op_sel_hi:[1,0,0]\n"
  "v_mfma_f32_16x16x32_bf16 a[52:55], a[180:183], v[52:55], a[52:55]\n"
  "s_add_u32 s67, %[sHoff], 22528\n"
  "buffer_load_dwordx4 v[100:103], %[vL16], %[srdH], s67 offen\n"
  "v_fma_mix_f32 v167, v75, v135, v143 op_sel:[1,0,0] op_sel_hi:[1,0,0]\n"
  "v_mfma_f32_16x16x32_bf16 a[84:87], a[180:183], v[56:59], a[84:87]\n"
  "ds_read_b64_tr_b16 a[150:151], v204 offset:37896\n"
  "v_cvt_pk_bf16_f32 v72, v160, v161\n"
  "v_mfma_f32_16x16x32_bf16 a[116:119], a[180:183], v[60:63], a[116:119]\n"
  "s_waitcnt lgkmcnt(15)\n"
  "v_cvt_pk_bf16_f32 v73, v162, v163\n"
  "v_cvt_pk_bf16_f32 v74, v164, v165\n"
  "v_cvt_pk_bf16_f32 v75, v166, v167\n"
  "v_mfma_f32_16x16x32_bf16 a[24:27], a[184:187], v[48:51], a[24:27]\n"
  "ds_read_b64_tr_b16 a[152:153], v204 offset:34304\n"
  "v_fma_mix_f32 v168, v76, v128, v136 op_sel:[0,0,0] op_sel_hi:[1,0,0]\n"
  "v_mfma_f32_16x16x32_bf16 a[56:59], a[184:187], v[52:55], a[56:59]\n"
  "s_add_u32 s66, %[sHoff], 38912\n"
  "buffer_load_dwordx4 v[104:107], %[vL16], %[srdH], s66 offen\n"
  "v_fma_mix_f32 v169, v76, v129, v137 op_sel:[1,0,0] op_sel_hi:[1,0,0]\n"
  "v_mfma_f32_16x16x32_bf16 a[88:91], a[184:187], v[56:59], a[88:91]\n"
  "ds_read_b64_tr_b16 a[154:155], v204 offset:38400\n"
  "v_fma_mix_f32 v170, v77, v130, v138 op_sel:[0,0,0] op_sel_hi:[1,0,0]\n"
  "v_fma_mix_f32 v171, v77, v131, v139 op_sel:[1,0,0] op_sel_hi:[1,0,0]\n"
  "v_mfma_f32_16x16x32_bf16 a[120:123], a[184:187], v[60:63], a[120:123]\n"
  "s_waitcnt lgkmcnt(15)\n"
  "v_fma_mix_f32 v172, v78, v132, v140 op_sel:[0,0,0] op_sel_hi:[1,0,0]\n"
  "v_fma_mix_f32 v173, v78, v133, v141 op_sel:[1,0,0] op_sel_hi:[1,0,0]\n"
  "v_mfma_f32_16x16x32_bf16 a[28:31], a[188:191], v[48:51], a[28:31]\n"
  "ds_read_b64_tr_b16 a[156:157], v204 offset:34312\n"
  "v_fma_mix_f32 v174, v79, v134, v142 op_sel:[0,0,0] op_sel_hi:[1,0,0]\n"
  "v_mfma_f32_16x16x32_bf16 a[60:63], a[188:191], v[52:55], a[60:63]\n"
  "s_add_u32 s67, %[sHoff], 55296\n"
  "buffer_load_dwordx4 v[108:111], %[vL16], %[srdH], s67 offen\n"
  "v_fma_mix_f32 v175, v79, v135, v143 op_sel:[1,0,0] op_sel_hi:[1,0,0]\n"
  "v_cvt_pk_bf16_f32 v76, v168, v169\n"
  "v_mfma_f32_16x16x32_bf16 a[92:95], a[188:191], v[56:59], a[92:95]\n"
  "ds_read_b64_tr_b16 a[158:159], v204 offset:38408\n"
  "v_cvt_pk_bf16_f32 v77, v170, v171\n"
  "v_mfma_f32_16x16x32_bf16 a[124:127], a[188:191], v[60:63], a[124:127]\n"
  "s_waitcnt lgkmcnt(15)\n"
  "v_cvt_pk_bf16_f32 v78, v172, v173\n"
  "v_cvt_pk_bf16_f32 v79, v174, v175\n"
  "v_mfma_f32_16x16x32_bf16 a[0:3], a[128:131], v[64:67], a[0:3]\n"
  "s_waitcnt vmcnt(8)\n"
  "s_waitcnt lgkmcnt(8)\n"
  "ds_read_b64_tr_b16 a[160:161], v204 offset:40960\n"
  "v_fma_mix_f32 v160, v80, v144, v152 op_sel:[0,0,0] op_sel_hi:[1,0,0]\n"
  "v_fma_mix_f32 v161, v80, v145, v153 op_sel:[1,0,0] op_sel_hi:[1,0,0]\n"
  "v_mfma_f32_16x16x32_bf16 a[32:35], a[128:131], v[68:71], a[32:35]\n"
  "v_fma_mix_f32 v162, v81, v146, v154 op_sel:[0,0,0] op_sel_hi:[1,0,0]\n"
  "v_fma_mix_f32 v163, v81, v147, v155 op_sel:[1,0,0] op_sel_hi:[1,0,0]\n"
  "v_mfma_f32_16x16x32_bf16 a[64:67], a[128:131], v[72:75], a[64:67]\n"
  "ds_read_b64_tr_b16 a[162:163], v204 offset:45056\n"
  "v_fma_mix_f32 v164, v82, v148, v156 op_sel:[0,0,0] op_sel_hi:[1,0,0]\n"
  "v_fma_mix_f32 v165, v82, v149, v157 op_sel:[1,0,0] op_sel_hi:[1,0,0]\n"
  "v_mfma_f32_16x16x32_bf16 a[96:99], a[128:131], v[76:79], a[96:99]\n"
  "s_waitcnt lgkmcnt(15)\n"
  "ds_read_b128 v[128:131], v176 offset:1536\n"
  "v_fma_mix_f32 v166, v83, v150, v158 op_sel:[0,0,0] op_sel_hi:[1,0,0]\n"
  "v_mfma_f32_16x16x32_bf16 a[4:7], a[132:135], v[64:67], a[4:7]\n"
  "ds_read_b64_tr_b16 a[164:165], v204 offset:40968\n"
  "v_fma_mix_f32 v167, v83, v151, v159 op_sel:[1,0,0] op_sel_hi:[1,0,0]\n"
  "v_mfma_f32_16x16x32_bf16 a[36:39], a[132:135], v[68:71], a[36:39]\n"
  "s_add_u32 s66, %[sHoff], 7168\n"
  "buffer_load_dwordx4 v[112:115], %[vL16], %[srdH], s66 offen\n"
  "v_cvt_pk_bf16_f32 v80, v160, v161\n"
  "v_cvt_pk_bf16_f32 v81, v162, v163\n"
  "v_mfma_f32_16x16x32_bf16 a[68:71], a[132:135], v[72:75], a[68:71]\n"
  "ds_read_b64_tr_b16 a[166:167], v204 offset:45064\n"
  "v_cvt_pk_bf16_f32 v82, v164, v165\n"
  "v_mfma_f32_16x16x32_bf16 a[100:103], a[132:135], v[76:79], a[100:103]\n"
  "s_waitcnt lgkmcnt(15)\n"
  "ds_read_b128 v[132:135], v176 offset:1552\n"
  "v_cvt_pk_bf16_f32 v83, v166, v167\n"
  "v_mfma_f32_16x16x32_bf16 a[8:11], a[136:139], v[64:67], a[8:11]\n"
  "ds_read_b64_tr_b16 a[168:169], v204 offset:41472\n"
  "v_fma_mix_f32 v168, v84, v144, v152 op_sel:[0,0,0] op_sel_hi:[1,0,0]\n"
  "v_fma_mix_f32 v169, v84, v145, v153 op_sel:[1,0,0] op_sel_hi:[1,0,0]\n"
  "v_mfma_f32_16x16x32_bf16 a[40:43], a[136:139], v[68:71], a[40:43]\n"
  "s_add_u32 s67, %[sHoff], 23552\n"
  "buffer_load_dwordx4 v[116:119], %[vL16], %[srdH], s67 offen\n"
  "v_fma_mix_f32 v170, v85, v146, v154 op_sel:[0,0,0] op_sel_hi:[1,0,0]\n"
  "v_mfma_f32_16x16x32_bf16 a[72:75], a[136:139], v[72:75], a[72:75]\n"
  "ds_read_b64_tr_b16 a[170:171], v204 offset:45568\n"
  "v_fma_mix_f32 v171, v85, v147, v155 op_sel:[1,0,0] op_sel_hi:[1,0,0]\n"
  "v_mfma_f32_16x16x32_bf16 a[104:107], a[136:139], v[76:79], a[104:107]\n"
  "s_waitcnt lgkmcnt(15)\n"
  "ds_read_b128 v[136:139], v176 offset:1568\n"
  "v_fma_mix_f32 v172, v86, v148, v156 op_sel:[0,0,0] op_sel_hi:[1,0,0]\n"
  "v_fma_mix_f32 v173, v86, v149, v157 op_sel:[1,0,0] op_sel_hi:[1,0,0]\n"
  "v_mfma_f32_16x16x32_bf16 a[12:15], a[140:143], v[64:67], a[12:15]\n"
  "ds_read_b64_tr_b16 a[172:173], v204 offset:41480\n"
  "v_fma_mix_f32 v174, v87, v150, v158 op_sel:[0,0,0] op_sel_hi:[1,0,0]\n"
  "v_mfma_f32_16x16x32_bf16 a[44:47], a[140:143], v[68:71], a[44:47]\n"
  "s_add_u32 s66, %[sHoff], 39936\n"
  "buffer_load_dwordx4 v[120:123], %[vL16], %[srdH], s66 offen\n"
  "v_fma_mix_f32 v175, v87, v151, v159 op_sel:[1,0,0] op_sel_hi:[1,0,0]\n"
  "v_mfma_f32_16x16x32_bf16 a[76:79], a[140:143], v[72:75], a[76:79]\n"
  "ds_read_b64_tr_b16 a[174:175], v204 offset:45576\n"
  "v_cvt_pk_bf16_f32 v84, v168, v169\n"
  "v_cvt_pk_bf16_f32 v85, v170, v171\n"
  "v_mfma_f32_16x16x32_bf16 a[108:111], a[140:143], v[76:79], a[108:111]\n"
  "s_waitcnt lgkmcnt(15)\n"
  "ds_read_b128 v[140:143], v176 offset:1584\n"
  "v_cvt_pk_bf16_f32 v86, v172, v173\n"
  "v_mfma_f32_16x16x32_bf16 a[16:19], a[144:147], v[64:67], a[16:19]\n"
  "ds_read_b64_tr_b16 a[176:177], v204 offset:41984\n"
  "v_cvt_pk_bf16_f32 v87, v174, v175\n"
  "v_mfma_f32_16x16x32_bf16 a[48:51], a[144:147], v[68:71], a[48:51]\n"
  "s_add_u32 s67, %[sHoff], 56320\n"
  "buffer_load_dwordx4 v[124:127], %[vL16], %[srdH], s67 offen\n"
  "v_fma_mix_f32 v160, v88, v144, v152 op_sel:[0,0,0] op_sel_hi:[1,0,0]\n"
  "v_fma_mix_f32 v161, v88, v145, v153 op_sel:[1,0,0] op_sel_hi:[1,0,0]\n"
  "v_mfma_f32_16x16x32_bf16 a[80:83], a[144:147], v[72:75], a[80:83]\n"
  "ds_read_b64_tr_b16 a[178:179], v204 offset:46080\n"
  "v_fma_mix_f32 v162, v89, v146, v154 op_sel:[0,0,0] op_sel_hi:[1,0,0]\n"
  "v_mfma_f32_16x16x32_bf16 a[112:115], a[144:147], v[76:79], a[112:115]\n"
  "s_waitcnt lgkmcnt(15)\n"
  "v_fma_mix_f32 v163, v89, v147, v155 op_sel:[1,0,0] op_sel_hi:[1,0,0]\n"
  "v_fma_mix_f32 v164, v90, v148, v156 op_sel:[0,0,0] op_sel_hi:[1,0,0]\n"
  "v_mfma_f32_16x16x32_bf16 a[20:23], a[148:151], v[64:67], a[20:23]\n"
  "s_add_u32 m0, %[sM0], 49152\n"
  "ds_read_b64_tr_b16 a[180:181], v204 offset:41992\n"
  "v_fma_mix_f32 v165, v90, v149, v157 op_sel:[1,0,0] op_sel_hi:[1,0,0]\n"
  "v_fma_mix_f32 v166, v91, v150, v158 op_sel:[0,0,0] op_sel_hi:[1,0,0]\n"
  "v_mfma_f32_16x16x32_bf16 a[52:55], a[148:151], v[68:71], a[52:55]\n"
  "s_add_u32 s60, %[sWoff], 2097152\n"
  "buffer_load_dwordx4 %[vWd], %[srdW], s60 offen lds\n"
  "v_fma_mix_f32 v167, v91, v151, v159 op_sel:[1,0,0] op_sel_hi:[1,0,0]\n"
  "v_mfma_f32_16x16x32_bf16 a[84:87], a[148:151], v[72:75], a[84:87]\n"
  "ds_read_b64_tr_b16 a[182:183], v204 offset:46088\n"
  "v_cvt_pk_bf16_f32 v88, v160, v161\n"
  "v_mfma_f32_16x16x32_bf16 a[116:119], a[148:151], v[76:79], a[116:119]\n"
  "s_waitcnt lgkmcnt(15)\n"
  "v_cvt_pk_bf16_f32 v89, v162, v163\n"
  "v_cvt_pk_bf16_f32 v90, v164, v165\n"
  "v_cvt_pk_bf16_f32 v91, v166, v167\n"
  "v_mfma_f32_16x16x32_bf16 a[24:27], a[152:155], v[64:67], a[24:27]\n"
  "s_add_u32 m0, %[sM0], 50176\n"
  "ds_read_b64_tr_b16 a[184:185], v204 offset:42496\n"
  "v_fma_mix_f32 v168, v92, v144, v152 op_sel:[0,0,0] op_sel_hi:[1,0,0]\n"
  "v_mfma_f32_16x16x32_bf16 a[56:59], a[152:155], v[68:71], a[56:59]\n"
  "s_add_u32 s61, %[sWoff], 2097280\n"
  "buffer_load_dwordx4 %[vWd], %[srdW], s61 offen lds\n"
  "v_fma_mix_f32 v169, v92, v145, v153 op_sel:[1,0,0] op_sel_hi:[1,0,0]\n"
  "v_mfma_f32_16x16x32_bf16 a[88:91], a[152:155], v[72:75], a[88:91]\n"
  "ds_read_b64_tr_b16 a[186:187], v204 offset:46592\n"
  "v_fma_mix_f32 v170, v93, v146, v154 op_sel:[0,0,0] op_sel_hi:[1,0,0]\n"
  "v_fma_mix_f32 v171, v93, v147, v155 op_sel:[1,0,0] op_sel_hi:[1,0,0]\n"
  "v_mfma_f32_16x16x32_bf16 a[120:123], a[152:155], v[76:79], a[120:123]\n"
  "s_waitcnt lgkmcnt(15)\n"
  "v_fma_mix_f32 v172, v94, v148, v156 op_sel:[0,0,0] op_sel_hi:[1,0,0]\n"
  "v_fma_mix_f32 v173, v94, v149, v157 op_sel:[1,0,0] op_sel_hi:[1,0,0]\n"
  "v_mfma_f32_16x16x32_bf16 a[28:31], a[156:159], v[64:67], a[28:31]\n"
  "s_add_u32 m0, %[sM0], 51200\n"
  "ds_read_b64_tr_b16 a[188:189], v204 offset:42504\n"
  "v_fma_mix_f32 v174, v95, v150, v158 op_sel:[0,0,0] op_sel_hi:[1,0,0]\n"
  "v_mfma_f32_16x16x32_bf16 a[60:63], a[156:159], v[68:71], a[60:63]\n"
  "s_add_u32 s60, %[sWoff], 2162688\n"
  "buffer_load_dwordx4 %[vWd], %[srdW], s60 offen lds\n"
  "v_fma_mix_f32 v175, v95, v151, v159 op_sel:[1,0,0] op_sel_hi:[1,0,0]\n"
  "v_cvt_pk_bf16_f32 v92, v168, v169\n"
  "v_mfma_f32_16x16x32_bf16 a[92:95], a[156:159], v[72:75], a[92:95]\n"
  "ds_read_b64_tr_b16 a[190:191], v204 offset:46600\n"
  "v_cvt_pk_bf16_f32 v93, v170, v171\n"
  "v_mfma_f32_16x16x32_bf16 a[124:127], a[156:159], v[76:79], a[124:127]\n"
  "s_waitcnt lgkmcnt(15)\n"
  "v_cvt_pk_bf16_f32 v94, v172, v173\n"
  "v_cvt_pk_bf16_f32 v95, v174, v175\n"
  "s_waitcnt vmcnt(11)\n"
  "s_barrier\n"
  "v_mfma_f32_16x16x32_bf16 a[0:3], a[160:163], v[80:83], a[0:3]\n"
  "s_waitcnt vmcnt(7)\n"
  "s_waitcnt lgkmcnt(8)\n"
  "ds_read_b64_tr_b16 a[128:129], v205 offset:32768\n"
  "v_fma_mix_f32 v160, v96, v128, v136 op_sel:[0,0,0] op_sel_hi:[1,0,0]\n"
  "v_fma_mix_f32 v161, v96, v129, v137 op_sel:[1,0,0] op_sel_hi:[1,0,0]\n"
  "v_mfma_f32_16x16x32_bf16 a[32:35], a[160:163], v[84:87], a[32:35]\n"
  "v_fma_mix_f32 v162, v97, v130, v138 op_sel:[0,0,0] op_sel_hi:[1,0,0]\n"
  "v_fma_mix_f32 v163, v97, v131, v139 op_sel:[1,0,0] op_sel_hi:[1,0,0]\n"
  "v_mfma_f32_16x16x32_bf16 a[64:67], a[160:163], v[88:91], a[64:67]\n"
  "ds_read_b64_tr_b16 a[130:131], v205 offset:36864\n"
  "v_fma_mix_f32 v164, v98, v132, v140 op_sel:[0,0,0] op_sel_hi:[1,0,0]\n"
  "v_fma_mix_f32 v165, v98, v133, v141 op_sel:[1,0,0] op_sel_hi:[1,0,0]\n"
  "v_mfma_f32_16x16x32_bf16 a[96:99], a[160:163], v[92:95], a[96:99]\n"
  "s_waitcnt lgkmcnt(15)\n"
  "ds_read_b128 v[144:147], v176 offset:1792\n"
  "v_fma_mix_f32 v166, v99, v134, v142 op_sel:[0,0,0] op_sel_hi:[1,0,0]\n"
  "v_mfma_f32_16x16x32_bf16 a[4:7], a[164:167], v[80:83], a[4:7]\n"
  "s_add_u32 m0, %[sM0], 52224\n"
  "ds_read_b64_tr_b16 a[132:133], v205 offset:32776\n"
  "v_fma_mix_f32 v167, v99, v135, v143 op_sel:[1,0,0] op_sel_hi:[1,0,0]\n"
  "v_mfma_f32_16x16x32_bf16 a[36:39], a[164:167], v[84:87], a[36:39]\n"
  "s_add_u32 s61, %[sWoff], 2162816\n"
  "buffer_load_dwordx4 %[vWd], %[srdW], s61 offen lds\n"
  "v_cvt_pk_bf16_f32 v96, v160, v161\n"
  "v_cvt_pk_bf16_f32 v97, v162, v163\n"
  "v_mfma_f32_16x16x32_bf16 a[68:71], a[164:167], v[88:91], a[68:71]\n"
  "ds_read_b64_tr_b16 a[134:135], v205 offset:36872\n"
  "v_cvt_pk_bf16_f32 v98, v164, v165\n"
  "v_mfma_f32_16x16x32_bf16 a[100:103], a[164:167], v[92:95], a[100:103]\n"
  "s_waitcnt lgkmcnt(15)\n"
  "ds_read_b128 v[148:151], v176 offset:1808\n"
  "v_cvt_pk_bf16_f32 v99, v166, v167\n"
  "v_mfma_f32_16x16x32_bf16 a[8:11], a[168:171], v[80:83], a[8:11]\n"
  "ds_read_b64_tr_b16 a[136:137], v205 offset:33280\n"
  "v_fma_mix_f32 v168, v100, v128, v136 op_sel:[0,0,0] op_sel_hi:[1,0,0]\n"
  "v_fma_mix_f32 v169, v100, v129, v137 op_sel:[1,0,0] op_sel_hi:[1,0,0]\n"
  "v_mfma_f32_16x16x32_bf16 a[40:43], a[168:171], v[84:87], a[40:43]\n"
  "s_add_u32 s66, %[sHoff], 8192\n"
  "buffer_load_dwordx4 v[0:3], %[vL16], %[srdH], s66 offen\n"
  "v_fma_mix_f32 v170, v101, v130, v138 op_sel:[0,0,0] op_sel_hi:[1,0,0]\n"
  "v_mfma_f32_16x16x32_bf16 a[72:75], a[168:171], v[88:91], a[72:75]\n"
  "ds_read_b64_tr_b16 a[138:139], v205 offset:37376\n"
  "v_fma_mix_f32 v171, v101, v131, v139 op_sel:[1,0,0] op_sel_hi:[1,0,0]\n"
  "v_mfma_f32_16x16x32_bf16 a[104:107], a[168:171], v[92:95], a[104:107]\n"
  "s_waitcnt lgkmcnt(15)\n"
  "ds_read_b128 v[152:155], v176 offset:1824\n"
  "v_fma_mix_f32 v172, v102, v132, v140 op_sel:[0,0,0] op_sel_hi:[1,0,0]\n"
  "v_fma_mix_f32 v173, v102, v133, v141 op_sel:[1,0,0] op_sel_hi:[1,0,0]\n"
  "v_mfma_f32_16x16x32_bf16 a[12:15], a[172:175], v[80:83], a[12:15]\n"
  "ds_read_b64_tr_b16 a[140:141], v205 offset:33288\n"
  "v_fma_mix_f32 v174, v103, v134, v142 op_sel:[0,0,0] op_sel_hi:[1,0,0]\n"
  "v_mfma_f32_16x16x32_bf16 a[44:47], a[172:175], v[84:87], a[44:47]\n"
  "s_add_u32 s67, %[sHoff], 24576\n"
  "buffer_load_dwordx4 v[4:7], %[vL16], %[srdH], s67 offen\n"
  "v_fma_mix_f32 v175, v103, v135, v143 op_sel:[1,0,0] op_sel_hi:[1,0,0]\n"
  "v_mfma_f32_16x16x32_bf16 a[76:79], a[172:175], v[88:91], a[76:79]\n"
  "ds_read_b64_tr_b16 a[142:143], v205 offset:37384\n"
  "v_cvt_pk_bf16_f32 v100, v168, v169\n"
  "v_cvt_pk_bf16_f32 v101, v170, v171\n"
  "v_mfma_f32_16x16x32_bf16 a[108:111], a[172:175], v[92:95], a[108:111]\n"
  "s_waitcnt lgkmcnt(15)\n"
  "ds_read_b128 v[156:159], v176 offset:1840\n"
  "v_cvt_pk_bf16_f32 v102, v172, v173\n"
  "v_mfma_f32_16x16x32_bf16 a[16:19], a[176:179], v[80:83], a[16:19]\n"
  "ds_read_b64_tr_b16 a[144:145], v205 offset:33792\n"
  "v_cvt_pk_bf16_f32 v103, v174, v175\n"
  "v_mfma_f32_16x16x32_bf16 a[48:51], a[176:179], v[84:87], a[48:51]\n"
  "s_add_u32 s66, %[sHoff], 40960\n"
  "buffer_load_dwordx4 v[8:11], %[vL16], %[srdH], s66 offen\n"
  "v_fma_mix_f32 v160, v104, v128, v136 op_sel:[0,0,0] op_sel_hi:[1,0,0]\n"
  "v_fma_mix_f32 v161, v104, v129, v137 op_sel:[1,0,0] op_sel_hi:[1,0,0]\n"
  "v_mfma_f32_16x16x32_bf16 a[80:83], a[176:179], v[88:91], a[80:83]\n"
  "ds_read_b64_tr_b16 a[146:147], v205 offset:37888\n"
  "v_fma_mix_f32 v162, v105, v130, v138 op_sel:[0,0,0] op_sel_hi:[1,0,0]\n"
  "v_mfma_f32_16x16x32_bf16 a[112:115], a[176:179], v[92:95], a[112:115]\n"
  "s_waitcnt lgkmcnt(15)\n"
  "v_fma_mix_f32 v163, v105, v131, v139 op_sel:[1,0,0] op_sel_hi:[1,0,0]\n"
  "v_fma_mix_f32 v164, v106, v132, v140 op_sel:[0,0,0] op_sel_hi:[1,0,0]\n"
  "v_mfma_f32_16x16x32_bf16 a[20:23], a[180:183], v[80:83], a[20:23]\n"
  "ds_read_b64_tr_b16 a[148:149], v205 offset:33800\n"
  "v_fma_mix_f32 v165, v106, v133, v141 op_sel:[1,0,0] op_sel_hi:[1,0,0]\n"
  "v_fma_mix_f32 v166, v107, v134, v142 op_sel:[0,0,0] op_sel_hi:[1,0,0]\n"
  "v_mfma_f32_16x16x32_bf16 a[52:55], a[180:183], v[84:87], a[52:55]\n"
  "s_add_u32 s67, %[sHoff], 57344\n"
  "buffer_load_dwordx4 v[12:15], %[vL16], %[srdH], s67 offen\n"
  "v_fma_mix_f32 v167, v107, v135, v143 op_sel:[1,0,0] op_sel_hi:[1,0,0]\n"
  "v_mfma_f32_16x16x32_bf16 a[84:87], a[180:183], v[88:91], a[84:87]\n"
  "ds_read_b64_tr_b16 a[150:151], v205 offset:37896\n"
  "v_cvt_pk_bf16_f32 v104, v160, v161\n"
  "v_mfma_f32_16x16x32_bf16 a[116:119], a[180:183], v[92:95], a[116:119]\n"
  "s_waitcnt lgkmcnt(15)\n"
  "v_cvt_pk_bf16_f32 v105, v162, v163\n"
  "v_cvt_pk_bf16_f32 v106, v164, v165\n"
  "v_cvt_pk_bf16_f32 v107, v166, v167\n"
  "v_mfma_f32_16x16x32_bf16 a[24:27], a[184:187], v[80:83], a[24:27]\n"
  "ds_read_b64_tr_b16 a[152:153], v205 offset:34304\n"
  "v_fma_mix_f32 v168, v108, v128, v136 op_sel:[0,0,0] op_sel_hi:[1,0,0]\n"
  "v_mfma_f32_16x16x32_bf16 a[56:59], a[184:187], v[84:87], a[56:59]\n"
  "s_add_u32 s66, %[sHoff], 9216\n"
  "buffer_load_dwordx4 v[16:19], %[vL16], %[srdH], s66 offen\n"
  "v_fma_mix_f32 v169, v108, v129, v137 op_sel:[1,0,0] op_sel_hi:[1,0,0]\n"
  "v_mfma_f32_16x16x32_bf16 a[88:91], a[184:187], v[88:91], a[88:91]\n"
  "ds_read_b64_tr_b16 a[154:155], v205 offset:38400\n"
  "v_fma_mix_f32 v170, v109, v130, v138 op_sel:[0,0,0] op_sel_hi:[1,0,0]\n"
  "v_fma_mix_f32 v171, v109, v131, v139 op_sel:[1,0,0] op_sel_hi:[1,0,0]\n"
  "v_mfma_f32_16x16x32_bf16 a[120:123], a[184:187], v[92:95], a[120:123]\n"
  "s_waitcnt lgkmcnt(15)\n"
  "v_fma_mix_f32 v172, v110, v132, v140 op_sel:[0,0,0] op_sel_hi:[1,0,0]\n"
  "v_fma_mix_f32 v173, v110, v133, v141 op_sel:[1,0,0] op_sel_hi:[1,0,0]\n"
  "v_mfma_f32_16x16x32_bf16 a[28:31], a[188:191], v[80:83], a[28:31]\n"
  "ds_read_b64_tr_b16 a[156:157], v205 offset:34312\n"
  "v_fma_mix_f32 v174, v111, v134, v142 op_sel:[0,0,0] op_sel_hi:[1,0,0]\n"
  "v_mfma_f32_16x16x32_bf16 a[60:63], a[188:191], v[84:87], a[60:63]\n"
  "s_add_u32 s67, %[sHoff], 25600\n"
  "buffer_load_dwordx4 v[20:23], %[vL16], %[srdH], s67 offen\n"
  "v_fma_mix_f32 v175, v111, v135, v143 op_sel:[1,0,0] op_sel_hi:[1,0,0]\n"
  "v_cvt_pk_bf16_f32 v108, v168, v169\n"
  "v_mfma_f32_16x16x32_bf16 a[92:95], a[188:191], v[88:91], a[92:95]\n"
  "ds_read_b64_tr_b16 a[158:159], v205 offset:38408\n"
  "v_cvt_pk_bf16_f32 v109, v170, v171\n"
  "v_mfma_f32_16x16x32_bf16 a[124:127], a[188:191], v[92:95], a[124:127]\n"
  "s_waitcnt lgkmcnt(15)\n"
  "v_cvt_pk_bf16_f32 v110, v172, v173\n"
  "v_cvt_pk_bf16_f32 v111, v174, v175\n"
  "v_mfma_f32_16x16x32_bf16 a[0:3], a[128:131], v[96:99], a[0:3]\n"
  "s_waitcnt vmcnt(10)\n"
  "s_waitcnt lgkmcnt(8)\n"
  "ds_read_b64_tr_b16 a[160:161], v205 offset:40960\n"
  "v_fma_mix_f32 v160, v112, v144, v152 op_sel:[0,0,0] op_sel_hi:[1,0,0]\n"
  "v_fma_mix_f32 v161, v112, v145, v153 op_sel:[1,0,0] op_sel_hi:[1,0,0]\n"
  "v_mfma_f32_16x16x32_bf16 a[32:35], a[128:131], v[100:103], a[32:35]\n"
  "v_fma_mix_f32 v162, v113, v146, v154 op_sel:[0,0,0] op_sel_hi:[1,0,0]\n"
  "v_fma_mix_f32 v163, v113, v147, v155 op_sel:[1,0,0] op_sel_hi:[1,0,0]\n"
  "v_mfma_f32_16x16x32_bf16 a[64:67], a[128:131], v[104:107], a[64:67]\n"
  "ds_read_b64_tr_b16 a[162:163], v205 offset:45056\n"
  "v_fma_mix_f32 v164, v114, v148, v156 op_sel:[0,0,0] op_sel_hi:[1,0,0]\n"
  "v_fma_mix_f32 v165, v114, v149, v157 op_sel:[1,0,0] op_sel_hi:[1,0,0]\n"
  "v_mfma_f32_16x16x32_bf16 a[96:99], a[128:131], v[108:111], a[96:99]\n"
  "s_waitcnt lgkmcnt(15)\n"
  "ds_read_b128 v[128:131], v176 offset:2048\n"
  "v_fma_mix_f32 v166, v115, v150, v158 op_sel:[0,0,0] op_sel_hi:[1,0,0]\n"
  "v_mfma_f32_16x16x32_bf16 a[4:7], a[132:135], v[96:99], a[4:7]\n"
  "ds_read_b64_tr_b16 a[164:165], v205 offset:40968\n"
  "v_fma_mix_f32 v167, v115, v151, v159 op_sel:[1,0,0] op_sel_hi:[1,0,0]\n"
  "v_mfma_f32_16x16x32_bf16 a[36:39], a[132:135], v[100:103], a[36:39]\n"
  "s_add_u32 s66, %[sHoff], 41984\n"
  "buffer_load_dwordx4 v[24:27], %[vL16], %[srdH], s66 offen\n"
  "v_cvt_pk_bf16_f32 v112, v160, v161\n"
  "v_cvt_pk_bf16_f32 v113, v162, v163\n"
  "v_mfma_f32_16x16x32_bf16 a[68:71], a[132:135], v[104:107], a[68:71]\n"
  "ds_read_b64_tr_b16 a[166:167], v205 offset:45064\n"
  "v_cvt_pk_bf16_f32 v114, v164, v165\n"
  "v_mfma_f32_16x16x32_bf16 a[100:103], a[132:135], v[108:111], a[100:103]\n"
  "s_waitcnt lgkmcnt(15)\n"
  "ds_read_b128 v[132:135], v176 offset:2064\n"
  "v_cvt_pk_bf16_f32 v115, v166, v167\n"
  "v_mfma_f32_16x16x32_bf16 a[8:11], a[136:139], v[96:99], a[8:11]\n"
  "ds_read_b64_tr_b16 a[168:169], v205 offset:41472\n"
  "v_fma_mix_f32 v168, v116, v144, v152 op_sel:[0,0,0] op_sel_hi:[1,0,0]\n"
  "v_fma_mix_f32 v169, v116, v145, v153 op_sel:[1,0,0] op_sel_hi:[1,0,0]\n"
  "v_mfma_f32_16x16x32_bf16 a[40:43], a[136:139], v[100:103], a[40:43]\n"
  "s_add_u32 s67, %[sHoff], 58368\n"
  "buffer_load_dwordx4 v[28:31], %[vL16], %[srdH], s67 offen\n"
  "v_fma_mix_f32 v170, v117, v146, v154 op_sel:[0,0,0] op_sel_hi:[1,0,0]\n"
  "v_mfma_f32_16x16x32_bf16 a[72:75], a[136:139], v[104:107], a[72:75]\n"
  "ds_read_b64_tr_b16 a[170:171], v205 offset:45568\n"
  "v_fma_mix_f32 v171, v117, v147, v155 op_sel:[1,0,0] op_sel_hi:[1,0,0]\n"
  "v_mfma_f32_16x16x32_bf16 a[104:107], a[136:139], v[108:111], a[104:107]\n"
  "s_waitcnt lgkmcnt(15)\n"
  "ds_read_b128 v[136:139], v176 offset:2080\n"
  "v_fma_mix_f32 v172, v118, v148, v156 op_sel:[0,0,0] op_sel_hi:[1,0,0]\n"
  "v_fma_mix_f32 v173, v118, v149, v157 op_sel:[1,0,0] op_sel_hi:[1,0,0]\n"
  "v_mfma_f32_16x16x32_bf16 a[12:15], a[140:143], v[96:99], a[12:15]\n"
  "s_add_u32 m0, %[sM0], 114688\n"
  "ds_read_b64_tr_b16 a[172:173], v205 offset:41480\n"
  "v_fma_mix_f32 v174, v119, v150, v158 op_sel:[0,0,0] op_sel_hi:[1,0,0]\n"
  "v_mfma_f32_16x16x32_bf16 a[44:47], a[140:143], v[100:103], a[44:47]\n"
  "s_add_u32 s60, %[sWoff], 2621440\n"
  "buffer_load_dwordx4 %[vWd], %[srdW], s60 offen lds\n"
  "v_fma_mix_f32 v175, v119, v151, v159 op_sel:[1,0,0] op_sel_hi:[1,0,0]\n"
  "v_mfma_f32_16x16x32_bf16 a[76:79], a[140:143], v[104:107], a[76:79]\n"
  "ds_read_b64_tr_b16 a[174:175], v205 offset:45576\n"
  "v_cvt_pk_bf16_f32 v116, v168, v169\n"
  "v_cvt_pk_bf16_f32 v117, v170, v171\n"
  "v_mfma_f32_16x16x32_bf16 a[108:111], a[140:143], v[108:111], a[108:111]\n"
  "s_waitcnt lgkmcnt(15)\n"
  "ds_read_b128 v[140:143], v176 offset:2096\n"
  "v_cvt_pk_bf16_f32 v118, v172, v173\n"
  "v_mfma_f32_16x16x32_bf16 a[16:19], a[144:147], v[96:99], a[16:19]\n"
  "s_add_u32 m0, %[sM0], 115712\n"
  "ds_read_b64_tr_b16 a[176:177], v205 offset:41984\n"
  "v_cvt_pk_bf16_f32 v119, v174, v175\n"
  "v_mfma_f32_16x16x32_bf16 a[48:51], a[144:147], v[100:103], a[48:51]\n"
  "s_add_u32 s61, %[sWoff], 2621568\n"
  "buffer_load_dwordx4 %[vWd], %[srdW], s61 offen lds\n"
  "v_fma_mix_f32 v160, v120, v144, v152 op_sel:[0,0,0] op_sel_hi:[1,0,0]\n"
  "v_fma_mix_f32 v161, v120, v145, v153 op_sel:[1,0,0] op_sel_hi:[1,0,0]\n"
  "v_mfma_f32_16x16x32_bf16 a[80:83], a[144:147], v[104:107], a[80:83]\n"
  "ds_read_b64_tr_b16 a[178:179], v205 offset:46080\n"
  "v_fma_mix_f32 v162, v121, v146, v154 op_sel:[0,0,0] op_sel_hi:[1,0,0]\n"
  "v_mfma_f32_16x16x32_bf16 a[112:115], a[144:147], v[108:111], a[112:115]\n"
  "s_waitcnt lgkmcnt(15)\n"
  "v_fma_mix_f32 v163, v121, v147, v155 op_sel:[1,0,0] op_sel_hi:[1,0,0]\n"
  "v_fma_mix_f32 v164, v122, v148, v156 op_sel:[0,0,0] op_sel_hi:[1,0,0]\n"
  "v_mfma_f32_16x16x32_bf16 a[20:23], a[148:151], v[96:99], a[20:23]\n"
  "s_add_u32 m0, %[sM0], 116736\n"
  "ds_read_b64_tr_b16 a[180:181], v205 offset:41992\n"
  "v_fma_mix_f32 v165, v122, v149, v157 op_sel:[1,0,0] op_sel_hi:[1,0,0]\n"
  "v_fma_mix_f32 v166, v123, v150, v158 op_sel:[0,0,0] op_sel_hi:[1,0,0]\n"
  "v_mfma_f32_16x16x32_bf16 a[52:55], a[148:151], v[100:103], a[52:55]\n"
  "s_add_u32 s60, %[sWoff], 2686976\n"
  "buffer_load_dwordx4 %[vWd], %[srdW], s60 offen lds\n"
  "v_fma_mix_f32 v167, v123, v151, v159 op_sel:[1,0,0] op_sel_hi:[1,0,0]\n"
  "v_mfma_f32_16x16x32_bf16 a[84:87], a[148:151], v[104:107], a[84:87]\n"
  "ds_read_b64_tr_b16 a[182:183], v205 offset:46088\n"
  "v_cvt_pk_bf16_f32 v120, v160, v161\n"
  "v_mfma_f32_16x16x32_bf16 a[116:119], a[148:151], v[108:111], a[116:119]\n"
  "s_waitcnt lgkmcnt(15)\n"
  "v_cvt_pk_bf16_f32 v121, v162, v163\n"
  "v_cvt_pk_bf16_f32 v122, v164, v165\n"
  "v_cvt_pk_bf16_f32 v123, v166, v167\n"
  "v_mfma_f32_16x16x32_bf16 a[24:27], a[152:155], v[96:99], a[24:27]\n"
  "s_add_u32 m0, %[sM0], 117760\n"
  "ds_read_b64_tr_b16 a[184:185], v205 offset:42496\n"
  "v_fma_mix_f32 v168, v124, v144, v152 op_sel:[0,0,0] op_sel_hi:[1,0,0]\n"
  "v_mfma_f32_16x16x32_bf16 a[56:59], a[152:155], v[100:103], a[56:59]\n"
  "s_add_u32 s61, %[sWoff], 2687104\n"
  "buffer_load_dwordx4 %[vWd], %[srdW], s61 offen lds\n"
  "v_fma_mix_f32 v169, v124, v145, v153 op_sel:[1,0,0] op_sel_hi:[1,0,0]\n"
  "v_mfma_f32_16x16x32_bf16 a[88:91], a[152:155], v[104:107], a[88:91]\n"
  "ds_read_b64_tr_b16 a[186:187], v205 offset:46592\n"
  "v_fma_mix_f32 v170, v125, v146, v154 op_sel:[0,0,0] op_sel_hi:[1,0,0]\n"
  "v_fma_mix_f32 v171, v125, v147, v155 op_sel:[1,0,0] op_sel_hi:[1,0,0]\n"
  "v_mfma_f32_16x16x32_bf16 a[120:123], a[152:155], v[108:111], a[120:123]\n"
  "s_waitcnt lgkmcnt(15)\n"
  "v_fma_mix_f32 v172, v126, v148, v156 op_sel:[0,0,0] op_sel_hi:[1,0,0]\n"
  "v_fma_mix_f32 v173, v126, v149, v157 op_sel:[1,0,0] op_sel_hi:[1,0,0]\n"
  "v_mfma_f32_16x16x32_bf16 a[28:31], a[156:159], v[96:99], a[28:31]\n"
  "ds_read_b64_tr_b16 a[188:189], v205 offset:42504\n"
  "v_fma_mix_f32 v174, v127, v150, v158 op_sel:[0,0,0] op_sel_hi:[1,0,0]\n"
  "v_mfma_f32_16x16x32_bf16 a[60:63], a[156:159], v[100:103], a[60:63]\n"
  "s_add_u32 s66, %[sHoff], 10240\n"
  "buffer_load_dwordx4 v[32:35], %[vL16], %[srdH], s66 offen\n"
  "v_fma_mix_f32 v175, v127, v151, v159 op_sel:[1,0,0] op_sel_hi:[1,0,0]\n"
  "v_cvt_pk_bf16_f32 v124, v168, v169\n"
  "v_mfma_f32_16x16x32_bf16 a[92:95], a[156:159], v[104:107], a[92:95]\n"
  "ds_read_b64_tr_b16 a[190:191], v205 offset:46600\n"
  "v_cvt_pk_bf16_f32 v125, v170, v171\n"
  "v_mfma_f32_16x16x32_bf16 a[124:127], a[156:159], v[108:111], a[124:127]\n"
  "s_waitcnt lgkmcnt(15)\n"
  "v_cvt_pk_bf16_f32 v126, v172, v173\n"
  "v_cvt_pk_bf16_f32 v127, v174, v175\n"
  "s_waitcnt vmcnt(13)\n"
  "s_barrier\n"
  "v_mfma_f32_16x16x32_bf16 a[0:3], a[160:163], v[112:115], a[0:3]\n"
  "s_waitcnt vmcnt(9)\n"
  "s_waitcnt lgkmcnt(8)\n"
  "ds_read_b64_tr_b16 a[128:129], v204 offset:49152\n"
  "v_fma_mix_f32 v160, v0, v128, v136 op_sel:[0,0,0] op_sel_hi:[1,0,0]\n"
  "v_fma_mix_f32 v161, v0, v129, v137 op_sel:[1,0,0] op_sel_hi:[1,0,0]\n"
  "v_mfma_f32_16x16x32_bf16 a[32:35], a[160:163], v[116:119], a[32:35]\n"
  "v_fma_mix_f32 v162, v1, v130, v138 op_sel:[0,0,0] op_sel_hi:[1,0,0]\n"
  "v_fma_mix_f32 v163, v1, v131, v139 op_sel:[1,0,0] op_sel_hi:[1,0,0]\n"
  "v_mfma_f32_16x16x32_bf16 a[64:67], a[160:163], v[120:123], a[64:67]\n"
  "ds_read_b64_tr_b16 a[130:131], v204 offset:53248\n"
  "v_fma_mix_f32 v164, v2, v132, v140 op_sel:[0,0,0] op_sel_hi:[1,0,0]\n"
  "v_fma_mix_f32 v165, v2, v133, v141 op_sel:[1,0,0] op_sel_hi:[1,0,0]\n"
  "v_mfma_f32_16x16x32_bf16 a[96:99], a[160:163], v[124:127], a[96:99]\n"
  "s_waitcnt lgkmcnt(15)\n"
  "ds_read_b128 v[144:147], v176 offset:2304\n"
  "v_fma_mix_f32 v166, v3, v134, v142 op_sel:[0,0,0] op_sel_hi:[1,0,0]\n"
  "v_mfma_f32_16x16x32_bf16 a[4:7], a[164:167], v[112:115], a[4:7]\n"
  "ds_read_b64_tr_b16 a[132:133], v204 offset:49160\n"
  "v_fma_mix_f32 v167, v3, v135, v143 op_sel:[1,0,0] op_sel_hi:[1,0,0]\n"
  "v_mfma_f32_16x16x32_bf16 a[36:39], a[164:167], v[116:119], a[36:39]\n"
  "s_add_u32 s67, %[sHoff], 26624\n"
  "buffer_load_dwordx4 v[36:39], %[vL16], %[srdH], s67 offen\n"
  "v_cvt_pk_bf16_f32 v0, v160, v161\n"
  "v_cvt_pk_bf16_f32 v1, v162, v163\n"
  "v_mfma_f32_16x16x32_bf16 a[68:71], a[164:167], v[120:123], a[68:71]\n"
  "ds_read_b64_tr_b16 a[134:135], v204 offset:53256\n"
  "v_cvt_pk_bf16_f32 v2, v164, v165\n"
  "v_mfma_f32_16x16x32_bf16 a[100:103], a[164:167], v[124:127], a[100:103]\n"
  "s_waitcnt lgkmcnt(15)\n"
  "ds_read_b128 v[148:151], v176 offset:2320\n"
  "v_cvt_pk_bf16_f32 v3, v166, v167\n"
  "v_mfma_f32_16x16x32_bf16 a[8:11], a[168:171], v[112:115], a[8:11]\n"
  "ds_read_b64_tr_b16 a[136:137], v204 offset:49664\n"
  "v_fma_mix_f32 v168, v4, v128, v136 op_sel:[0,0,0] op_sel_hi:[1,0,0]\n"
  "v_fma_mix_f32 v169, v4, v129, v137 op_sel:[1,0,0] op_sel_hi:[1,0,0]\n"
  "v_mfma_f32_16x16x32_bf16 a[40:43], a[168:171], v[116:119], a[40:43]\n"
  "s_add_u32 s66, %[sHoff], 43008\n"
  "buffer_load_dwordx4 v[40:43], %[vL16], %[srdH], s66 offen\n"
  "v_fma_mix_f32 v170, v5, v130, v138 op_sel:[0,0,0] op_sel_hi:[1,0,0]\n"
  "v_mfma_f32_16x16x32_bf16 a[72:75], a[168:171], v[120:123], a[72:75]\n"
  "ds_read_b64_tr_b16 a[138:139], v204 offset:53760\n"
  "v_fma_mix_f32 v171, v5, v131, v139 op_sel:[1,0,0] op_sel_hi:[1,0,0]\n"
  "v_mfma_f32_16x16x32_bf16 a[104:107], a[168:171], v[124:127], a[104:107]\n"
  "s_waitcnt lgkmcnt(15)\n"
  "ds_read_b128 v[152:155], v176 offset:2336\n"
  "v_fma_mix_f32 v172, v6, v132, v140 op_sel:[0,0,0] op_sel_hi:[1,0,0]\n"
  "v_fma_mix_f32 v173, v6, v133, v141 op_sel:[1,0,0] op_sel_hi:[1,0,0]\n"
  "v_mfma_f32_16x16x32_bf16 a[12:15], a[172:175], v[112:115], a[12:15]\n"
  "ds_read_b64_tr_b16 a[140:141], v204 offset:49672\n"
  "v_fma_mix_f32 v174, v7, v134, v142 op_sel:[0,0,0] op_sel_hi:[1,0,0]\n"
  "v_mfma_f32_16x16x32_bf16 a[44:47], a[172:175], v[116:119], a[44:47]\n"
  "s_add_u32 s67, %[sHoff], 59392\n"
  "buffer_load_dwordx4 v[44:47], %[vL16], %[srdH], s67 offen\n"
  "v_fma_mix_f32 v175, v7, v135, v143 op_sel:[1,0,0] op_sel_hi:[1,0,0]\n"
  "v_mfma_f32_16x16x32_bf16 a[76:79], a[172:175], v[120:123], a[76:79]\n"
  "ds_read_b64_tr_b16 a[142:143], v204 offset:53768\n"
  "v_cvt_pk_bf16_f32 v4, v168, v169\n"
  "v_cvt_pk_bf16_f32 v5, v170, v171\n"
  "v_mfma_f32_16x16x32_bf16 a[108:111], a[172:175], v[124:127], a[108:111]\n"
  "s_waitcnt lgkmcnt(15)\n"
  "ds_read_b128 v[156:159], v176 offset:2352\n"
  "v_cvt_pk_bf16_f32 v6, v172, v173\n"
  "v_mfma_f32_16x16x32_bf16 a[16:19], a[176:179], v[112:115], a[16:19]\n"
  "ds_read_b64_tr_b16 a[144:145], v204 offset:50176\n"
  "v_cvt_pk_bf16_f32 v7, v174, v175\n"
  "v_mfma_f32_16x16x32_bf16 a[48:51], a[176:179], v[116:119], a[48:51]\n"
  "s_add_u32 s66, %[sHoff], 11264\n"
  "buffer_load_dwordx4 v[48:51], %[vL16], %[srdH], s66 offen\n"
  "v_fma_mix_f32 v160, v8, v128, v136 op_sel:[0,0,0] op_sel_hi:[1,0,0]\n"
  "v_fma_mix_f32 v161, v8, v129, v137 op_sel:[1,0,0] op_sel_hi:[1,0,0]\n"
  "v_mfma_f32_16x16x32_bf16 a[80:83], a[176:179], v[120:123], a[80:83]\n"
  "ds_read_b64_tr_b16 a[146:147], v204 offset:54272\n"
  "v_fma_mix_f32 v162, v9, v130, v138 op_sel:[0,0,0] op_sel_hi:[1,0,0]\n"
  "v_mfma_f32_16x16x32_bf16 a[112:115], a[176:179], v[124:127], a[112:115]\n"
  "s_waitcnt lgkmcnt(15)\n"
  "v_fma_mix_f32 v163, v9, v131, v139 op_sel:[1,0,0] op_sel_hi:[1,0,0]\n"
  "v_fma_mix_f32 v164, v10, v132, v140 op_sel:[0,0,0] op_sel_hi:[1,0,0]\n"
  "v_mfma_f32_16x16x32_bf16 a[20:23], a[180:183], v[112:115], a[20:23]\n"
  "ds_read_b64_tr_b16 a[148:149], v204 offset:50184\n"
  "v_fma_mix_f32 v165, v10, v133, v141 op_sel:[1,0,0] op_sel_hi:[1,0,0]\n"
  "v_fma_mix_f32 v166, v11, v134, v142 op_sel:[0,0,0] op_sel_hi:[1,0,0]\n"
  "v_mfma_f32_16x16x32_bf16 a[52:55], a[180:183], v[116:119], a[52:55]\n"
  "s_add_u32 s67, %[sHoff], 27648\n"
  "buffer_load_dwordx4 v[52:55], %[vL16], %[srdH], s67 offen\n"
  "v_fma_mix_f32 v167, v11, v135, v143 op_sel:[1,0,0] op_sel_hi:[1,0,0]\n"
  "v_mfma_f32_16x16x32_bf16 a[84:87], a[180:183], v[120:123], a[84:87]\n"
  "ds_read_b64_tr_b16 a[150:151], v204 offset:54280\n"
  "v_cvt_pk_bf16_f32 v8, v160, v161\n"
  "v_mfma_f32_16x16x32_bf16 a[116:119], a[180:183], v[124:127], a[116:119]\n"
  "s_waitcnt lgkmcnt(15)\n"
  "v_cvt_pk_bf16_f32 v9, v162, v163\n"
  "v_cvt_pk_bf16_f32 v10, v164, v165\n"
  "v_cvt_pk_bf16_f32 v11, v166, v167\n"
  "v_mfma_f32_16x16x32_bf16 a[24:27], a[184:187], v[112:115], a[24:27]\n"
  "ds_read_b64_tr_b16 a[152:153], v204 offset:50688\n"
  "v_fma_mix_f32 v168, v12, v128, v136 op_sel:[0,0,0] op_sel_hi:[1,0,0]\n"
  "v_mfma_f32_16x16x32_bf16 a[56:59], a[184:187], v[116:119], a[56:59]\n"
  "s_add_u32 s66, %[sHoff], 44032\n"
  "buffer_load_dwordx4 v[56:59], %[vL16], %[srdH], s66 offen\n"
  "v_fma_mix_f32 v169, v12, v129, v137 op_sel:[1,0,0] op_sel_hi:[1,0,0]\n"
  "v_mfma_f32_16x16x32_bf16 a[88:91], a[184:187], v[120:123], a[88:91]\n"
  "ds_read_b64_tr_b16 a[154:155], v204 offset:54784\n"
  "v_fma_mix_f32 v170, v13, v130, v138 op_sel:[0,0,0] op_sel_hi:[1,0,0]\n"
  "v_fma_mix_f32 v171, v13, v131, v139 op_sel:[1,0,0] op_sel_hi:[1,0,0]\n"
  "v_mfma_f32_16x16x32_bf16 a[120:123], a[184:187], v[124:127], a[120:123]\n"
  "s_waitcnt lgkmcnt(15)\n"
  "v_fma_mix_f32 v172, v14, v132, v140 op_sel:[0,0,0] op_sel_hi:[1,0,0]\n"
  "v_fma_mix_f32 v173, v14, v133, v141 op_sel:[1,0,0] op_sel_hi:[1,0,0]\n"
  "v_mfma_f32_16x16x32_bf16 a[28:31], a[188:191], v[112:115], a[28:31]\n"
  "ds_read_b64_tr_b16 a[156:157], v204 offset:50696\n"
  "v_fma_mix_f32 v174, v15, v134, v142 op_sel:[0,0,0] op_sel_hi:[1,0,0]\n"
  "v_mfma_f32_16x16x32_bf16 a[60:63], a[188:191], v[116:119], a[60:63]\n"
  "s_add_u32 s67, %[sHoff], 60416\n"
  "buffer_load_dwordx4 v[60:63], %[vL16], %[srdH], s67 offen\n"
  "v_fma_mix_f32 v175, v15, v135, v143 op_sel:[1,0,0] op_sel_hi:[1,0,0]\n"
  "v_cvt_pk_bf16_f32 v12, v168, v169\n"
  "v_mfma_f32_16x16x32_bf16 a[92:95], a[188:191], v[120:123], a[92:95]\n"
  "ds_read_b64_tr_b16 a[158:159], v204 offset:54792\n"
  "v_cvt_pk_bf16_f32 v13, v170, v171\n"
  "v_mfma_f32_16x16x32_bf16 a[124:127], a[188:191], v[124:127], a[124:127]\n"
  "s_waitcnt lgkmcnt(15)\n"
  "v_cvt_pk_bf16_f32 v14, v172, v173\n"
  "v_cvt_pk_bf16_f32 v15, v174, v175\n"
  "v_mfma_f32_16x16x32_bf16 a[0:3], a[128:131], v[0:3], a[0:3]\n"
  "s_waitcnt vmcnt(12)\n"
  "s_waitcnt lgkmcnt(8)\n"
  "ds_read_b64_tr_b16 a[160:161], v204 offset:57344\n"
  "v_fma_mix_f32 v160, v16, v144, v152 op_sel:[0,0,0] op_sel_hi:[1,0,0]\n"
  "v_fma_mix_f32 v161, v16, v145, v153 op_sel:[1,0,0] op_sel_hi:[1,0,0]\n"
  "v_mfma_f32_16x16x32_bf16 a[32:35], a[128:131], v[4:7], a[32:35]\n"
  "v_fma_mix_f32 v162, v17, v146, v154 op_sel:[0,0,0] op_sel_hi:[1,0,0]\n"
  "v_fma_mix_f32 v163, v17, v147, v155 op_sel:[1,0,0] op_sel_hi:[1,0,0]\n"
  "v_mfma_f32_16x16x32_bf16 a[64:67], a[128:131], v[8:11], a[64:67]\n"
  "ds_read_b64_tr_b16 a[162:163], v204 offset:61440\n"
  "v_fma_mix_f32 v164, v18, v148, v156 op_sel:[0,0,0] op_sel_hi:[1,0,0]\n"
  "v_fma_mix_f32 v165, v18, v149, v157 op_sel:[1,0,0] op_sel_hi:[1,0,0]\n"
  "v_mfma_f32_16x16x32_bf16 a[96:99], a[128:131], v[12:15], a[96:99]\n"
  "s_waitcnt lgkmcnt(15)\n"
  "ds_read_b128 v[128:131], v176 offset:2560\n"
  "v_fma_mix_f32 v166, v19, v150, v158 op_sel:[0,0,0] op_sel_hi:[1,0,0]\n"
  "v_mfma_f32_16x16x32_bf16 a[4:7], a[132:135], v[0:3], a[4:7]\n"
  "s_add_u32 m0, %[sM0], 131072\n"
  "ds_read_b64_tr_b16 a[164:165], v204 offset:57352\n"
  "v_fma_mix_f32 v167, v19, v151, v159 op_sel:[1,0,0] op_sel_hi:[1,0,0]\n"
  "v_mfma_f32_16x16x32_bf16 a[36:39], a[132:135], v[4:7], a[36:39]\n"
  "s_add_u32 s60, %[sWoff], 3145728\n"
  "buffer_load_dwordx4 %[vWd], %[srdW], s60 offen lds\n"
  "v_cvt_pk_bf16_f32 v16, v160, v161\n"
  "v_cvt_pk_bf16_f32 v17, v162, v163\n"
  "v_mfma_f32_16x16x32_bf16 a[68:71], a[132:135], v[8:11], a[68:71]\n"
  "ds_read_b64_tr_b16 a[166:167], v204 offset:61448\n"
  "v_cvt_pk_bf16_f32 v18, v164, v165\n"
  "v_mfma_f32_16x16x32_bf16 a[100:103], a[132:135], v[12:15], a[100:103]\n"
  "s_waitcnt lgkmcnt(15)\n"
  "ds_read_b128 v[132:135], v176 offset:2576\n"
  "v_cvt_pk_bf16_f32 v19, v166, v167\n"
  "v_mfma_f32_16x16x32_bf16 a[8:11], a[136:139], v[0:3], a[8:11]\n"
  "s_add_u32 m0, %[sM0], 132096\n"
  "ds_read_b64_tr_b16 a[168:169], v204 offset:57856\n"
  "v_fma_mix_f32 v168, v20, v144, v152 op_sel:[0,0,0] op_sel_hi:[1,0,0]\n"
  "v_fma_mix_f32 v169, v20, v145, v153 op_sel:[1,0,0] op_sel_hi:[1,0,0]\n"
  "v_mfma_f32_16x16x32_bf16 a[40:43], a[136:139], v[4:7], a[40:43]\n"
  "s_add_u32 s61, %[sWoff], 3145856\n"
  "buffer_load_dwordx4 %[vWd], %[srdW], s61 offen lds\n"
  "v_fma_mix_f32 v170, v21, v146, v154 op_sel:[0,0,0] op_sel_hi:[1,0,0]\n"
  "v_mfma_f32_16x16x32_bf16 a[72:75], a[136:139], v[8:11], a[72:75]\n"
  "ds_read_b64_tr_b16 a[170:171], v204 offset:61952\n"
  "v_fma_mix_f32 v171, v21, v147, v155 op_sel:[1,0,0] op_sel_hi:[1,0,0]\n"
  "v_mfma_f32_16x16x32_bf16 a[104:107], a[136:139], v[12:15], a[104:107]\n"
  "s_waitcnt lgkmcnt(15)\n"
  "ds_read_b128 v[136:139], v176 offset:2592\n"
  "v_fma_mix_f32 v172, v22, v148, v156 op_sel:[0,0,0] op_sel_hi:[1,0,0]\n"
  "v_fma_mix_f32 v173, v22, v149, v157 op_sel:[1,0,0] op_sel_hi:[1,0,0]\n"
  "v_mfma_f32_16x16x32_bf16 a[12:15], a[140:143], v[0:3], a[12:15]\n"
  "s_add_u32 m0, %[sM0], 133120\n"
  "ds_read_b64_tr_b16 a[172:173], v204 offset:57864\n"
  "v_fma_mix_f32 v174, v23, v150, v158 op_sel:[0,0,0] op_sel_hi:[1,0,0]\n"
  "v_mfma_f32_16x16x32_bf16 a[44:47], a[140:143], v[4:7], a[44:47]\n"
  "s_add_u32 s60, %[sWoff], 3211264\n"
  "buffer_load_dwordx4 %[vWd], %[srdW], s60 offen lds\n"
  "v_fma_mix_f32 v175, v23, v151, v159 op_sel:[1,0,0] op_sel_hi:[1,0,0]\n"
  "v_mfma_f32_16x16x32_bf16 a[76:79], a[140:143], v[8:11], a[76:79]\n"
  "ds_read_b64_tr_b16 a[174:175], v204 offset:61960\n"
  "v_cvt_pk_bf16_f32 v20, v168, v169\n"
  "v_cvt_pk_bf16_f32 v21, v170, v171\n"
  "v_mfma_f32_16x16x32_bf16 a[108:111], a[140:143], v[12:15], a[108:111]\n"
  "s_waitcnt lgkmcnt(15)\n"
  "ds_read_b128 v[140:143], v176 offset:2608\n"
  "v_cvt_pk_bf16_f32 v22, v172, v173\n"
  "v_mfma_f32_16x16x32_bf16 a[16:19], a[144:147], v[0:3], a[16:19]\n"
  "s_add_u32 m0, %[sM0], 134144\n"
  "ds_read_b64_tr_b16 a[176:177], v204 offset:58368\n"
  "v_cvt_pk_bf16_f32 v23, v174, v175\n"
  "v_mfma_f32_16x16x32_bf16 a[48:51], a[144:147], v[4:7], a[48:51]\n"
  "s_add_u32 s61, %[sWoff], 3211392\n"
  "buffer_load_dwordx4 %[vWd], %[srdW], s61 offen lds\n"
  "v_fma_mix_f32 v160, v24, v144, v152 op_sel:[0,0,0] op_sel_hi:[1,0,0]\n"
  "v_fma_mix_f32 v161, v24, v145, v153 op_sel:[1,0,0] op_sel_hi:[1,0,0]\n"
  "v_mfma_f32_16x16x32_bf16 a[80:83], a[144:147], v[8:11], a[80:83]\n"
  "ds_read_b64_tr_b16 a[178:179], v204 offset:62464\n"
  "v_fma_mix_f32 v162, v25, v146, v154 op_sel:[0,0,0] op_sel_hi:[1,0,0]\n"
  "v_mfma_f32_16x16x32_bf16 a[112:115], a[144:147], v[12:15], a[112:115]\n"
  "s_waitcnt lgkmcnt(15)\n"
  "v_fma_mix_f32 v163, v25, v147, v155 op_sel:[1,0,0] op_sel_hi:[1,0,0]\n"
  "v_fma_mix_f32 v164, v26, v148, v156 op_sel:[0,0,0] op_sel_hi:[1,0,0]\n"
  "v_mfma_f32_16x16x32_bf16 a[20:23], a[148:151], v[0:3], a[20:23]\n"
  "ds_read_b64_tr_b16 a[180:181], v204 offset:58376\n"
  "v_fma_mix_f32 v165, v26, v149, v157 op_sel:[1,0,0] op_sel_hi:[1,0,0]\n"
  "v_fma_mix_f32 v166, v27, v150, v158 op_sel:[0,0,0] op_sel_hi:[1,0,0]\n"
  "v_mfma_f32_16x16x32_bf16 a[52:55], a[148:151], v[4:7], a[52:55]\n"
  "s_add_u32 s66, %[sHoff], 12288\n"
  "buffer_load_dwordx4 v[64:67], %[vL16], %[srdH], s66 offen\n"
  "v_fma_mix_f32 v167, v27, v151, v159 op_sel:[1,0,0] op_sel_hi:[1,0,0]\n"
  "v_mfma_f32_16x16x32_bf16 a[84:87], a[148:151], v[8:11], a[84:87]\n"
  "ds_read_b64_tr_b16 a[182:183], v204 offset:62472\n"
  "v_cvt_pk_bf16_f32 v24, v160, v161\n"
  "v_mfma_f32_16x16x32_bf16 a[116:119], a[148:151], v[12:15], a[116:119]\n"
  "s_waitcnt lgkmcnt(15)\n"
  "v_cvt_pk_bf16_f32 v25, v162, v163\n"
  "v_cvt_pk_bf16_f32 v26, v164, v165\n"
  "v_cvt_pk_bf16_f32 v27, v166, v167\n"
  "v_mfma_f32_16x16x32_bf16 a[24:27], a[152:155], v[0:3], a[24:27]\n"
  "ds_read_b64_tr_b16 a[184:185], v204 offset:58880\n"
  "v_fma_mix_f32 v168, v28, v144, v152 op_sel:[0,0,0] op_sel_hi:[1,0,0]\n"
  "v_mfma_f32_16x16x32_bf16 a[56:59], a[152:155], v[4:7], a[56:59]\n"
  "s_add_u32 s67, %[sHoff], 28672\n"
  "buffer_load_dwordx4 v[68:71], %[vL16], %[srdH], s67 offen\n"
  "v_fma_mix_f32 v169, v28, v145, v153 op_sel:[1,0,0] op_sel_hi:[1,0,0]\n"
  "v_mfma_f32_16x16x32_bf16 a[88:91], a[152:155], v[8:11], a[88:91]\n"
  "ds_read_b64_tr_b16 a[186:187], v204 offset:62976\n"
  "v_fma_mix_f32 v170, v29, v146, v154 op_sel:[0,0,0] op_sel_hi:[1,0,0]\n"
  "v_fma_mix_f32 v171, v29, v147, v155 op_sel:[1,0,0] op_sel_hi:[1,0,0]\n"
  "v_mfma_f32_16x16x32_bf16 a[120:123], a[152:155], v[12:15], a[120:123]\n"
  "s_waitcnt lgkmcnt(15)\n"
  "v_fma_mix_f32 v172, v30, v148, v156 op_sel:[0,0,0] op_sel_hi:[1,0,0]\n"
  "v_fma_mix_f32 v173, v30, v149, v157 op_sel:[1,0,0] op_sel_hi:[1,0,0]\n"
  "v_mfma_f32_16x16x32_bf16 a[28:31], a[156:159], v[0:3], a[28:31]\n"
  "ds_read_b64_tr_b16 a[188:189], v204 offset:58888\n"
  "v_fma_mix_f32 v174, v31, v150, v158 op_sel:[0,0,0] op_sel_hi:[1,0,0]\n"
  "v_mfma_f32_16x16x32_bf16 a[60:63], a[156:159], v[4:7], a[60:63]\n"
  "s_add_u32 s66, %[sHoff], 45056\n"
  "buffer_load_dwordx4 v[72:75], %[vL16], %[srdH], s66 offen\n"
  "v_fma_mix_f32 v175, v31, v151, v159 op_sel:[1,0,0] op_sel_hi:[1,0,0]\n"
  "v_cvt_pk_bf16_f32 v28, v168, v169\n"
  "v_mfma_f32_16x16x32_bf16 a[92:95], a[156:159], v[8:11], a[92:95]\n"
  "ds_read_b64_tr_b16 a[190:191], v204 offset:62984\n"
  "v_cvt_pk_bf16_f32 v29, v170, v171\n"
  "v_mfma_f32_16x16x32_bf16 a[124:127], a[156:159], v[12:15], a[124:127]\n"
  "s_waitcnt lgkmcnt(15)\n"
  "v_cvt_pk_bf16_f32 v30, v172, v173\n"
  "v_cvt_pk_bf16_f32 v31, v174, v175\n"
  "s_waitcnt vmcnt(15)\n"
  "s_barrier\n"
  "v_mfma_f32_16x16x32_bf16 a[0:3], a[160:163], v[16:19], a[0:3]\n"
  "s_waitcnt vmcnt(11)\n"
  "s_waitcnt lgkmcnt(8)\n"
  "ds_read_b64_tr_b16 a[128:129], v205 offset:49152\n"
  "v_fma_mix_f32 v160, v32, v128, v136 op_sel:[0,0,0] op_sel_hi:[1,0,0]\n"
  "v_fma_mix_f32 v161, v32, v129, v137 op_sel:[1,0,0] op_sel_hi:[1,0,0]\n"
  "v_mfma_f32_16x16x32_bf16 a[32:35], a[160:163], v[20:23], a[32:35]\n"
  "v_fma_mix_f32 v162, v33, v130, v138 op_sel:[0,0,0] op_sel_hi:[1,0,0]\n"
  "v_fma_mix_f32 v163, v33, v131, v139 op_sel:[1,0,0] op_sel_hi:[1,0,0]\n"
  "v_mfma_f32_16x16x32_bf16 a[64:67], a[160:163], v[24:27], a[64:67]\n"
  "ds_read_b64_tr_b16 a[130:131], v205 offset:53248\n"
  "v_fma_mix_f32 v164, v34, v132, v140 op_sel:[0,0,0] op_sel_hi:[1,0,0]\n"
  "v_fma_mix_f32 v165, v34, v133, v141 op_sel:[1,0,0] op_sel_hi:[1,0,0]\n"
  "v_mfma_f32_16x16x32_bf16 a[96:99], a[160:163], v[28:31], a[96:99]\n"
  "s_waitcnt lgkmcnt(15)\n"
  "ds_read_b128 v[144:147], v176 offset:2816\n"
  "v_fma_mix_f32 v166, v35, v134, v142 op_sel:[0,0,0] op_sel_hi:[1,0,0]\n"
  "v_mfma_f32_16x16x32_bf16 a[4:7], a[164:167], v[16:19], a[4:7]\n"
  "ds_read_b64_tr_b16 a[132:133], v205 offset:49160\n"
  "v_fma_mix_f32 v167, v35, v135, v143 op_sel:[1,0,0] op_sel_hi:[1,0,0]\n"
  "v_mfma_f32_16x16x32_bf16 a[36:39], a[164:167], v[20:23], a[36:39]\n"
  "s_add_u32 s67, %[sHoff], 61440\n"
  "buffer_load_dwordx4 v[76:79], %[vL16], %[srdH], s67 offen\n"
  "v_cvt_pk_bf16_f32 v32, v160, v161\n"
  "v_cvt_pk_bf16_f32 v33, v162, v163\n"
  "v_mfma_f32_16x16x32_bf16 a[68:71], a[164:167], v[24:27], a[68:71]\n"
  "ds_read_b64_tr_b16 a[134:135], v205 offset:53256\n"
  "v_cvt_pk_bf16_f32 v34, v164, v165\n"
  "v_mfma_f32_16x16x32_bf16 a[100:103], a[164:167], v[28:31], a[100:103]\n"
  "s_waitcnt lgkmcnt(15)\n"
  "ds_read_b128 v[148:151], v176 offset:2832\n"
  "v_cvt_pk_bf16_f32 v35, v166, v167\n"
  "v_mfma_f32_16x16x32_bf16 a[8:11], a[168:171], v[16:19], a[8:11]\n"
  "ds_read_b64_tr_b16 a[136:137], v205 offset:49664\n"
  "v_fma_mix_f32 v168, v36, v128, v136 op_sel:[0,0,0] op_sel_hi:[1,0,0]\n"
  "v_fma_mix_f32 v169, v36, v129, v137 op_sel:[1,0,0] op_sel_hi:[1,0,0]\n"
  "v_mfma_f32_16x16x32_bf16 a[40:43], a[168:171], v[20:23], a[40:43]\n"
  "s_add_u32 s66, %[sHoff], 13312\n"
  "buffer_load_dwordx4 v[80:83], %[vL16], %[srdH], s66 offen\n"
  "v_fma_mix_f32 v170, v37, v130, v138 op_sel:[0,0,0] op_sel_hi:[1,0,0]\n"
  "v_mfma_f32_16x16x32_bf16 a[72:75], a[168:171], v[24:27], a[72:75]\n"
  "ds_read_b64_tr_b16 a[138:139], v205 offset:53760\n"
  "v_fma_mix_f32 v171, v37, v131, v139 op_sel:[1,0,0] op_sel_hi:[1,0,0]\n"
  "v_mfma_f32_16x16x32_bf16 a[104:107], a[168:171], v[28:31], a[104:107]\n"
  "s_waitcnt lgkmcnt(15)\n"
  "ds_read_b128 v[152:155], v176 offset:2848\n"
  "v_fma_mix_f32 v172, v38, v132, v140 op_sel:[0,0,0] op_sel_hi:[1,0,0]\n"
  "v_fma_mix_f32 v173, v38, v133, v141 op_sel:[1,0,0] op_sel_hi:[1,0,0]\n"
  "v_mfma_f32_16x16x32_bf16 a[12:15], a[172:175], v[16:19], a[12:15]\n"
  "ds_read_b64_tr_b16 a[140:141], v205 offset:49672\n"
  "v_fma_mix_f32 v174, v39, v134, v142 op_sel:[0,0,0] op_sel_hi:[1,0,0]\n"
  "v_mfma_f32_16x16x32_bf16 a[44:47], a[172:175], v[20:23], a[44:47]\n"
  "s_add_u32 s67, %[sHoff], 29696\n"
  "buffer_load_dwordx4 v[84:87], %[vL16], %[srdH], s67 offen\n"
  "v_fma_mix_f32 v175, v39, v135, v143 op_sel:[1,0,0] op_sel_hi:[1,0,0]\n"
  "v_mfma_f32_16x16x32_bf16 a[76:79], a[172:175], v[24:27], a[76:79]\n"
  "ds_read_b64_tr_b16 a[142:143], v205 offset:53768\n"
  "v_cvt_pk_bf16_f32 v36, v168, v169\n"
  "v_cvt_pk_bf16_f32 v37, v170, v171\n"
  "v_mfma_f32_16x16x32_bf16 a[108:111], a[172:175], v[28:31], a[108:111]\n"
  "s_waitcnt lgkmcnt(15)\n"
  "ds_read_b128 v[156:159], v176 offset:2864\n"
  "v_cvt_pk_bf16_f32 v38, v172, v173\n"
  "v_mfma_f32_16x16x32_bf16 a[16:19], a[176:179], v[16:19], a[16:19]\n"
  "ds_read_b64_tr_b16 a[144:145], v205 offset:50176\n"
  "v_cvt_pk_bf16_f32 v39, v174, v175\n"
  "v_mfma_f32_16x16x32_bf16 a[48:51], a[176:179], v[20:23], a[48:51]\n"
  "s_add_u32 s66, %[sHoff], 46080\n"
  "buffer_load_dwordx4 v[88:91], %[vL16], %[srdH], s66 offen\n"
  "v_fma_mix_f32 v160, v40, v128, v136 op_sel:[0,0,0] op_sel_hi:[1,0,0]\n"
  "v_fma_mix_f32 v161, v40, v129, v137 op_sel:[1,0,0] op_sel_hi:[1,0,0]\n"
  "v_mfma_f32_16x16x32_bf16 a[80:83], a[176:179], v[24:27], a[80:83]\n"
  "ds_read_b64_tr_b16 a[146:147], v205 offset:54272\n"
  "v_fma_mix_f32 v162, v41, v130, v138 op_sel:[0,0,0] op_sel_hi:[1,0,0]\n"
  "v_mfma_f32_16x16x32_bf16 a[112:115], a[176:179], v[28:31], a[112:115]\n"
  "s_waitcnt lgkmcnt(15)\n"
  "v_fma_mix_f32 v163, v41, v131, v139 op_sel:[1,0,0] op_sel_hi:[1,0,0]\n"
  "v_fma_mix_f32 v164, v42, v132, v140 op_sel:[0,0,0] op_sel_hi:[1,0,0]\n"
  "v_mfma_f32_16x16x32_bf16 a[20:23], a[180:183], v[16:19], a[20:23]\n"
  "ds_read_b64_tr_b16 a[148:149], v205 offset:50184\n"
  "v_fma_mix_f32 v165, v42, v133, v141 op_sel:[1,0,0] op_sel_hi:[1,0,0]\n"
  "v_fma_mix_f32 v166, v43, v134, v142 op_sel:[0,0,0] op_sel_hi:[1,0,0]\n"
  "v_mfma_f32_16x16x32_bf16 a[52:55], a[180:183], v[20:23], a[52:55]\n"
  "s_add_u32 s67, %[sHoff], 62464\n"
  "buffer_load_dwordx4 v[92:95], %[vL16], %[srdH], s67 offen\n"
  "v_fma_mix_f32 v167, v43, v135, v143 op_sel:[1,0,0] op_sel_hi:[1,0,0]\n"
  "v_mfma_f32_16x16x32_bf16 a[84:87], a[180:183], v[24:27], a[84:87]\n"
  "ds_read_b64_tr_b16 a[150:151], v205 offset:54280\n"
  "v_cvt_pk_bf16_f32 v40, v160, v161\n"
  "v_mfma_f32_16x16x32_bf16 a[116:119], a[180:183], v[28:31], a[116:119]\n"
  "s_waitcnt lgkmcnt(15)\n"
  "v_cvt_pk_bf16_f32 v41, v162, v163\n"
  "v_cvt_pk_bf16_f32 v42, v164, v165\n"
  "v_cvt_pk_bf16_f32 v43, v166, v167\n"
  "v_mfma_f32_16x16x32_bf16 a[24:27], a[184:187], v[16:19], a[24:27]\n"
  "s_add_u32 m0, %[sM0], 32768\n"
  "ds_read_b64_tr_b16 a[152:153], v205 offset:50688\n"
  "v_fma_mix_f32 v168, v44, v128, v136 op_sel:[0,0,0] op_sel_hi:[1,0,0]\n"
  "v_mfma_f32_16x16x32_bf16 a[56:59], a[184:187], v[20:23], a[56:59]\n"
  "s_add_u32 s60, %[sWoff], 3670016\n"
  "buffer_load_dwordx4 %[vWd], %[srdW], s60 offen lds\n"
  "v_fma_mix_f32 v169, v44, v129, v137 op_sel:[1,0,0] op_sel_hi:[1,0,0]\n"
  "v_mfma_f32_16x16x32_bf16 a[88:91], a[184:187], v[24:27], a[88:91]\n"
  "ds_read_b64_tr_b16 a[154:155], v205 offset:54784\n"
  "v_fma_mix_f32 v170, v45, v130, v138 op_sel:[0,0,0] op_sel_hi:[1,0,0]\n"
  "v_fma_mix_f32 v171, v45, v131, v139 op_sel:[1,0,0] op_sel_hi:[1,0,0]\n"
  "v_mfma_f32_16x16x32_bf16 a[120:123], a[184:187], v[28:31], a[120:123]\n"
  "s_waitcnt lgkmcnt(15)\n"
  "v_fma_mix_f32 v172, v46, v132, v140 op_sel:[0,0,0] op_sel_hi:[1,0,0]\n"
  "v_fma_mix_f32 v173, v46, v133, v141 op_sel:[1,0,0] op_sel_hi:[1,0,0]\n"
  "v_mfma_f32_16x16x32_bf16 a[28:31], a[188:191], v[16:19], a[28:31]\n"
  "s_add_u32 m0, %[sM0], 33792\n"
  "ds_read_b64_tr_b16 a[156:157], v205 offset:50696\n"
  "v_fma_mix_f32 v174, v47, v134, v142 op_sel:[0,0,0] op_sel_hi:[1,0,0]\n"
  "v_mfma_f32_16x16x32_bf16 a[60:63], a[188:191], v[20:23], a[60:63]\n"
  "s_add_u32 s61, %[sWoff], 3670144\n"
  "buffer_load_dwordx4 %[vWd], %[srdW], s61 offen lds\n"
  "v_fma_mix_f32 v175, v47, v135, v143 op_sel:[1,0,0] op_sel_hi:[1,0,0]\n"
  "v_cvt_pk_bf16_f32 v44, v168, v169\n"
  "v_mfma_f32_16x16x32_bf16 a[92:95], a[188:191], v[24:27], a[92:95]\n"
  "ds_read_b64_tr_b16 a[158:159], v205 offset:54792\n"
  "v_cvt_pk_bf16_f32 v45, v170, v171\n"
  "v_mfma_f32_16x16x32_bf16 a[124:127], a[188:191], v[28:31], a[124:127]\n"
  "s_waitcnt lgkmcnt(15)\n"
  "v_cvt_pk_bf16_f32 v46, v172, v173\n"
  "v_cvt_pk_bf16_f32 v47, v174, v175\n"
  "v_mfma_f32_16x16x32_bf16 a[0:3], a[128:131], v[32:35], a[0:3]\n"
  "s_waitcnt vmcnt(14)\n"
  "s_waitcnt lgkmcnt(8)\n"
  "ds_read_b64_tr_b16 a[160:161], v205 offset:57344\n"
  "v_fma_mix_f32 v160, v48, v144, v152 op_sel:[0,0,0] op_sel_hi:[1,0,0]\n"
  "v_fma_mix_f32 v161, v48, v145, v153 op_sel:[1,0,0] op_sel_hi:[1,0,0]\n"
  "v_mfma_f32_16x16x32_bf16 a[32:35], a[128:131], v[36:39], a[32:35]\n"
  "v_fma_mix_f32 v162, v49, v146, v154 op_sel:[0,0,0] op_sel_hi:[1,0,0]\n"
  "v_fma_mix_f32 v163, v49, v147, v155 op_sel:[1,0,0] op_sel_hi:[1,0,0]\n"
  "v_mfma_f32_16x16x32_bf16 a[64:67], a[128:131], v[40:43], a[64:67]\n"
  "ds_read_b64_tr_b16 a[162:163], v205 offset:61440\n"
  "v_fma_mix_f32 v164, v50, v148, v156 op_sel:[0,0,0] op_sel_hi:[1,0,0]\n"
  "v_fma_mix_f32 v165, v50, v149, v157 op_sel:[1,0,0] op_sel_hi:[1,0,0]\n"
  "v_mfma_f32_16x16x32_bf16 a[96:99], a[128:131], v[44:47], a[96:99]\n"
  "s_waitcnt lgkmcnt(15)\n"
  "ds_read_b128 v[128:131], v176 offset:3072\n"
  "v_fma_mix_f32 v166, v51, v150, v158 op_sel:[0,0,0] op_sel_hi:[1,0,0]\n"
  "v_mfma_f32_16x16x32_bf16 a[4:7], a[132:135], v[32:35], a[4:7]\n"
  "s_add_u32 m0, %[sM0], 34816\n"
  "ds_read_b64_tr_b16 a[164:165], v205 offset:57352\n"
  "v_fma_mix_f32 v167, v51, v151, v159 op_sel:[1,0,0] op_sel_hi:[1,0,0]\n"
  "v_mfma_f32_16x16x32_bf16 a[36:39], a[132:135], v[36:39], a[36:39]\n"
  "s_add_u32 s60, %[sWoff], 3735552\n"
  "buffer_load_dwordx4 %[vWd], %[srdW], s60 offen lds\n"
  "v_cvt_pk_bf16_f32 v48, v160, v161\n"
  "v_cvt_pk_bf16_f32 v49, v162, v163\n"
  "v_mfma_f32_16x16x32_bf16 a[68:71], a[132:135], v[40:43], a[68:71]\n"
  "ds_read_b64_tr_b16 a[166:167], v205 offset:61448\n"
  "v_cvt_pk_bf16_f32 v50, v164, v165\n"
  "v_mfma_f32_16x16x32_bf16 a[100:103], a[132:135], v[44:47], a[100:103]\n"
  "s_waitcnt lgkmcnt(15)\n"
  "ds_read_b128 v[132:135], v176 offset:3088\n"
  "v_cvt_pk_bf16_f32 v51, v166, v167\n"
  "v_mfma_f32_16x16x32_bf16 a[8:11], a[136:139], v[32:35], a[8:11]\n"
  "s_add_u32 m0, %[sM0], 35840\n"
  "ds_read_b64_tr_b16 a[168:169], v205 offset:57856\n"
  "v_fma_mix_f32 v168, v52, v144, v152 op_sel:[0,0,0] op_sel_hi:[1,0,0]\n"
  "v_fma_mix_f32 v169, v52, v145, v153 op_sel:[1,0,0] op_sel_hi:[1,0,0]\n"
  "v_mfma_f32_16x16x32_bf16 a[40:43], a[136:139], v[36:39], a[40:43]\n"
  "s_add_u32 s61, %[sWoff], 3735680\n"
  "buffer_load_dwordx4 %[vWd], %[srdW], s61 offen lds\n"
  "v_fma_mix_f32 v170, v53, v146, v154 op_sel:[0,0,0] op_sel_hi:[1,0,0]\n"
  "v_mfma_f32_16x16x32_bf16 a[72:75], a[136:139], v[40:43], a[72:75]\n"
  "ds_read_b64_tr_b16 a[170:171], v205 offset:61952\n"
  "v_fma_mix_f32 v171, v53, v147, v155 op_sel:[1,0,0] op_sel_hi:[1,0,0]\n"
  "v_mfma_f32_16x16x32_bf16 a[104:107], a[136:139], v[44:47], a[104:107]\n"
  "s_waitcnt lgkmcnt(15)\n"
  "ds_read_b128 v[136:139], v176 offset:3104\n"
  "v_fma_mix_f32 v172, v54, v148, v156 op_sel:[0,0,0] op_sel_hi:[1,0,0]\n"
  "v_fma_mix_f32 v173, v54, v149, v157 op_sel:[1,0,0] op_sel_hi:[1,0,0]\n"
  "v_mfma_f32_16x16x32_bf16 a[12:15], a[140:143], v[32:35], a[12:15]\n"
  "ds_read_b64_tr_b16 a[172:173], v205 offset:57864\n"
  "v_fma_mix_f32 v174, v55, v150, v158 op_sel:[0,0,0] op_sel_hi:[1,0,0]\n"
  "v_mfma_f32_16x16x32_bf16 a[44:47], a[140:143], v[36:39], a[44:47]\n"
  "s_add_u32 s66, %[sHoff], 14336\n"
  "buffer_load_dwordx4 v[96:99], %[vL16], %[srdH], s66 offen\n"
  "v_fma_mix_f32 v175, v55, v151, v159 op_sel:[1,0,0] op_sel_hi:[1,0,0]\n"
  "v_mfma_f32_16x16x32_bf16 a[76:79], a[140:143], v[40:43], a[76:79]\n"
  "ds_read_b64_tr_b16 a[174:175], v205 offset:61960\n"
  "v_cvt_pk_bf16_f32 v52, v168, v169\n"
  "v_cvt_pk_bf16_f32 v53, v170, v171\n"
  "v_mfma_f32_16x16x32_bf16 a[108:111], a[140:143], v[44:47], a[108:111]\n"
  "s_waitcnt lgkmcnt(15)\n"
  "ds_read_b128 v[140:143], v176 offset:3120\n"
  "v_cvt_pk_bf16_f32 v54, v172, v173\n"
  "v_mfma_f32_16x16x32_bf16 a[16:19], a[144:147], v[32:35], a[16:19]\n"
  "ds_read_b64_tr_b16 a[176:177], v205 offset:58368\n"
  "v_cvt_pk_bf16_f32 v55, v174, v175\n"
  "v_mfma_f32_16x16x32_bf16 a[48:51], a[144:147], v[36:39], a[48:51]\n"
  "s_add_u32 s67, %[sHoff], 30720\n"
  "buffer_load_dwordx4 v[100:103], %[vL16], %[srdH], s67 offen\n"
  "v_fma_mix_f32 v160, v56, v144, v152 op_sel:[0,0,0] op_sel_hi:[1,0,0]\n"
  "v_fma_mix_f32 v161, v56, v145, v153 op_sel:[1,0,0] op_sel_hi:[1,0,0]\n"
  "v_mfma_f32_16x16x32_bf16 a[80:83], a[144:147], v[40:43], a[80:83]\n"
  "ds_read_b64_tr_b16 a[178:179], v205 offset:62464\n"
  "v_fma_mix_f32 v162, v57, v146, v154 op_sel:[0,0,0] op_sel_hi:[1,0,0]\n"
  "v_mfma_f32_16x16x32_bf16 a[112:115], a[144:147], v[44:47], a[112:115]\n"
  "s_waitcnt lgkmcnt(15)\n"
  "v_fma_mix_f32 v163, v57, v147, v155 op_sel:[1,0,0] op_sel_hi:[1,0,0]\n"
  "v_fma_mix_f32 v164, v58, v148, v156 op_sel:[0,0,0] op_sel_hi:[1,0,0]\n"
  "v_mfma_f32_16x16x32_bf16 a[20:23], a[148:151], v[32:35], a[20:23]\n"
  "ds_read_b64_tr_b16 a[180:181], v205 offset:58376\n"
  "v_fma_mix_f32 v165, v58, v149, v157 op_sel:[1,0,0] op_sel_hi:[1,0,0]\n"
  "v_fma_mix_f32 v166, v59, v150, v158 op_sel:[0,0,0] op_sel_hi:[1,0,0]\n"
  "v_mfma_f32_16x16x32_bf16 a[52:55], a[148:151], v[36:39], a[52:55]\n"
  "s_add_u32 s66, %[sHoff], 47104\n"
  "buffer_load_dwordx4 v[104:107], %[vL16], %[srdH], s66 offen\n"
  "v_fma_mix_f32 v167, v59, v151, v159 op_sel:[1,0,0] op_sel_hi:[1,0,0]\n"
  "v_mfma_f32_16x16x32_bf16 a[84:87], a[148:151], v[40:43], a[84:87]\n"
  "ds_read_b64_tr_b16 a[182:183], v205 offset:62472\n"
  "v_cvt_pk_bf16_f32 v56, v160, v161\n"
  "v_mfma_f32_16x16x32_bf16 a[116:119], a[148:151], v[44:47], a[116:119]\n"
  "s_waitcnt lgkmcnt(15)\n"
  "v_cvt_pk_bf16_f32 v57, v162, v163\n"
  "v_cvt_pk_bf16_f32 v58, v164, v165\n"
  "v_cvt_pk_bf16_f32 v59, v166, v167\n"
  "v_mfma_f32_16x16x32_bf16 a[24:27], a[152:155], v[32:35], a[24:27]\n"
  "ds_read_b64_tr_b16 a[184:185], v205 offset:58880\n"
  "v_fma_mix_f32 v168, v60, v144, v152 op_sel:[0,0,0] op_sel_hi:[1,0,0]\n"
  "v_mfma_f32_16x16x32_bf16 a[56:59], a[152:155], v[36:39], a[56:59]\n"
  "s_add_u32 s67, %[sHoff], 63488\n"
  "buffer_load_dwordx4 v[108:111], %[vL16], %[srdH], s67 offen\n"
  "v_fma_mix_f32 v169, v60, v145, v153 op_sel:[1,0,0] op_sel_hi:[1,0,0]\n"
  "v_mfma_f32_16x16x32_bf16 a[88:91], a[152:155], v[40:43], a[88:91]\n"
  "ds_read_b64_tr_b16 a[186:187], v205 offset:62976\n"
  "v_fma_mix_f32 v170, v61, v146, v154 op_sel:[0,0,0] op_sel_hi:[1,0,0]\n"
  "v_fma_mix_f32 v171, v61, v147, v155 op_sel:[1,0,0] op_sel_hi:[1,0,0]\n"
  "v_mfma_f32_16x16x32_bf16 a[120:123], a[152:155], v[44:47], a[120:123]\n"
  "s_waitcnt lgkmcnt(15)\n"
  "v_fma_mix_f32 v172, v62, v148, v156 op_sel:[0,0,0] op_sel_hi:[1,0,0]\n"
  "v_fma_mix_f32 v173, v62, v149, v157 op_sel:[1,0,0] op_sel_hi:[1,0,0]\n"
  "v_mfma_f32_16x16x32_bf16 a[28:31], a[156:159], v[32:35], a[28:31]\n"
  "ds_read_b64_tr_b16 a[188:189], v205 offset:58888\n"
  "v_fma_mix_f32 v174, v63, v150, v158 op_sel:[0,0,0] op_sel_hi:[1,0,0]\n"
  "v_mfma_f32_16x16x32_bf16 a[60:63], a[156:159], v[36:39], a[60:63]\n"
  "s_add_u32 s66, %[sHoff], 15360\n"
  "buffer_load_dwordx4 v[112:115], %[vL16], %[srdH], s66 offen\n"
  "v_fma_mix_f32 v175, v63, v151, v159 op_sel:[1,0,0] op_sel_hi:[1,0,0]\n"
  "v_cvt_pk_bf16_f32 v60, v168, v169\n"
  "v_mfma_f32_16x16x32_bf16 a[92:95], a[156:159], v[40:43], a[92:95]\n"
  "ds_read_b64_tr_b16 a[190:191], v205 offset:62984\n"
  "v_cvt_pk_bf16_f32 v61, v170, v171\n"
  "v_mfma_f32_16x16x32_bf16 a[124:127], a[156:159], v[44:47], a[124:127]\n"
  "s_waitcnt lgkmcnt(15)\n"
  "v_cvt_pk_bf16_f32 v62, v172, v173\n"
  "v_cvt_pk_bf16_f32 v63, v174, v175\n"
  "s_waitcnt vmcnt(17)\n"
  "s_barrier\n"
  "v_mfma_f32_16x16x32_bf16 a[0:3], a[160:163], v[48:51], a[0:3]\n"
  "s_waitcnt vmcnt(13)\n"
  "s_waitcnt lgkmcnt(8)\n"
  "ds_read_b64_tr_b16 a[128:129], v206 offset:0\n"
  "v_fma_mix_f32 v160, v64, v128, v136 op_sel:[0,0,0] op_sel_hi:[1,0,0]\n"
  "v_fma_mix_f32 v161, v64, v129, v137 op_sel:[1,0,0] op_sel_hi:[1,0,0]\n"
  "v_mfma_f32_16x16x32_bf16 a[32:35], a[160:163], v[52:55], a[32:35]\n"
  "v_fma_mix_f32 v162, v65, v130, v138 op_sel:[0,0,0] op_sel_hi:[1,0,0]\n"
  "v_fma_mix_f32 v163, v65, v131, v139 op_sel:[1,0,0] op_sel_hi:[1,0,0]\n"
  "v_mfma_f32_16x16x32_bf16 a[64:67], a[160:163], v[56:59], a[64:67]\n"
  "ds_read_b64_tr_b16 a[130:131], v206 offset:4096\n"
  "v_fma_mix_f32 v164, v66, v132, v140 op_sel:[0,0,0] op_sel_hi:[1,0,0]\n"
  "v_fma_mix_f32 v165, v66, v133, v141 op_sel:[1,0,0] op_sel_hi:[1,0,0]\n"
  "v_mfma_f32_16x16x32_bf16 a[96:99], a[160:163], v[60:63], a[96:99]\n"
  "s_waitcnt lgkmcnt(15)\n"
  "ds_read_b128 v[144:147], v176 offset:3328\n"
  "v_fma_mix_f32 v166, v67, v134, v142 op_sel:[0,0,0] op_sel_hi:[1,0,0]\n"
  "v_mfma_f32_16x16x32_bf16 a[4:7], a[164:167], v[48:51], a[4:7]\n"
  "ds_read_b64_tr_b16 a[132:133], v206 offset:8\n"
  "v_fma_mix_f32 v167, v67, v135, v143 op_sel:[1,0,0] op_sel_hi:[1,0,0]\n"
  "v_mfma_f32_16x16x32_bf16 a[36:39], a[164:167], v[52:55], a[36:39]\n"
  "s_add_u32 s67, %[sHoff], 31744\n"
  "buffer_load_dwordx4 v[116:119], %[vL16], %[srdH], s67 offen\n"
  "v_cvt_pk_bf16_f32 v64, v160, v161\n"
  "v_cvt_pk_bf16_f32 v65, v162, v163\n"
  "v_mfma_f32_16x16x32_bf16 a[68:71], a[164:167], v[56:59], a[68:71]\n"
  "ds_read_b64_tr_b16 a[134:135], v206 offset:4104\n"
  "v_cvt_pk_bf16_f32 v66, v164, v165\n"
  "v_mfma_f32_16x16x32_bf16 a[100:103], a[164:167], v[60:63], a[100:103]\n"
  "s_waitcnt lgkmcnt(15)\n"
  "ds_read_b128 v[148:151], v176 offset:3344\n"
  "v_cvt_pk_bf16_f32 v67, v166, v167\n"
  "v_mfma_f32_16x16x32_bf16 a[8:11], a[168:171], v[48:51], a[8:11]\n"
  "ds_read_b64_tr_b16 a[136:137], v206 offset:512\n"
  "v_fma_mix_f32 v168, v68, v128, v136 op_sel:[0,0,0] op_sel_hi:[1,0,0]\n"
  "v_fma_mix_f32 v169, v68, v129, v137 op_sel:[1,0,0] op_sel_hi:[1,0,0]\n"
  "v_mfma_f32_16x16x32_bf16 a[40:43], a[168:171], v[52:55], a[40:43]\n"
  "s_add_u32 s66, %[sHoff], 48128\n"
  "buffer_load_dwordx4 v[120:123], %[vL16], %[srdH], s66 offen\n"
  "v_fma_mix_f32 v170, v69, v130, v138 op_sel:[0,0,0] op_sel_hi:[1,0,0]\n"
  "v_mfma_f32_16x16x32_bf16 a[72:75], a[168:171], v[56:59], a[72:75]\n"
  "ds_read_b64_tr_b16 a[138:139], v206 offset:4608\n"
  "v_fma_mix_f32 v171, v69, v131, v139 op_sel:[1,0,0] op_sel_hi:[1,0,0]\n"
  "v_mfma_f32_16x16x32_bf16 a[104:107], a[168:171], v[60:63], a[104:107]\n"
  "s_waitcnt lgkmcnt(15)\n"
  "ds_read_b128 v[152:155], v176 offset:3360\n"
  "v_fma_mix_f32 v172, v70, v132, v140 op_sel:[0,0,0] op_sel_hi:[1,0,0]\n"
  "v_fma_mix_f32 v173, v70, v133, v141 op_sel:[1,0,0] op_sel_hi:[1,0,0]\n"
  "v_mfma_f32_16x16x32_bf16 a[12:15], a[172:175], v[48:51], a[12:15]\n"
  "ds_read_b64_tr_b16 a[140:141], v206 offset:520\n"
  "v_fma_mix_f32 v174, v71, v134, v142 op_sel:[0,0,0] op_sel_hi:[1,0,0]\n"
  "v_mfma_f32_16x16x32_bf16 a[44:47], a[172:175], v[52:55], a[44:47]\n"
  "s_add_u32 s67, %[sHoff], 64512\n"
  "buffer_load_dwordx4 v[124:127], %[vL16], %[srdH], s67 offen\n"
  "v_fma_mix_f32 v175, v71, v135, v143 op_sel:[1,0,0] op_sel_hi:[1,0,0]\n"
  "v_mfma_f32_16x16x32_bf16 a[76:79], a[172:175], v[56:59], a[76:79]\n"
  "ds_read_b64_tr_b16 a[142:143], v206 offset:4616\n"
  "v_cvt_pk_bf16_f32 v68, v168, v169\n"
  "v_cvt_pk_bf16_f32 v69, v170, v171\n"
  "v_mfma_f32_16x16x32_bf16 a[108:111], a[172:175], v[60:63], a[108:111]\n"
  "s_waitcnt lgkmcnt(15)\n"
  "ds_read_b128 v[156:159], v176 offset:3376\n"
  "v_cvt_pk_bf16_f32 v70, v172, v173\n"
  "v_mfma_f32_16x16x32_bf16 a[16:19], a[176:179], v[48:51], a[16:19]\n"
  "s_add_u32 m0, %[sM0], 0\n"
  "ds_read_b64_tr_b16 a[144:145], v206 offset:1024\n"
  "v_cvt_pk_bf16_f32 v71, v174, v175\n"
  "v_mfma_f32_16x16x32_bf16 a[48:51], a[176:179], v[52:55], a[48:51]\n"
  "s_add_u32 s60, %[sKV], 0\n"
  "buffer_load_dwordx4 v198, %[srdK], s60 offen offset:0 lds\n"
  "v_fma_mix_f32 v160, v72, v128, v136 op_sel:[0,0,0] op_sel_hi:[1,0,0]\n"
  "v_fma_mix_f32 v161, v72, v129, v137 op_sel:[1,0,0] op_sel_hi:[1,0,0]\n"
  "v_mfma_f32_16x16x32_bf16 a[80:83], a[176:179], v[56:59], a[80:83]\n"
  "ds_read_b64_tr_b16 a[146:147], v206 offset:5120\n"
  "v_fma_mix_f32 v162, v73, v130, v138 op_sel:[0,0,0] op_sel_hi:[1,0,0]\n"
  "v_mfma_f32_16x16x32_bf16 a[112:115], a[176:179], v[60:63], a[112:115]\n"
  "s_waitcnt lgkmcnt(15)\n"
  "v_fma_mix_f32 v163, v73, v131, v139 op_sel:[1,0,0] op_sel_hi:[1,0,0]\n"
  "v_fma_mix_f32 v164, v74, v132, v140 op_sel:[0,0,0] op_sel_hi:[1,0,0]\n"
  "v_mfma_f32_16x16x32_bf16 a[20:23], a[180:183], v[48:51], a[20:23]\n"
  "ds_read_b64_tr_b16 a[148:149], v206 offset:1032\n"
  "v_fma_mix_f32 v165, v74, v133, v141 op_sel:[1,0,0] op_sel_hi:[1,0,0]\n"
  "v_fma_mix_f32 v166, v75, v134, v142 op_sel:[0,0,0] op_sel_hi:[1,0,0]\n"
  "v_mfma_f32_16x16x32_bf16 a[52:55], a[180:183], v[52:55], a[52:55]\n"
  "buffer_load_dwordx4 v199, %[srdK], s60 offen offset:1024 lds\n"
  "v_fma_mix_f32 v167, v75, v135, v143 op_sel:[1,0,0] op_sel_hi:[1,0,0]\n"
  "v_mfma_f32_16x16x32_bf16 a[84:87], a[180:183], v[56:59], a[84:87]\n"
  "ds_read_b64_tr_b16 a[150:151], v206 offset:5128\n"
  "v_cvt_pk_bf16_f32 v72, v160, v161\n"
  "v_mfma_f32_16x16x32_bf16 a[116:119], a[180:183], v[60:63], a[116:119]\n"
  "s_waitcnt lgkmcnt(15)\n"
  "v_cvt_pk_bf16_f32 v73, v162, v163\n"
  "v_cvt_pk_bf16_f32 v74, v164, v165\n"
  "v_cvt_pk_bf16_f32 v75, v166, v167\n"
  "v_mfma_f32_16x16x32_bf16 a[24:27], a[184:187], v[48:51], a[24:27]\n"
  "ds_read_b64_tr_b16 a[152:153], v206 offset:1536\n"
  "v_fma_mix_f32 v168, v76, v128, v136 op_sel:[0,0,0] op_sel_hi:[1,0,0]\n"
  "v_mfma_f32_16x16x32_bf16 a[56:59], a[184:187], v[52:55], a[56:59]\n"
  "buffer_load_dwordx4 v200, %[srdK], s60 offen offset:2048 lds\n"
  "v_fma_mix_f32 v169, v76, v129, v137 op_sel:[1,0,0] op_sel_hi:[1,0,0]\n"
  "v_mfma_f32_16x16x32_bf16 a[88:91], a[184:187], v[56:59], a[88:91]\n"
  "ds_read_b64_tr_b16 a[154:155], v206 offset:5632\n"
  "v_fma_mix_f32 v170, v77, v130, v138 op_sel:[0,0,0] op_sel_hi:[1,0,0]\n"
  "v_fma_mix_f32 v171, v77, v131, v139 op_sel:[1,0,0] op_sel_hi:[1,0,0]\n"
  "v_mfma_f32_16x16x32_bf16 a[120:123], a[184:187], v[60:63], a[120:123]\n"
  "s_waitcnt lgkmcnt(15)\n"
  "v_fma_mix_f32 v172, v78, v132, v140 op_sel:[0,0,0] op_sel_hi:[1,0,0]\n"
  "v_fma_mix_f32 v173, v78, v133, v141 op_sel:[1,0,0] op_sel_hi:[1,0,0]\n"
  "v_mfma_f32_16x16x32_bf16 a[28:31], a[188:191], v[48:51], a[28:31]\n"
  "ds_read_b64_tr_b16 a[156:157], v206 offset:1544\n"
  "v_fma_mix_f32 v174, v79, v134, v142 op_sel:[0,0,0] op_sel_hi:[1,0,0]\n"
  "v_mfma_f32_16x16x32_bf16 a[60:63], a[188:191], v[52:55], a[60:63]\n"
  "buffer_load_dwordx4 v201, %[srdK], s60 offen offset:3072 lds\n"
  "v_fma_mix_f32 v175, v79, v135, v143 op_sel:[1,0,0] op_sel_hi:[1,0,0]\n"
  "v_cvt_pk_bf16_f32 v76, v168, v169\n"
  "v_mfma_f32_16x16x32_bf16 a[92:95], a[188:191], v[56:59], a[92:95]\n"
  "ds_read_b64_tr_b16 a[158:159], v206 offset:5640\n"
  "v_cvt_pk_bf16_f32 v77, v170, v171\n"
  "v_mfma_f32_16x16x32_bf16 a[124:127], a[188:191], v[60:63], a[124:127]\n"
  "s_waitcnt lgkmcnt(15)\n"
  "v_cvt_pk_bf16_f32 v78, v172, v173\n"
  "v_cvt_pk_bf16_f32 v79, v174, v175\n"
  "v_mfma_f32_16x16x32_bf16 a[0:3], a[128:131], v[64:67], a[0:3]\n"
  "s_waitcnt vmcnt(16)\n"
  "s_waitcnt lgkmcnt(8)\n"
  "ds_read_b64_tr_b16 a[160:161], v206 offset:8192\n"
  "v_fma_mix_f32 v160, v80, v144, v152 op_sel:[0,0,0] op_sel_hi:[1,0,0]\n"
  "v_fma_mix_f32 v161, v80, v145, v153 op_sel:[1,0,0] op_sel_hi:[1,0,0]\n"
  "v_mfma_f32_16x16x32_bf16 a[32:35], a[128:131], v[68:71], a[32:35]\n"
  "v_fma_mix_f32 v162, v81, v146, v154 op_sel:[0,0,0] op_sel_hi:[1,0,0]\n"
  "v_fma_mix_f32 v163, v81, v147, v155 op_sel:[1,0,0] op_sel_hi:[1,0,0]\n"
  "v_mfma_f32_16x16x32_bf16 a[64:67], a[128:131], v[72:75], a[64:67]\n"
  "ds_read_b64_tr_b16 a[162:163], v206 offset:12288\n"
  "v_fma_mix_f32 v164, v82, v148, v156 op_sel:[0,0,0] op_sel_hi:[1,0,0]\n"
  "v_fma_mix_f32 v165, v82, v149, v157 op_sel:[1,0,0] op_sel_hi:[1,0,0]\n"
  "v_mfma_f32_16x16x32_bf16 a[96:99], a[128:131], v[76:79], a[96:99]\n"
  "s_waitcnt lgkmcnt(15)\n"
  "ds_read_b128 v[128:131], v176 offset:3584\n"
  "v_fma_mix_f32 v166, v83, v150, v158 op_sel:[0,0,0] op_sel_hi:[1,0,0]\n"
  "v_mfma_f32_16x16x32_bf16 a[4:7], a[132:135], v[64:67], a[4:7]\n"
  "s_add_u32 m0, %[sM0], 65536\n"
  "ds_read_b64_tr_b16 a[164:165], v206 offset:8200\n"
  "v_fma_mix_f32 v167, v83, v151, v159 op_sel:[1,0,0] op_sel_hi:[1,0,0]\n"
  "v_mfma_f32_16x16x32_bf16 a[36:39], a[132:135], v[68:71], a[36:39]\n"
  "s_add_u32 s60, %[sKV], 0\n"
  "buffer_load_dwordx4 v202, %[srdV], s60 offen offset:0 lds\n"
  "v_cvt_pk_bf16_f32 v80, v160, v161\n"
  "v_cvt_pk_bf16_f32 v81, v162, v163\n"
  "v_mfma_f32_16x16x32_bf16 a[68:71], a[132:135], v[72:75], a[68:71]\n"
  "ds_read_b64_tr_b16 a[166:167], v206 offset:12296\n"
  "v_cvt_pk_bf16_f32 v82, v164, v165\n"
  "v_mfma_f32_16x16x32_bf16 a[100:103], a[132:135], v[76:79], a[100:103]\n"
  "s_waitcnt lgkmcnt(15)\n"
  "ds_read_b128 v[132:135], v176 offset:3600\n"
  "v_cvt_pk_bf16_f32 v83, v166, v167\n"
  "v_mfma_f32_16x16x32_bf16 a[8:11], a[136:139], v[64:67], a[8:11]\n"
  "ds_read_b64_tr_b16 a[168:169], v206 offset:8704\n"
  "v_fma_mix_f32 v168, v84, v144, v152 op_sel:[0,0,0] op_sel_hi:[1,0,0]\n"
  "v_fma_mix_f32 v169, v84, v145, v153 op_sel:[1,0,0] op_sel_hi:[1,0,0]\n"
  "v_mfma_f32_16x16x32_bf16 a[40:43], a[136:139], v[68:71], a[40:43]\n"
  "buffer_load_dwordx4 v203, %[srdV], s60 offen offset:1024 lds\n"
  "v_fma_mix_f32 v170, v85, v146, v154 op_sel:[0,0,0] op_sel_hi:[1,0,0]\n"
  "v_mfma_f32_16x16x32_bf16 a[72:75], a[136:139], v[72:75], a[72:75]\n"
  "ds_read_b64_tr_b16 a[170:171], v206 offset:12800\n"
  "v_fma_mix_f32 v171, v85, v147, v155 op_sel:[1,0,0] op_sel_hi:[1,0,0]\n"
  "v_mfma_f32_16x16x32_bf16 a[104:107], a[136:139], v[76:79], a[104:107]\n"
  "s_waitcnt lgkmcnt(15)\n"
  "ds_read_b128 v[136:139], v176 offset:3616\n"
  "v_fma_mix_f32 v172, v86, v148, v156 op_sel:[0,0,0] op_sel_hi:[1,0,0]\n"
  "v_fma_mix_f32 v173, v86, v149, v157 op_sel:[1,0,0] op_sel_hi:[1,0,0]\n"
  "v_mfma_f32_16x16x32_bf16 a[12:15], a[140:143], v[64:67], a[12:15]\n"
  "ds_read_b64_tr_b16 a[172:173], v206 offset:8712\n"
  "v_fma_mix_f32 v174, v87, v150, v158 op_sel:[0,0,0] op_sel_hi:[1,0,0]\n"
  "v_mfma_f32_16x16x32_bf16 a[44:47], a[140:143], v[68:71], a[44:47]\n"
  "buffer_load_dwordx4 v202, %[srdV], s60 offen offset:2048 lds\n"
  "v_fma_mix_f32 v175, v87, v151, v159 op_sel:[1,0,0] op_sel_hi:[1,0,0]\n"
  "v_mfma_f32_16x16x32_bf16 a[76:79], a[140:143], v[72:75], a[76:79]\n"
  "ds_read_b64_tr_b16 a[174:175], v206 offset:12808\n"
  "v_cvt_pk_bf16_f32 v84, v168, v169\n"
  "v_cvt_pk_bf16_f32 v85, v170, v171\n"
  "v_mfma_f32_16x16x32_bf16 a[108:111], a[140:143], v[76:79], a[108:111]\n"
  "s_waitcnt lgkmcnt(15)\n"
  "ds_read_b128 v[140:143], v176 offset:3632\n"
  "v_cvt_pk_bf16_f32 v86, v172, v173\n"
  "v_mfma_f32_16x16x32_bf16 a[16:19], a[144:147], v[64:67], a[16:19]\n"
  "ds_read_b64_tr_b16 a[176:177], v206 offset:9216\n"
  "v_cvt_pk_bf16_f32 v87, v174, v175\n"
  "v_mfma_f32_16x16x32_bf16 a[48:51], a[144:147], v[68:71], a[48:51]\n"
  "buffer_load_dwordx4 v203, %[srdV], s60 offen offset:3072 lds\n"
  "v_fma_mix_f32 v160, v88, v144, v152 op_sel:[0,0,0] op_sel_hi:[1,0,0]\n"
  "v_fma_mix_f32 v161, v88, v145, v153 op_sel:[1,0,0] op_sel_hi:[1,0,0]\n"
  "v_mfma_f32_16x16x32_bf16 a[80:83], a[144:147], v[72:75], a[80:83]\n"
  "ds_read_b64_tr_b16 a[178:179], v206 offset:13312\n"
  "v_fma_mix_f32 v162, v89, v146, v154 op_sel:[0,0,0] op_sel_hi:[1,0,0]\n"
  "v_mfma_f32_16x16x32_bf16 a[112:115], a[144:147], v[76:79], a[112:115]\n"
  "s_waitcnt lgkmcnt(15)\n"
  "v_fma_mix_f32 v163, v89, v147, v155 op_sel:[1,0,0] op_sel_hi:[1,0,0]\n"
  "v_fma_mix_f32 v164, v90, v148, v156 op_sel:[0,0,0] op_sel_hi:[1,0,0]\n"
  "v_mfma_f32_16x16x32_bf16 a[20:23], a[148:151], v[64:67], a[20:23]\n"
  "s_add_u32 m0, %[sM0], 16384\n"
  "ds_read_b64_tr_b16 a[180:181], v206 offset:9224\n"
  "v_fma_mix_f32 v165, v90, v149, v157 op_sel:[1,0,0] op_sel_hi:[1,0,0]\n"
  "v_fma_mix_f32 v166, v91, v150, v158 op_sel:[0,0,0] op_sel_hi:[1,0,0]\n"
  "v_mfma_f32_16x16x32_bf16 a[52:55], a[148:151], v[68:71], a[52:55]\n"
  "s_add_u32 s60, %[sKV], 16384\n"
  "buffer_load_dwordx4 v198, %[srdK], s60 offen offset:0 lds\n"
  "v_fma_mix_f32 v167, v91, v151, v159 op_sel:[1,0,0] op_sel_hi:[1,0,0]\n"
  "v_mfma_f32_16x16x32_bf16 a[84:87], a[148:151], v[72:75], a[84:87]\n"
  "ds_read_b64_tr_b16 a[182:183], v206 offset:13320\n"
  "v_cvt_pk_bf16_f32 v88, v160, v161\n"
  "v_mfma_f32_16x16x32_bf16 a[116:119], a[148:151], v[76:79], a[116:119]\n"
  "s_waitcnt lgkmcnt(15)\n"
  "v_cvt_pk_bf16_f32 v89, v162, v163\n"
  "v_cvt_pk_bf16_f32 v90, v164, v165\n"
  "v_cvt_pk_bf16_f32 v91, v166, v167\n"
  "v_mfma_f32_16x16x32_bf16 a[24:27], a[152:155], v[64:67], a[24:27]\n"
  "ds_read_b64_tr_b16 a[184:185], v206 offset:9728\n"
  "v_fma_mix_f32 v168, v92, v144, v152 op_sel:[0,0,0] op_sel_hi:[1,0,0]\n"
  "v_mfma_f32_16x16x32_bf16 a[56:59], a[152:155], v[68:71], a[56:59]\n"
  "buffer_load_dwordx4 v199, %[srdK], s60 offen offset:1024 lds\n"
  "v_fma_mix_f32 v169, v92, v145, v153 op_sel:[1,0,0] op_sel_hi:[1,0,0]\n"
  "v_mfma_f32_16x16x32_bf16 a[88:91], a[152:155], v[72:75], a[88:91]\n"
  "ds_read_b64_tr_b16 a[186:187], v206 offset:13824\n"
  "v_fma_mix_f32 v170, v93, v146, v154 op_sel:[0,0,0] op_sel_hi:[1,0,0]\n"
  "v_fma_mix_f32 v171, v93, v147, v155 op_sel:[1,0,0] op_sel_hi:[1,0,0]\n"
  "v_mfma_f32_16x16x32_bf16 a[120:123], a[152:155], v[76:79], a[120:123]\n"
  "s_waitcnt lgkmcnt(15)\n"
  "v_fma_mix_f32 v172, v94, v148, v156 op_sel:[0,0,0] op_sel_hi:[1,0,0]\n"
  "v_fma_mix_f32 v173, v94, v149, v157 op_sel:[1,0,0] op_sel_hi:[1,0,0]\n"
  "v_mfma_f32_16x16x32_bf16 a[28:31], a[156:159], v[64:67], a[28:31]\n"
  "ds_read_b64_tr_b16 a[188:189], v206 offset:9736\n"
  "v_fma_mix_f32 v174, v95, v150, v158 op_sel:[0,0,0] op_sel_hi:[1,0,0]\n"
  "v_mfma_f32_16x16x32_bf16 a[60:63], a[156:159], v[68:71], a[60:63]\n"
  "buffer_load_dwordx4 v200, %[srdK], s60 offen offset:2048 lds\n"
  "v_fma_mix_f32 v175, v95, v151, v159 op_sel:[1,0,0] op_sel_hi:[1,0,0]\n"
  "v_cvt_pk_bf16_f32 v92, v168, v169\n"
  "v_mfma_f32_16x16x32_bf16 a[92:95], a[156:159], v[72:75], a[92:95]\n"
  "ds_read_b64_tr_b16 a[190:191], v206 offset:13832\n"
  "v_cvt_pk_bf16_f32 v93, v170, v171\n"
  "v_mfma_f32_16x16x32_bf16 a[124:127], a[156:159], v[76:79], a[124:127]\n"
  "s_waitcnt lgkmcnt(15)\n"
  "v_cvt_pk_bf16_f32 v94, v172, v173\n"
  "v_cvt_pk_bf16_f32 v95, v174, v175\n"
  "s_waitcnt vmcnt(19)\n"
  "s_barrier\n"
  "v_mfma_f32_16x16x32_bf16 a[0:3], a[160:163], v[80:83], a[0:3]\n"
  "s_waitcnt vmcnt(15)\n"
  "s_waitcnt lgkmcnt(8)\n"
  "ds_read_b64_tr_b16 a[128:129], v204 offset:32768\n"
  "v_fma_mix_f32 v160, v96, v128, v136 op_sel:[0,0,0] op_sel_hi:[1,0,0]\n"
  "v_fma_mix_f32 v161, v96, v129, v137 op_sel:[1,0,0] op_sel_hi:[1,0,0]\n"
  "v_mfma_f32_16x16x32_bf16 a[32:35], a[160:163], v[84:87], a[32:35]\n"
  "v_fma_mix_f32 v162, v97, v130, v138 op_sel:[0,0,0] op_sel_hi:[1,0,0]\n"
  "v_fma_mix_f32 v163, v97, v131, v139 op_sel:[1,0,0] op_sel_hi:[1,0,0]\n"
  "v_mfma_f32_16x16x32_bf16 a[64:67], a[160:163], v[88:91], a[64:67]\n"
  "ds_read_b64_tr_b16 a[130:131], v204 offset:36864\n"
  "v_fma_mix_f32 v164, v98, v132, v140 op_sel:[0,0,0] op_sel_hi:[1,0,0]\n"
  "v_mfma_f32_16x16x32_bf16 a[96:99], a[160:163], v[92:95], a[96:99]\n"
  "s_waitcnt lgkmcnt(15)\n"
  "ds_read_b128 v[144:147], v176 offset:3840\n"
  "v_fma_mix_f32 v165, v98, v133, v141 op_sel:[1,0,0] op_sel_hi:[1,0,0]\n"
  "v_fma_mix_f32 v166, v99, v134, v142 op_sel:[0,0,0] op_sel_hi:[1,0,0]\n"
  "v_mfma_f32_16x16x32_bf16 a[4:7], a[164:167], v[80:83], a[4:7]\n"
  "ds_read_b64_tr_b16 a[132:133], v204 offset:32776\n"
  "v_fma_mix_f32 v167, v99, v135, v143 op_sel:[1,0,0] op_sel_hi:[1,0,0]\n"
  "v_mfma_f32_16x16x32_bf16 a[36:39], a[164:167], v[84:87], a[36:39]\n"
  "buffer_load_dwordx4 v201, %[srdK], s60 offen offset:3072 lds\n"
  "v_cvt_pk_bf16_f32 v96, v160, v161\n"
  "v_mfma_f32_16x16x32_bf16 a[68:71], a[164:167], v[88:91], a[68:71]\n"
  "ds_read_b64_tr_b16 a[134:135], v204 offset:36872\n"
  "v_cvt_pk_bf16_f32 v97, v162, v163\n"
  "v_mfma_f32_16x16x32_bf16 a[100:103], a[164:167], v[92:95], a[100:103]\n"
  "s_waitcnt lgkmcnt(15)\n"
  "ds_read_b128 v[148:151], v176 offset:3856\n"
  "v_cvt_pk_bf16_f32 v98, v164, v165\n"
  "v_cvt_pk_bf16_f32 v99, v166, v167\n"
  "v_mfma_f32_16x16x32_bf16 a[8:11], a[168:171], v[80:83], a[8:11]\n"
  "s_add_u32 m0, %[sM0], 81920\n"
  "ds_read_b64_tr_b16 a[136:137], v204 offset:33280\n"
  "v_fma_mix_f32 v168, v100, v128, v136 op_sel:[0,0,0] op_sel_hi:[1,0,0]\n"
  "v_mfma_f32_16x16x32_bf16 a[40:43], a[168:171], v[84:87], a[40:43]\n"
  "s_add_u32 s60, %[sKV], 16384\n"
  "buffer_load_dwordx4 v202, %[srdV], s60 offen offset:0 lds\n"
  "v_fma_mix_f32 v169, v100, v129, v137 op_sel:[1,0,0] op_sel_hi:[1,0,0]\n"
  "v_mfma_f32_16x16x32_bf16 a[72:75], a[168:171], v[88:91], a[72:75]\n"
  "ds_read_b64_tr_b16 a[138:139], v204 offset:37376\n"
  "v_fma_mix_f32 v170, v101, v130, v138 op_sel:[0,0,0] op_sel_hi:[1,0,0]\n"
  "v_fma_mix_f32 v171, v101, v131, v139 op_sel:[1,0,0] op_sel_hi:[1,0,0]\n"
  "v_mfma_f32_16x16x32_bf16 a[104:107], a[168:171], v[92:95], a[104:107]\n"
  "s_waitcnt lgkmcnt(15)\n"
  "ds_read_b128 v[152:155], v176 offset:3872\n"
  "v_fma_mix_f32 v172, v102, v132, v140 op_sel:[0,0,0] op_sel_hi:[1,0,0]\n"
  "v_mfma_f32_16x16x32_bf16 a[12:15], a[172:175], v[80:83], a[12:15]\n"
  "ds_read_b64_tr_b16 a[140:141], v204 offset:33288\n"
  "v_fma_mix_f32 v173, v102, v133, v141 op_sel:[1,0,0] op_sel_hi:[1,0,0]\n"
  "v_mfma_f32_16x16x32_bf16 a[44:47], a[172:175], v[84:87], a[44:47]\n"
  "buffer_load_dwordx4 v203, %[srdV], s60 offen offset:1024 lds\n"
  "v_fma_mix_f32 v174, v103, v134, v142 op_sel:[0,0,0] op_sel_hi:[1,0,0]\n"
  "v_mfma_f32_16x16x32_bf16 a[76:79], a[172:175], v[88:91], a[76:79]\n"
  "ds_read_b64_tr_b16 a[142:143], v204 offset:37384\n"
  "v_fma_mix_f32 v175, v103, v135, v143 op_sel:[1,0,0] op_sel_hi:[1,0,0]\n"
  "v_cvt_pk_bf16_f32 v100, v168, v169\n"
  "v_mfma_f32_16x16x32_bf16 a[108:111], a[172:175], v[92:95], a[108:111]\n"
  "s_waitcnt lgkmcnt(15)\n"
  "ds_read_b128 v[156:159], v176 offset:3888\n"
  "v_cvt_pk_bf16_f32 v101, v170, v171\n"
  "v_mfma_f32_16x16x32_bf16 a[16:19], a[176:179], v[80:83], a[16:19]\n"
  "ds_read_b64_tr_b16 a[144:145], v204 offset:33792\n"
  "v_cvt_pk_bf16_f32 v102, v172, v173\n"
  "v_mfma_f32_16x16x32_bf16 a[48:51], a[176:179], v[84:87], a[48:51]\n"
  "buffer_load_dwordx4 v202, %[srdV], s60 offen offset:2048 lds\n"
  "v_cvt_pk_bf16_f32 v103, v174, v175\n"
  "v_fma_mix_f32 v160, v104, v128, v136 op_sel:[0,0,0] op_sel_hi:[1,0,0]\n"
  "v_mfma_f32_16x16x32_bf16 a[80:83], a[176:179], v[88:91], a[80:83]\n"
  "ds_read_b64_tr_b16 a[146:147], v204 offset:37888\n"
  "v_fma_mix_f32 v161, v104, v129, v137 op_sel:[1,0,0] op_sel_hi:[1,0,0]\n"
  "v_mfma_f32_16x16x32_bf16 a[112:115], a[176:179], v[92:95], a[112:115]\n"
  "s_waitcnt lgkmcnt(15)\n"
  "v_fma_mix_f32 v162, v105, v130, v138 op_sel:[0,0,0] op_sel_hi:[1,0,0]\n"
  "v_fma_mix_f32 v163, v105, v131, v139 op_sel:[1,0,0] op_sel_hi:[1,0,0]\n"
  "v_mfma_f32_16x16x32_bf16 a[20:23], a[180:183], v[80:83], a[20:23]\n"
  "ds_read_b64_tr_b16 a[148:149], v204 offset:33800\n"
  "v_fma_mix_f32 v164, v106, v132, v140 op_sel:[0,0,0] op_sel_hi:[1,0,0]\n"
  "v_mfma_f32_16x16x32_bf16 a[52:55], a[180:183], v[84:87], a[52:55]\n"
  "buffer_load_dwordx4 v203, %[srdV], s60 offen offset:3072 lds\n"
  "v_fma_mix_f32 v165, v106, v133, v141 op_sel:[1,0,0] op_sel_hi:[1,0,0]\n"
  "v_fma_mix_f32 v166, v107, v134, v142 op_sel:[0,0,0] op_sel_hi:[1,0,0]\n"
  "v_mfma_f32_16x16x32_bf16 a[84:87], a[180:183], v[88:91], a[84:87]\n"
  "ds_read_b64_tr_b16 a[150:151], v204 offset:37896\n"
  "v_fma_mix_f32 v167, v107, v135, v143 op_sel:[1,0,0] op_sel_hi:[1,0,0]\n"
  "v_mfma_f32_16x16x32_bf16 a[116:119], a[180:183], v[92:95], a[116:119]\n"
  "s_waitcnt lgkmcnt(15)\n"
  "v_cvt_pk_bf16_f32 v104, v160, v161\n"
  "v_cvt_pk_bf16_f32 v105, v162, v163\n"
  "v_mfma_f32_16x16x32_bf16 a[24:27], a[184:187], v[80:83], a[24:27]\n"
  "ds_read_b64_tr_b16 a[152:153], v204 offset:34304\n"
  "v_cvt_pk_bf16_f32 v106, v164, v165\n"
  "v_cvt_pk_bf16_f32 v107, v166, v167\n"
  "v_mfma_f32_16x16x32_bf16 a[56:59], a[184:187], v[84:87], a[56:59]\n"
  "v_fma_mix_f32 v168, v108, v128, v136 op_sel:[0,0,0] op_sel_hi:[1,0,0]\n"
  "v_fma_mix_f32 v169, v108, v129, v137 op_sel:[1,0,0] op_sel_hi:[1,0,0]\n"
  "v_mfma_f32_16x16x32_bf16 a[88:91], a[184:187], v[88:91], a[88:91]\n"
  "ds_read_b64_tr_b16 a[154:155], v204 offset:38400\n"
  "v_fma_mix_f32 v170, v109, v130, v138 op_sel:[0,0,0] op_sel_hi:[1,0,0]\n"
  "v_mfma_f32_16x16x32_bf16 a[120:123], a[184:187], v[92:95], a[120:123]\n"
  "s_waitcnt lgkmcnt(15)\n"
  "v_fma_mix_f32 v171, v109, v131, v139 op_sel:[1,0,0] op_sel_hi:[1,0,0]\n"
  "v_fma_mix_f32 v172, v110, v132, v140 op_sel:[0,0,0] op_sel_hi:[1,0,0]\n"
  "v_mfma_f32_16x16x32_bf16 a[28:31], a[188:191], v[80:83], a[28:31]\n"
  "ds_read_b64_tr_b16 a[156:157], v204 offset:34312\n"
  "v_fma_mix_f32 v173, v110, v133, v141 op_sel:[1,0,0] op_sel_hi:[1,0,0]\n"
  "v_fma_mix_f32 v174, v111, v134, v142 op_sel:[0,0,0] op_sel_hi:[1,0,0]\n"
  "v_mfma_f32_16x16x32_bf16 a[60:63], a[188:191], v[84:87], a[60:63]\n"
  "v_fma_mix_f32 v175, v111, v135, v143 op_sel:[1,0,0] op_sel_hi:[1,0,0]\n"
  "v_cvt_pk_bf16_f32 v108, v168, v169\n"
  "v_mfma_f32_16x16x32_bf16 a[92:95], a[188:191], v[88:91], a[92:95]\n"
  "ds_read_b64_tr_b16 a[158:159], v204 offset:38408\n"
  "v_cvt_pk_bf16_f32 v109, v170, v171\n"
  "v_mfma_f32_16x16x32_bf16 a[124:127], a[188:191], v[92:95], a[124:127]\n"
  "s_waitcnt lgkmcnt(15)\n"
  "v_cvt_pk_bf16_f32 v110, v172, v173\n"
  "v_cvt_pk_bf16_f32 v111, v174, v175\n"
  "v_mfma_f32_16x16x32_bf16 a[0:3], a[128:131], v[96:99], a[0:3]\n"
  "s_waitcnt vmcnt(16)\n"
  "s_waitcnt lgkmcnt(8)\n"
  "ds_read_b64_tr_b16 a[160:161], v204 offset:40960\n"
  "v_fma_mix_f32 v160, v112, v144, v152 op_sel:[0,0,0] op_sel_hi:[1,0,0]\n"
  "v_mfma_f32_16x16x32_bf16 a[32:35], a[128:131], v[100:103], a[32:35]\n"
  "v_fma_mix_f32 v161, v112, v145, v153 op_sel:[1,0,0] op_sel_hi:[1,0,0]\n"
  "v_fma_mix_f32 v162, v113, v146, v154 op_sel:[0,0,0] op_sel_hi:[1,0,0]\n"
  "v_mfma_f32_16x16x32_bf16 a[64:67], a[128:131], v[104:107], a[64:67]\n"
  "ds_read_b64_tr_b16 a[162:163], v204 offset:45056\n"
  "v_fma_mix_f32 v163, v113, v147, v155 op_sel:[1,0,0] op_sel_hi:[1,0,0]\n"
  "v_mfma_f32_16x16x32_bf16 a[96:99], a[128:131], v[108:111], a[96:99]\n"
  "s_waitcnt lgkmcnt(15)\n"
  "v_fma_mix_f32 v164, v114, v148, v156 op_sel:[0,0,0] op_sel_hi:[1,0,0]\n"
  "v_fma_mix_f32 v165, v114, v149, v157 op_sel:[1,0,0] op_sel_hi:[1,0,0]\n"
  "v_mfma_f32_16x16x32_bf16 a[4:7], a[132:135], v[96:99], a[4:7]\n"
  "ds_read_b64_tr_b16 a[164:165], v204 offset:40968\n"
  "v_fma_mix_f32 v166, v115, v150, v158 op_sel:[0,0,0] op_sel_hi:[1,0,0]\n"
  "v_mfma_f32_16x16x32_bf16 a[36:39], a[132:135], v[100:103], a[36:39]\n"
  "v_fma_mix_f32 v167, v115, v151, v159 op_sel:[1,0,0] op_sel_hi:[1,0,0]\n"
  "v_cvt_pk_bf16_f32 v112, v160, v161\n"
  "v_mfma_f32_16x16x32_bf16 a[68:71], a[132:135], v[104:107], a[68:71]\n"
  "ds_read_b64_tr_b16 a[166:167], v204 offset:45064\n"
  "v_cvt_pk_bf16_f32 v113, v162, v163\n"
  "v_mfma_f32_16x16x32_bf16 a[100:103], a[132:135], v[108:111], a[100:103]\n"
  "s_waitcnt lgkmcnt(15)\n"
  "v_cvt_pk_bf16_f32 v114, v164, v165\n"
  "v_cvt_pk_bf16_f32 v115, v166, v167\n"
  "v_mfma_f32_16x16x32_bf16 a[8:11], a[136:139], v[96:99], a[8:11]\n"
  "ds_read_b64_tr_b16 a[168:169], v204 offset:41472\n"
  "v_fma_mix_f32 v168, v116, v144, v152 op_sel:[0,0,0] op_sel_hi:[1,0,0]\n"
  "v_mfma_f32_16x16x32_bf16 a[40:43], a[136:139], v[100:103], a[40:43]\n"
  "v_fma_mix_f32 v169, v116, v145, v153 op_sel:[1,0,0] op_sel_hi:[1,0,0]\n"
  "v_fma_mix_f32 v170, v117, v146, v154 op_sel:[0,0,0] op_sel_hi:[1,0,0]\n"
  "v_mfma_f32_16x16x32_bf16 a[72:75], a[136:139], v[104:107], a[72:75]\n"
  "ds_read_b64_tr_b16 a[170:171], v204 offset:45568\n"
  "v_fma_mix_f32 v171, v117, v147, v155 op_sel:[1,0,0] op_sel_hi:[1,0,0]\n"
  "v_mfma_f32_16x16x32_bf16 a[104:107], a[136:139], v[108:111], a[104:107]\n"
  "s_waitcnt lgkmcnt(15)\n"
  "v_fma_mix_f32 v172, v118, v148, v156 op_sel:[0,0,0] op_sel_hi:[1,0,0]\n"
  "v_fma_mix_f32 v173, v118, v149, v157 op_sel:[1,0,0] op_sel_hi:[1,0,0]\n"
  "v_mfma_f32_16x16x32_bf16 a[12:15], a[140:143], v[96:99], a[12:15]\n"
  "ds_read_b64_tr_b16 a[172:173], v204 offset:41480\n"
  "v_fma_mix_f32 v174, v119, v150, v158 op_sel:[0,0,0] op_sel_hi:[1,0,0]\n"
  "v_mfma_f32_16x16x32_bf16 a[44:47], a[140:143], v[100:103], a[44:47]\n"
  "v_fma_mix_f32 v175, v119, v151, v159 op_sel:[1,0,0] op_sel_hi:[1,0,0]\n"
  "v_cvt_pk_bf16_f32 v116, v168, v169\n"
  "v_mfma_f32_16x16x32_bf16 a[76:79], a[140:143], v[104:107], a[76:79]\n"
  "ds_read_b64_tr_b16 a[174:175], v204 offset:45576\n"
  "v_cvt_pk_bf16_f32 v117, v170, v171\n"
  "v_mfma_f32_16x16x32_bf16 a[108:111], a[140:143], v[108:111], a[108:111]\n"
  "s_waitcnt lgkmcnt(14)\n"
  "v_cvt_pk_bf16_f32 v118, v172, v173\n"
  "v_cvt_pk_bf16_f32 v119, v174, v175\n"
  "v_mfma_f32_16x16x32_bf16 a[16:19], a[144:147], v[96:99], a[16:19]\n"
  "ds_read_b64_tr_b16 a[176:177], v204 offset:41984\n"
  "v_fma_mix_f32 v160, v120, v144, v152 op_sel:[0,0,0] op_sel_hi:[1,0,0]\n"
  "v_mfma_f32_16x16x32_bf16 a[48:51], a[144:147], v[100:103], a[48:51]\n"
  "v_fma_mix_f32 v161, v120, v145, v153 op_sel:[1,0,0] op_sel_hi:[1,0,0]\n"
  "v_fma_mix_f32 v162, v121, v146, v154 op_sel:[0,0,0] op_sel_hi:[1,0,0]\n"
  "v_mfma_f32_16x16x32_bf16 a[80:83], a[144:147], v[104:107], a[80:83]\n"
  "ds_read_b64_tr_b16 a[178:179], v204 offset:46080\n"
  "v_fma_mix_f32 v163, v121, v147, v155 op_sel:[1,0,0] op_sel_hi:[1,0,0]\n"
  "v_mfma_f32_16x16x32_bf16 a[112:115], a[144:147], v[108:111], a[112:115]\n"
  "s_waitcnt lgkmcnt(14)\n"
  "v_fma_mix_f32 v164, v122, v148, v156 op_sel:[0,0,0] op_sel_hi:[1,0,0]\n"
  "v_fma_mix_f32 v165, v122, v149, v157 op_sel:[1,0,0] op_sel_hi:[1,0,0]\n"
  "v_mfma_f32_16x16x32_bf16 a[20:23], a[148:151], v[96:99], a[20:23]\n"
  "ds_read_b64_tr_b16 a[180:181], v204 offset:41992\n"
  "v_fma_mix_f32 v166, v123, v150, v158 op_sel:[0,0,0] op_sel_hi:[1,0,0]\n"
  "v_mfma_f32_16x16x32_bf16 a[52:55], a[148:151], v[100:103], a[52:55]\n"
  "v_fma_mix_f32 v167, v123, v151, v159 op_sel:[1,0,0] op_sel_hi:[1,0,0]\n"
  "v_cvt_pk_bf16_f32 v120, v160, v161\n"
  "v_mfma_f32_16x16x32_bf16 a[84:87], a[148:151], v[104:107], a[84:87]\n"
  "ds_read_b64_tr_b16 a[182:183], v204 offset:46088\n"
  "v_cvt_pk_bf16_f32 v121, v162, v163\n"
  "v_mfma_f32_16x16x32_bf16 a[116:119], a[148:151], v[108:111], a[116:119]\n"
  "s_waitcnt lgkmcnt(14)\n"
  "v_cvt_pk_bf16_f32 v122, v164, v165\n"
  "v_cvt_pk_bf16_f32 v123, v166, v167\n"
  "v_mfma_f32_16x16x32_bf16 a[24:27], a[152:155], v[96:99], a[24:27]\n"
  "ds_read_b64_tr_b16 a[184:185], v204 offset:42496\n"
  "v_fma_mix_f32 v168, v124, v144, v152 op_sel:[0,0,0] op_sel_hi:[1,0,0]\n"
  "v_mfma_f32_16x16x32_bf16 a[56:59], a[152:155], v[100:103], a[56:59]\n"
  "v_fma_mix_f32 v169, v124, v145, v153 op_sel:[1,0,0] op_sel_hi:[1,0,0]\n"
  "v_fma_mix_f32 v170, v125, v146, v154 op_sel:[0,0,0] op_sel_hi:[1,0,0]\n"
  "v_mfma_f32_16x16x32_bf16 a[88:91], a[152:155], v[104:107], a[88:91]\n"
  "ds_read_b64_tr_b16 a[186:187], v204 offset:46592\n"
  "v_fma_mix_f32 v171, v125, v147, v155 op_sel:[1,0,0] op_sel_hi:[1,0,0]\n"
  "v_mfma_f32_16x16x32_bf16 a[120:123], a[152:155], v[108:111], a[120:123]\n"
  "s_waitcnt lgkmcnt(14)\n"
  "v_fma_mix_f32 v172, v126, v148, v156 op_sel:[0,0,0] op_sel_hi:[1,0,0]\n"
  "v_fma_mix_f32 v173, v126, v149, v157 op_sel:[1,0,0] op_sel_hi:[1,0,0]\n"
  "v_mfma_f32_16x16x32_bf16 a[28:31], a[156:159], v[96:99], a[28:31]\n"
  "ds_read_b64_tr_b16 a[188:189], v204 offset:42504\n"
  "v_fma_mix_f32 v174, v127, v150, v158 op_sel:[0,0,0] op_sel_hi:[1,0,0]\n"
  "v_mfma_f32_16x16x32_bf16 a[60:63], a[156:159], v[100:103], a[60:63]\n"
  "v_fma_mix_f32 v175, v127, v151, v159 op_sel:[1,0,0] op_sel_hi:[1,0,0]\n"
  "v_cvt_pk_bf16_f32 v124, v168, v169\n"
  "v_mfma_f32_16x16x32_bf16 a[92:95], a[156:159], v[104:107], a[92:95]\n"
  "ds_read_b64_tr_b16 a[190:191], v204 offset:46600\n"
  "v_cvt_pk_bf16_f32 v125, v170, v171\n"
  "v_mfma_f32_16x16x32_bf16 a[124:127], a[156:159], v[108:111], a[124:127]\n"
  "s_waitcnt lgkmcnt(14)\n"
  "v_cvt_pk_bf16_f32 v126, v172, v173\n"
  "v_cvt_pk_bf16_f32 v127, v174, v175\n"
  "s_waitcnt vmcnt(8)\n"
  "s_barrier\n"
  "v_mfma_f32_16x16x32_bf16 a[0:3], a[160:163], v[112:115], a[0:3]\n"
  "ds_read_b128 a[192:195], v192 offset:0\n"
  "v_mfma_f32_16x16x32_bf16 a[32:35], a[160:163], v[116:119], a[32:35]\n"
  "v_mfma_f32_16x16x32_bf16 a[64:67], a[160:163], v[120:123], a[64:67]\n"
  "v_mfma_f32_16x16x32_bf16 a[96:99], a[160:163], v[124:127], a[96:99]\n"
  "s_waitcnt lgkmcnt(13)\n"
  "v_mfma_f32_16x16x32_bf16 a[4:7], a[164:167], v[112:115], a[4:7]\n"
  "ds_read_b128 a[196:199], v193 offset:0\n"
  "v_mfma_f32_16x16x32_bf16 a[36:39], a[164:167], v[116:119], a[36:39]\n"
  "v_mfma_f32_16x16x32_bf16 a[68:71], a[164:167], v[120:123], a[68:71]\n"
  "v_mfma_f32_16x16x32_bf16 a[100:103], a[164:167], v[124:127], a[100:103]\n"
  "s_waitcnt lgkmcnt(12)\n"
  "v_mfma_f32_16x16x32_bf16 a[8:11], a[168:171], v[112:115], a[8:11]\n"
  "ds_read_b128 a[200:203], v194 offset:0\n"
  "v_mfma_f32_16x16x32_bf16 a[40:43], a[168:171], v[116:119], a[40:43]\n"
  "v_mfma_f32_16x16x32_bf16 a[72:75], a[168:171], v[120:123], a[72:75]\n"
  "v_mfma_f32_16x16x32_bf16 a[104:107], a[168:171], v[124:127], a[104:107]\n"
  "s_waitcnt lgkmcnt(11)\n"
  "v_mfma_f32_16x16x32_bf16 a[12:15], a[172:175], v[112:115], a[12:15]\n"
  "ds_read_b128 a[204:207], v195 offset:0\n"
  "v_mfma_f32_16x16x32_bf16 a[44:47], a[172:175], v[116:119], a[44:47]\n"
  "v_mfma_f32_16x16x32_bf16 a[76:79], a[172:175], v[120:123], a[76:79]\n"
  "v_mfma_f32_16x16x32_bf16 a[108:111], a[172:175], v[124:127], a[108:111]\n"
  "s_waitcnt lgkmcnt(10)\n"
  "v_mfma_f32_16x16x32_bf16 a[16:19], a[176:179], v[112:115], a[16:19]\n"
  "ds_read_b128 a[208:211], v192 offset:4096\n"
  "v_mfma_f32_16x16x32_bf16 a[48:51], a[176:179], v[116:119], a[48:51]\n"
  "v_mfma_f32_16x16x32_bf16 a[80:83], a[176:179], v[120:123], a[80:83]\n"
  "v_mfma_f32_16x16x32_bf16 a[112:115], a[176:179], v[124:127], a[112:115]\n"
  "s_waitcnt lgkmcnt(9)\n"
  "v_mfma_f32_16x16x32_bf16 a[20:23], a[180:183], v[112:115], a[20:23]\n"
  "ds_read_b128 a[212:215], v193 offset:4096\n"
  "v_mfma_f32_16x16x32_bf16 a[52:55], a[180:183], v[116:119], a[52:55]\n"
  "v_mfma_f32_16x16x32_bf16 a[84:87], a[180:183], v[120:123], a[84:87]\n"
  "v_mfma_f32_16x16x32_bf16 a[116:119], a[180:183], v[124:127], a[116:119]\n"
  "s_waitcnt lgkmcnt(8)\n"
  "v_mfma_f32_16x16x32_bf16 a[24:27], a[184:187], v[112:115], a[24:27]\n"
  "ds_read_b128 a[216:219], v194 offset:4096\n"
  "v_mfma_f32_16x16x32_bf16 a[56:59], a[184:187], v[116:119], a[56:59]\n"
  "v_mfma_f32_16x16x32_bf16 a[88:91], a[184:187], v[120:123], a[88:91]\n"
  "v_mfma_f32_16x16x32_bf16 a[120:123], a[184:187], v[124:127], a[120:123]\n"
  "s_waitcnt lgkmcnt(7)\n"
  "v_mfma_f32_16x16x32_bf16 a[28:31], a[188:191], v[112:115], a[28:31]\n"
  "ds_read_b128 a[220:223], v195 offset:4096\n"
  "v_mfma_f32_16x16x32_bf16 a[60:63], a[188:191], v[116:119], a[60:63]\n"
  "v_mfma_f32_16x16x32_bf16 a[92:95], a[188:191], v[120:123], a[92:95]\n"
  "v_mfma_f32_16x16x32_bf16 a[124:127], a[188:191], v[124:127], a[124:127]\n"
  "s_barrier\n"
  "s_add_u32 m0, %[sM0], 32768\n"
  "s_add_u32 s60, %[sKV], 32768\n"
  "buffer_load_dwordx4 v198, %[srdK], s60 offen offset:0 lds\n"
  "buffer_load_dwordx4 v199, %[srdK], s60 offen offset:1024 lds\n"
  "buffer_load_dwordx4 v200, %[srdK], s60 offen offset:2048 lds\n"
  "buffer_load_dwordx4 v201, %[srdK], s60 offen offset:3072 lds\n"
  "v_accvgpr_read_b32 v0, a0\n"
  "v_accvgpr_read_b32 v1, a1\n"
  "v_accvgpr_read_b32 v2, a2\n"
  "v_accvgpr_read_b32 v3, a3\n"
  "v_accvgpr_read_b32 v4, a4\n"
  "v_accvgpr_read_b32 v5, a5\n"
  "v_accvgpr_read_b32 v6, a6\n"
  "v_accvgpr_read_b32 v7, a7\n"
  "v_cvt_pk_bf16_f32 v0, v0, v1\n"
  "v_cvt_pk_bf16_f32 v1, v2, v3\n"
  "v_cvt_pk_bf16_f32 v2, v4, v5\n"
  "v_cvt_pk_bf16_f32 v3, v6, v7\n"
  "v_accvgpr_read_b32 v8, a8\n"
  "v_accvgpr_read_b32 v9, a9\n"
  "v_accvgpr_read_b32 v10, a10\n"
  "v_accvgpr_read_b32 v11, a11\n"
  "v_accvgpr_read_b32 v12, a12\n"
  "v_accvgpr_read_b32 v13, a13\n"
  "v_accvgpr_read_b32 v14, a14\n"
  "v_accvgpr_read_b32 v15, a15\n"
  "v_cvt_pk_bf16_f32 v8, v8, v9\n"
  "v_cvt_pk_bf16_f32 v9, v10, v11\n"
  "v_cvt_pk_bf16_f32 v10, v12, v13\n"
  "v_cvt_pk_bf16_f32 v11, v14, v15\n"
  "v_accvgpr_read_b32 v16, a16\n"
  "v_accvgpr_read_b32 v17, a17\n"
  "v_accvgpr_read_b32 v18, a18\n"
  "v_accvgpr_read_b32 v19, a19\n"
  "v_accvgpr_read_b32 v20, a20\n"
  "v_accvgpr_read_b32 v21, a21\n"
  "v_accvgpr_read_b32 v22, a22\n"
  "v_accvgpr_read_b32 v23, a23\n"
  "v_cvt_pk_bf16_f32 v16, v16, v17\n"
  "v_cvt_pk_bf16_f32 v17, v18, v19\n"
  "v_cvt_pk_bf16_f32 v18, v20, v21\n"
  "v_cvt_pk_bf16_f32 v19, v22, v23\n"
  "v_accvgpr_read_b32 v24, a24\n"
  "v_accvgpr_read_b32 v25, a25\n"
  "v_accvgpr_read_b32 v26, a26\n"
  "v_accvgpr_read_b32 v27, a27\n"
  "v_accvgpr_read_b32 v28, a28\n"
  "v_accvgpr_read_b32 v29, a29\n"
  "v_accvgpr_read_b32 v30, a30\n"
  "v_accvgpr_read_b32 v31, a31\n"
  "v_cvt_pk_bf16_f32 v24, v24, v25\n"
  "v_cvt_pk_bf16_f32 v25, v26, v27\n"
  "v_cvt_pk_bf16_f32 v26, v28, v29\n"
  "v_cvt_pk_bf16_f32 v27, v30, v31\n"
  "v_accvgpr_read_b32 v32, a32\n"
  "v_accvgpr_read_b32 v33, a33\n"
  "v_accvgpr_read_b32 v34, a34\n"
  "v_accvgpr_read_b32 v35, a35\n"
  "v_accvgpr_read_b32 v36, a36\n"
  "v_accvgpr_read_b32 v37, a37\n"
  "v_accvgpr_read_b32 v38, a38\n"
  "v_accvgpr_read_b32 v39, a39\n"
  "v_cvt_pk_bf16_f32 v32, v32, v33\n"
  "v_cvt_pk_bf16_f32 v33, v34, v35\n"
  "v_cvt_pk_bf16_f32 v34, v36, v37\n"
  "v_cvt_pk_bf16_f32 v35, v38, v39\n"
  "v_accvgpr_read_b32 v40, a40\n"
  "v_accvgpr_read_b32 v41, a41\n"
  "v_accvgpr_read_b32 v42, a42\n"
  "v_accvgpr_read_b32 v43, a43\n"
  "v_accvgpr_read_b32 v44, a44\n"
  "v_accvgpr_read_b32 v45, a45\n"
  "v_accvgpr_read_b32 v46, a46\n"
  "v_accvgpr_read_b32 v47, a47\n"
  "v_cvt_pk_bf16_f32 v40, v40, v41\n"
  "v_cvt_pk_bf16_f32 v41, v42, v43\n"
  "v_cvt_pk_bf16_f32 v42, v44, v45\n"
  "v_cvt_pk_bf16_f32 v43, v46, v47\n"
  "v_accvgpr_read_b32 v48, a48\n"
  "v_accvgpr_read_b32 v49, a49\n"
  "v_accvgpr_read_b32 v50, a50\n"
  "v_accvgpr_read_b32 v51, a51\n"
  "v_accvgpr_read_b32 v52, a52\n"
  "v_accvgpr_read_b32 v53, a53\n"
  "v_accvgpr_read_b32 v54, a54\n"
  "v_accvgpr_read_b32 v55, a55\n"
  "v_cvt_pk_bf16_f32 v48, v48, v49\n"
  "v_cvt_pk_bf16_f32 v49, v50, v51\n"
  "v_cvt_pk_bf16_f32 v50, v52, v53\n"
  "v_cvt_pk_bf16_f32 v51, v54, v55\n"
  "v_accvgpr_read_b32 v56, a56\n"
  "v_accvgpr_read_b32 v57, a57\n"
  "v_accvgpr_read_b32 v58, a58\n"
  "v_accvgpr_read_b32 v59, a59\n"
  "v_accvgpr_read_b32 v60, a60\n"
  "v_accvgpr_read_b32 v61, a61\n"
  "v_accvgpr_read_b32 v62, a62\n"
  "v_accvgpr_read_b32 v63, a63\n"
  "v_cvt_pk_bf16_f32 v56, v56, v57\n"
  "v_cvt_pk_bf16_f32 v57, v58, v59\n"
  "v_cvt_pk_bf16_f32 v58, v60, v61\n"
  "v_cvt_pk_bf16_f32 v59, v62, v63\n"
  "v_accvgpr_read_b32 v64, a64\n"
  "v_accvgpr_read_b32 v65, a65\n"
  "v_accvgpr_read_b32 v66, a66\n"
  "v_accvgpr_read_b32 v67, a67\n"
  "v_accvgpr_read_b32 v68, a68\n"
  "v_accvgpr_read_b32 v69, a69\n"
  "v_accvgpr_read_b32 v70, a70\n"
  "v_accvgpr_read_b32 v71, a71\n"
  "v_cvt_pk_bf16_f32 v64, v64, v65\n"
  "v_cvt_pk_bf16_f32 v65, v66, v67\n"
  "v_cvt_pk_bf16_f32 v66, v68, v69\n"
  "v_cvt_pk_bf16_f32 v67, v70, v71\n"
  "v_accvgpr_read_b32 v72, a72\n"
  "v_accvgpr_read_b32 v73, a73\n"
  "v_accvgpr_read_b32 v74, a74\n"
  "v_accvgpr_read_b32 v75, a75\n"
  "v_accvgpr_read_b32 v76, a76\n"
  "v_accvgpr_read_b32 v77, a77\n"
  "v_accvgpr_read_b32 v78, a78\n"
  "v_accvgpr_read_b32 v79, a79\n"
  "v_cvt_pk_bf16_f32 v72, v72, v73\n"
  "v_cvt_pk_bf16_f32 v73, v74, v75\n"
  "v_cvt_pk_bf16_f32 v74, v76, v77\n"
  "v_cvt_pk_bf16_f32 v75, v78, v79\n"
  "v_accvgpr_read_b32 v80, a80\n"
  "v_accvgpr_read_b32 v81, a81\n"
  "v_accvgpr_read_b32 v82, a82\n"
  "v_accvgpr_read_b32 v83, a83\n"
  "v_accvgpr_read_b32 v84, a84\n"
  "v_accvgpr_read_b32 v85, a85\n"
  "v_accvgpr_read_b32 v86, a86\n"
  "v_accvgpr_read_b32 v87, a87\n"
  "v_cvt_pk_bf16_f32 v80, v80, v81\n"
  "v_cvt_pk_bf16_f32 v81, v82, v83\n"
  "v_cvt_pk_bf16_f32 v82, v84, v85\n"
  "v_cvt_pk_bf16_f32 v83, v86, v87\n"
  "v_accvgpr_read_b32 v88, a88\n"
  "v_accvgpr_read_b32 v89, a89\n"
  "v_accvgpr_read_b32 v90, a90\n"
  "v_accvgpr_read_b32 v91, a91\n"
  "v_accvgpr_read_b32 v92, a92\n"
  "v_accvgpr_read_b32 v93, a93\n"
  "v_accvgpr_read_b32 v94, a94\n"
  "v_accvgpr_read_b32 v95, a95\n"
  "v_cvt_pk_bf16_f32 v88, v88, v89\n"
  "v_cvt_pk_bf16_f32 v89, v90, v91\n"
  "v_cvt_pk_bf16_f32 v90, v92, v93\n"
  "v_cvt_pk_bf16_f32 v91, v94, v95\n"
  "v_accvgpr_read_b32 v96, a96\n"
  "v_accvgpr_read_b32 v97, a97\n"
  "v_accvgpr_read_b32 v98, a98\n"
  "v_accvgpr_read_b32 v99, a99\n"
  "v_accvgpr_read_b32 v100, a100\n"
  "v_accvgpr_read_b32 v101, a101\n"
  "v_accvgpr_read_b32 v102, a102\n"
  "v_accvgpr_read_b32 v103, a103\n"
  "v_cvt_pk_bf16_f32 v96, v96, v97\n"
  "v_cvt_pk_bf16_f32 v97, v98, v99\n"
  "v_cvt_pk_bf16_f32 v98, v100, v101\n"
  "v_cvt_pk_bf16_f32 v99, v102, v103\n"
  "v_accvgpr_read_b32 v104, a104\n"
  "v_accvgpr_read_b32 v105, a105\n"
  "v_accvgpr_read_b32 v106, a106\n"
  "v_accvgpr_read_b32 v107, a107\n"
  "v_accvgpr_read_b32 v108, a108\n"
  "v_accvgpr_read_b32 v109, a109\n"
  "v_accvgpr_read_b32 v110, a110\n"
  "v_accvgpr_read_b32 v111, a111\n"
  "v_cvt_pk_bf16_f32 v104, v104, v105\n"
  "v_cvt_pk_bf16_f32 v105, v106, v107\n"
  "v_cvt_pk_bf16_f32 v106, v108, v109\n"
  "v_cvt_pk_bf16_f32 v107, v110, v111\n"
  "v_accvgpr_read_b32 v112, a112\n"
  "v_accvgpr_read_b32 v113, a113\n"
  "v_accvgpr_read_b32 v114, a114\n"
  "v_accvgpr_read_b32 v115, a115\n"
  "v_accvgpr_read_b32 v116, a116\n"
  "v_accvgpr_read_b32 v117, a117\n"
  "v_accvgpr_read_b32 v118, a118\n"
  "v_accvgpr_read_b32 v119, a119\n"
  "v_cvt_pk_bf16_f32 v112, v112, v113\n"
  "v_cvt_pk_bf16_f32 v113, v114, v115\n"
  "v_cvt_pk_bf16_f32 v114, v116, v117\n"
  "v_cvt_pk_bf16_f32 v115, v118, v119\n"
  "v_accvgpr_read_b32 v120, a120\n"
  "v_accvgpr_read_b32 v121, a121\n"
  "v_accvgpr_read_b32 v122, a122\n"
  "v_accvgpr_read_b32 v123, a123\n"
  "v_accvgpr_read_b32 v124, a124\n"
  "v_accvgpr_read_b32 v125, a125\n"
  "v_accvgpr_read_b32 v126, a126\n"
  "v_accvgpr_read_b32 v127, a127\n"
  "v_cvt_pk_bf16_f32 v120, v120, v121\n"
  "v_cvt_pk_bf16_f32 v121, v122, v123\n"
  "v_cvt_pk_bf16_f32 v122, v124, v125\n"
  "v_cvt_pk_bf16_f32 v123, v126, v127\n"
  "v_and_b32 v208, 0xf0, %[vL16]\n"
  "v_cmp_eq_u32 vcc, 0, v208\n"
  "v_mov_b32 v209, 0x3f803f80\n"
  "s_nop 0\n"
  "v_cndmask_b32 v168, 0, v209, vcc\n"
  "v_cndmask_b32 v169, 0, v209, vcc\n"
  "v_cndmask_b32 v170, 0, v209, vcc\n"
  "v_cndmask_b32 v171, 0, v209, vcc\n"
  "s_add_u32 s62, %[sKV], 32768\n"
  "s_mov_b64 s[68:69], %[pP0]\n"
  "ds_read_b64_tr_b16 v[4:5], v196 offset:0\n"
  "ds_read_b64_tr_b16 v[6:7], v196 offset:4096\n"
  "ds_read_b64_tr_b16 v[12:13], v197 offset:0\n"
  "ds_read_b64_tr_b16 v[14:15], v197 offset:4096\n"
  "ds_read_b64_tr_b16 v[20:21], v196 offset:512\n"
  "ds_read_b64_tr_b16 v[22:23], v196 offset:4608\n"
  "ds_read_b64_tr_b16 v[28:29], v197 offset:512\n"
  "ds_read_b64_tr_b16 v[30:31], v197 offset:4608\n"
  "ds_read_b64_tr_b16 v[36:37], v196 offset:1024\n"
  "ds_read_b64_tr_b16 v[38:39], v196 offset:5120\n"
  "ds_read_b64_tr_b16 v[44:45], v197 offset:1024\n"
  "ds_read_b64_tr_b16 v[46:47], v197 offset:5120\n"
  "ds_read_b128 a[224:227], v192 offset:8192\n"
  "s_waitcnt lgkmcnt(13)\n"
  "v_mfma_f32_16x16x32_bf16 v[128:131], a[192:195], v[0:3], 0\n"
  "ds_read_b64_tr_b16 v[52:53], v196 offset:1536\n"
  "v_mfma_f32_16x16x32_bf16 v[128:131], a[196:199], v[8:11], v[128:131]\n"
  "v_mfma_f32_16x16x32_bf16 v[128:131], a[200:203], v[16:19], v[128:131]\n"
  "ds_read_b64_tr_b16 v[54:55], v196 offset:5632\n"
  "v_mfma_f32_16x16x32_bf16 v[128:131], a[204:207], v[24:27], v[128:131]\n"
  "v_mfma_f32_16x16x32_bf16 v[132:135], a[208:211], v[0:3], 0\n"
  "ds_read_b128 a[228:231], v193 offset:8192\n"
  "v_mfma_f32_16x16x32_bf16 v[132:135], a[212:215], v[8:11], v[132:135]\n"
  "s_add_u32 m0, %[sM0], 98304\n"
  "v_mfma_f32_16x16x32_bf16 v[132:135], a[216:219], v[16:19], v[132:135]\n"
  "v_mfma_f32_16x16x32_bf16 v[132:135], a[220:223], v[24:27], v[132:135]\n"
  "buffer_load_dwordx4 v202, %[srdV], s62 offen offset:0 lds\n"
  "s_nop 0\n"
  "v_max3_f32 v208, v128, v129, v130\n"
  "s_nop 4\n"
  "v_max3_f32 v208, v208, v131, v132\n"
  "v_max3_f32 v208, v208, v133, v134\n"
  "v_max_f32 v208, v208, v135\n"
  "v_mov_b32 v209, v208\n"
  "s_nop 1\n"
  "v_permlane16_swap_b32 v208, v209\n"
  "v_max_f32 v208, v208, v209\n"
  "v_mov_b32 v209, v208\n"
  "s_nop 1\n"
  "v_permlane32_swap_b32 v208, v209\n"
  "v_max_f32 v208, v208, v209\n"
  "v_sub_f32 v128, v128, v208\n"
  "v_sub_f32 v129, v129, v208\n"
  "v_sub_f32 v130, v130, v208\n"
  "v_sub_f32 v131, v131, v208\n"
  "v_sub_f32 v132, v132, v208\n"
  "v_sub_f32 v133, v133, v208\n"
  "v_sub_f32 v134, v134, v208\n"
  "v_sub_f32 v135, v135, v208\n"
  "v_sub_f32 v152, 0, v208\n"
  "v_sub_f32 v153, 0, v208\n"
  "v_sub_f32 v154, 0, v208\n"
  "v_sub_f32 v155, 0, v208\n"
  "v_exp_f32 v128, v128\n"
  "ds_read_b64_tr_b16 v[60:61], v197 offset:1536\n"
  "v_exp_f32 v129, v129\n"
  "ds_read_b64_tr_b16 v[62:63], v197 offset:5632\n"
  "v_cvt_pk_bf16_f32 v144, v128, v129\n"
  "v_exp_f32 v130, v130\n"
  "ds_read_b128 a[232:235], v194 offset:8192\n"
  "v_exp_f32 v131, v131\n"
  "s_nop 0\n"
  "v_cvt_pk_bf16_f32 v145, v130, v131\n"
  "v_exp_f32 v132, v132\n"
  "v_mfma_f32_16x16x32_bf16 v[136:139], a[192:195], v[32:35], 0\n"
  "ds_read_b64_tr_b16 v[68:69], v196 offset:8192\n"
  "v_mfma_f32_16x16x32_bf16 v[136:139], a[196:199], v[40:43], v[136:139]\n"
  "v_exp_f32 v133, v133\n"
  "v_mfma_f32_16x16x32_bf16 v[136:139], a[200:203], v[48:51], v[136:139]\n"
  "ds_read_b64_tr_b16 v[70:71], v196 offset:12288\n"
  "v_cvt_pk_bf16_f32 v146, v132, v133\n"
  "v_mfma_f32_16x16x32_bf16 v[136:139], a[204:207], v[56:59], v[136:139]\n"
  "v_exp_f32 v134, v134\n"
  "v_mfma_f32_16x16x32_bf16 v[140:143], a[208:211], v[32:35], 0\n"
  "ds_read_b128 a[236:239], v195 offset:8192\n"
  "v_mfma_f32_16x16x32_bf16 v[140:143], a[212:215], v[40:43], v[140:143]\n"
  "v_exp_f32 v135, v135\n"
  "v_mfma_f32_16x16x32_bf16 v[140:143], a[216:219], v[48:51], v[140:143]\n"
  "v_cvt_pk_bf16_f32 v147, v134, v135\n"
  "v_mfma_f32_16x16x32_bf16 v[140:143], a[220:223], v[56:59], v[140:143]\n"
  "s_waitcnt lgkmcnt(4)\n"
  "buffer_load_dwordx4 v203, %[srdV], s62 offen offset:1024 lds\n"
  "v_mfma_f32_16x16x32_bf16 a[0:3], v[4:7], v[144:147], 0\n"
  "v_max3_f32 v208, v136, v137, v138\n"
  "s_nop 3\n"
  "v_max3_f32 v208, v208, v139, v140\n"
  "v_max3_f32 v208, v208, v141, v142\n"
  "v_max_f32 v208, v208, v143\n"
  "v_mov_b32 v209, v208\n"
  "s_nop 1\n"
  "v_permlane16_swap_b32 v208, v209\n"
  "v_max_f32 v208, v208, v209\n"
  "v_mov_b32 v209, v208\n"
  "s_nop 1\n"
  "v_permlane32_swap_b32 v208, v209\n"
  "v_max_f32 v208, v208, v209\n"
  "v_sub_f32 v136, v136, v208\n"
  "v_sub_f32 v137, v137, v208\n"
  "v_sub_f32 v138, v138, v208\n"
  "v_sub_f32 v139, v139, v208\n"
  "v_sub_f32 v140, v140, v208\n"
  "v_sub_f32 v141, v141, v208\n"
  "v_sub_f32 v142, v142, v208\n"
  "v_sub_f32 v143, v143, v208\n"
  "v_sub_f32 v156, 0, v208\n"
  "v_sub_f32 v157, 0, v208\n"
  "v_sub_f32 v158, 0, v208\n"
  "v_sub_f32 v159, 0, v208\n"
  "v_exp_f32 v136, v136\n"
  "v_mfma_f32_16x16x32_bf16 a[4:7], v[12:15], v[144:147], 0\n"
  "ds_read_b64_tr_b16 v[76:77], v197 offset:8192\n"
  "v_mfma_f32_16x16x32_bf16 a[8:11], v[20:23], v[144:147], 0\n"
  "v_exp_f32 v137, v137\n"
  "v_mfma_f32_16x16x32_bf16 a[12:15], v[28:31], v[144:147], 0\n"
  "ds_read_b64_tr_b16 v[78:79], v197 offset:12288\n"
  "v_cvt_pk_bf16_f32 v148, v136, v137\n"
  "v_mfma_f32_16x16x32_bf16 a[16:19], v[36:39], v[144:147], 0\n"
  "v_exp_f32 v138, v138\n"
  "v_mfma_f32_16x16x32_bf16 a[20:23], v[44:47], v[144:147], 0\n"
  "ds_read_b128 a[240:243], v192 offset:12288\n"
  "v_mfma_f32_16x16x32_bf16 a[24:27], v[52:55], v[144:147], 0\n"
  "v_exp_f32 v139, v139\n"
  "v_mfma_f32_16x16x32_bf16 a[28:31], v[60:63], v[144:147], 0\n"
  "v_cvt_pk_bf16_f32 v149, v138, v139\n"
  "v_mfma_f32_16x16x32_bf16 a[128:131], v[168:171], v[144:147], 0\n"
  "v_exp_f32 v140, v140\n"
  "v_mfma_f32_16x16x32_bf16 v[128:131], a[192:195], v[64:67], 0\n"
  "ds_read_b64_tr_b16 v[84:85], v196 offset:8704\n"
  "v_mfma_f32_16x16x32_bf16 v[128:131], a[196:199], v[72:75], v[128:131]\n"
  "v_exp_f32 v141, v141\n"
  "v_mfma_f32_16x16x32_bf16 v[128:131], a[200:203], v[80:83], v[128:131]\n"
  "ds_read_b64_tr_b16 v[86:87], v196 offset:12800\n"
  "v_cvt_pk_bf16_f32 v150, v140, v141\n"
  "v_mfma_f32_16x16x32_bf16 v[128:131], a[204:207], v[88:91], v[128:131]\n"
  "v_exp_f32 v142, v142\n"
  "v_mfma_f32_16x16x32_bf16 v[132:135], a[208:211], v[64:67], 0\n"
  "ds_read_b128 a[244:247], v193 offset:12288\n"
  "v_mfma_f32_16x16x32_bf16 v[132:135], a[212:215], v[72:75], v[132:135]\n"
  "v_exp_f32 v143, v143\n"
  "v_mfma_f32_16x16x32_bf16 v[132:135], a[216:219], v[80:83], v[132:135]\n"
  "v_cvt_pk_bf16_f32 v151, v142, v143\n"
  "v_mfma_f32_16x16x32_bf16 v[132:135], a[220:223], v[88:91], v[132:135]\n"
  "buffer_load_dwordx4 v202, %[srdV], s62 offen offset:2048 lds\n"
  "v_mfma_f32_16x16x32_bf16 a[32:35], v[4:7], v[148:151], 0\n"
  "v_max3_f32 v208, v128, v129, v130\n"
  "s_nop 4\n"
  "v_max3_f32 v208, v208, v131, v132\n"
  "v_max3_f32 v208, v208, v133, v134\n"
  "v_max_f32 v208, v208, v135\n"
  "v_mov_b32 v209, v208\n"
  "s_nop 1\n"
  "v_permlane16_swap_b32 v208, v209\n"
  "v_max_f32 v208, v208, v209\n"
  "v_mov_b32 v209, v208\n"
  "s_nop 1\n"
  "v_permlane32_swap_b32 v208, v209\n"
  "v_max_f32 v208, v208, v209\n"
  "v_sub_f32 v128, v128, v208\n"
  "v_sub_f32 v129, v129, v208\n"
  "v_sub_f32 v130, v130, v208\n"
  "v_sub_f32 v131, v131, v208\n"
  "v_sub_f32 v132, v132, v208\n"
  "v_sub_f32 v133, v133, v208\n"
  "v_sub_f32 v134, v134, v208\n"
  "v_sub_f32 v135, v135, v208\n"
  "v_sub_f32 v160, 0, v208\n"
  "v_sub_f32 v161, 0, v208\n"
  "v_sub_f32 v162, 0, v208\n"
  "v_sub_f32 v163, 0, v208\n"
  "v_exp_f32 v128, v128\n"
  "v_mfma_f32_16x16x32_bf16 a[36:39], v[12:15], v[148:151], 0\n"
  "ds_read_b64_tr_b16 v[92:93], v197 offset:8704\n"
  "v_mfma_f32_16x16x32_bf16 a[40:43], v[20:23], v[148:151], 0\n"
  "v_exp_f32 v129, v129\n"
  "v_mfma_f32_16x16x32_bf16 a[44:47], v[28:31], v[148:151], 0\n"
  "ds_read_b64_tr_b16 v[94:95], v197 offset:12800\n"
  "v_cvt_pk_bf16_f32 v144, v128, v129\n"
  "v_mfma_f32_16x16x32_bf16 a[48:51], v[36:39], v[148:151], 0\n"
  "v_exp_f32 v130, v130\n"
  "v_mfma_f32_16x16x32_bf16 a[52:55], v[44:47], v[148:151], 0\n"
  "ds_read_b128 a[248:251], v194 offset:12288\n"
  "v_mfma_f32_16x16x32_bf16 a[56:59], v[52:55], v[148:151], 0\n"
  "v_exp_f32 v131, v131\n"
  "v_mfma_f32_16x16x32_bf16 a[60:63], v[60:63], v[148:151], 0\n"
  "v_cvt_pk_bf16_f32 v145, v130, v131\n"
  "v_mfma_f32_16x16x32_bf16 a[132:135], v[168:171], v[148:151], 0\n"
  "v_exp_f32 v132, v132\n"
  "v_mfma_f32_16x16x32_bf16 v[136:139], a[192:195], v[96:99], 0\n"
  "ds_read_b64_tr_b16 v[100:101], v196 offset:9216\n"
  "v_mfma_f32_16x16x32_bf16 v[136:139], a[196:199], v[104:107], v[136:139]\n"
  "v_exp_f32 v133, v133\n"
  "v_mfma_f32_16x16x32_bf16 v[136:139], a[200:203], v[112:115], v[136:139]\n"
  "ds_read_b64_tr_b16 v[102:103], v196 offset:13312\n"
  "v_cvt_pk_bf16_f32 v146, v132, v133\n"
  "v_mfma_f32_16x16x32_bf16 v[136:139], a[204:207], v[120:123], v[136:139]\n"
  "v_exp_f32 v134, v134\n"
  "v_mfma_f32_16x16x32_bf16 v[140:143], a[208:211], v[96:99], 0\n"
  "ds_read_b128 a[252:255], v195 offset:12288\n"
  "v_mfma_f32_16x16x32_bf16 v[140:143], a[212:215], v[104:107], v[140:143]\n"
  "v_exp_f32 v135, v135\n"
  "v_mfma_f32_16x16x32_bf16 v[140:143], a[216:219], v[112:115], v[140:143]\n"
  "s_waitcnt vmcnt(7)\n"
  "s_barrier\n"
  "v_cvt_pk_bf16_f32 v147, v134, v135\n"
  "v_mfma_f32_16x16x32_bf16 v[140:143], a[220:223], v[120:123], v[140:143]\n"
  "buffer_load_dwordx4 v203, %[srdV], s62 offen offset:3072 lds\n"
  "v_mfma_f32_16x16x32_bf16 a[64:67], v[4:7], v[144:147], 0\n"
  "v_max3_f32 v208, v136, v137, v138\n"
  "s_nop 4\n"
  "v_max3_f32 v208, v208, v139, v140\n"
  "v_max3_f32 v208, v208, v141, v142\n"
  "v_max_f32 v208, v208, v143\n"
  "v_mov_b32 v209, v208\n"
  "s_nop 1\n"
  "v_permlane16_swap_b32 v208, v209\n"
  "v_max_f32 v208, v208, v209\n"
  "v_mov_b32 v209, v208\n"
  "s_nop 1\n"
  "v_permlane32_swap_b32 v208, v209\n"
  "v_max_f32 v208, v208, v209\n"
  "v_sub_f32 v136, v136, v208\n"
  "v_sub_f32 v137, v137, v208\n"
  "v_sub_f32 v138, v138, v208\n"
  "v_sub_f32 v139, v139, v208\n"
  "v_sub_f32 v140, v140, v208\n"
  "v_sub_f32 v141, v141, v208\n"
  "v_sub_f32 v142, v142, v208\n"
  "v_sub_f32 v143, v143, v208\n"
  "v_sub_f32 v164, 0, v208\n"
  "v_sub_f32 v165, 0, v208\n"
  "v_sub_f32 v166, 0, v208\n"
  "v_sub_f32 v167, 0, v208\n"
  "v_exp_f32 v136, v136\n"
  "v_mfma_f32_16x16x32_bf16 a[68:71], v[12:15], v[144:147], 0\n"
  "ds_read_b64_tr_b16 v[108:109], v197 offset:9216\n"
  "v_mfma_f32_16x16x32_bf16 a[72:75], v[20:23], v[144:147], 0\n"
  "v_exp_f32 v137, v137\n"
  "v_mfma_f32_16x16x32_bf16 a[76:79], v[28:31], v[144:147], 0\n"
  "ds_read_b64_tr_b16 v[110:111], v197 offset:13312\n"
  "v_cvt_pk_bf16_f32 v148, v136, v137\n"
  "v_mfma_f32_16x16x32_bf16 a[80:83], v[36:39], v[144:147], 0\n"
  "v_exp_f32 v138, v138\n"
  "v_mfma_f32_16x16x32_bf16 a[84:87], v[44:47], v[144:147], 0\n"
  "ds_read_b128 a[192:195], v192 offset:16384\n"
  "v_mfma_f32_16x16x32_bf16 a[88:91], v[52:55], v[144:147], 0\n"
  "v_exp_f32 v139, v139\n"
  "v_mfma_f32_16x16x32_bf16 a[92:95], v[60:63], v[144:147], 0\n"
  "v_cvt_pk_bf16_f32 v149, v138, v139\n"
  "v_mfma_f32_16x16x32_bf16 a[136:139], v[168:171], v[144:147], 0\n"
  "s_waitcnt lgkmcnt(3)\n"
  "v_exp_f32 v140, v140\n"
  "v_mfma_f32_16x16x32_bf16 v[128:131], a[224:227], v[0:3], v[152:155]\n"
  "ds_read_b64_tr_b16 v[116:117], v196 offset:9728\n"
  "v_mfma_f32_16x16x32_bf16 v[128:131], a[228:231], v[8:11], v[128:131]\n"
  "v_exp_f32 v141, v141\n"
  "v_mfma_f32_16x16x32_bf16 v[128:131], a[232:235], v[16:19], v[128:131]\n"
  "ds_read_b64_tr_b16 v[118:119], v196 offset:13824\n"
  "v_cvt_pk_bf16_f32 v150, v140, v141\n"
  "v_mfma_f32_16x16x32_bf16 v[128:131], a[236:239], v[24:27], v[128:131]\n"
  "s_add_u32 s62, s62, 16384\n"
  "v_exp_f32 v142, v142\n"
  "v_mfma_f32_16x16x32_bf16 v[132:135], a[240:243], v[0:3], v[152:155]\n"
  "ds_read_b128 a[196:199], v193 offset:16384\n"
  "v_mfma_f32_16x16x32_bf16 v[132:135], a[244:247], v[8:11], v[132:135]\n"
  "s_add_u32 m0, %[sM0], 49152\n"
  "v_exp_f32 v143, v143\n"
  "v_mfma_f32_16x16x32_bf16 v[132:135], a[248:251], v[16:19], v[132:135]\n"
  "v_cvt_pk_bf16_f32 v151, v142, v143\n"
  "v_mfma_f32_16x16x32_bf16 v[132:135], a[252:255], v[24:27], v[132:135]\n"
  "buffer_load_dwordx4 v198, %[srdK], s62 offen offset:0 lds\n"
  "v_mfma_f32_16x16x32_bf16 a[96:99], v[4:7], v[148:151], 0\n"
  "v_exp_f32 v128, v128\n"
  "v_mfma_f32_16x16x32_bf16 a[100:103], v[12:15], v[148:151], 0\n"
  "ds_read_b64_tr_b16 v[124:125], v197 offset:9728\n"
  "v_mfma_f32_16x16x32_bf16 a[104:107], v[20:23], v[148:151], 0\n"
  "v_exp_f32 v129, v129\n"
  "v_mfma_f32_16x16x32_bf16 a[108:111], v[28:31], v[148:151], 0\n"
  "ds_read_b64_tr_b16 v[126:127], v197 offset:13824\n"
  "v_cvt_pk_bf16_f32 v144, v128, v129\n"
  "v_mfma_f32_16x16x32_bf16 a[112:115], v[36:39], v[148:151], 0\n"
  "v_exp_f32 v130, v130\n"
  "v_mfma_f32_16x16x32_bf16 a[116:119], v[44:47], v[148:151], 0\n"
  "ds_read_b128 a[200:203], v194 offset:16384\n"
  "v_mfma_f32_16x16x32_bf16 a[120:123], v[52:55], v[148:151], 0\n"
  "v_exp_f32 v131, v131\n"
  "v_mfma_f32_16x16x32_bf16 a[124:127], v[60:63], v[148:151], 0\n"
  "v_cvt_pk_bf16_f32 v145, v130, v131\n"
  "v_mfma_f32_16x16x32_bf16 a[140:143], v[168:171], v[148:151], 0\n"
  "v_exp_f32 v132, v132\n"
  "v_mfma_f32_16x16x32_bf16 v[136:139], a[224:227], v[32:35], v[156:159]\n"
  "ds_read_b64_tr_b16 v[4:5], v196 offset:16384\n"
  "v_mfma_f32_16x16x32_bf16 v[136:139], a[228:231], v[40:43], v[136:139]\n"
  "v_exp_f32 v133, v133\n"
  "v_mfma_f32_16x16x32_bf16 v[136:139], a[232:235], v[48:51], v[136:139]\n"
  "ds_read_b64_tr_b16 v[6:7], v196 offset:20480\n"
  "v_cvt_pk_bf16_f32 v146, v132, v133\n"
  "v_mfma_f32_16x16x32_bf16 v[136:139], a[236:239], v[56:59], v[136:139]\n"
  "v_exp_f32 v134, v134\n"
  "v_mfma_f32_16x16x32_bf16 v[140:143], a[240:243], v[32:35], v[156:159]\n"
  "ds_read_b128 a[204:207], v195 offset:16384\n"
  "v_mfma_f32_16x16x32_bf16 v[140:143], a[244:247], v[40:43], v[140:143]\n"
  "v_exp_f32 v135, v135\n"
  "v_mfma_f32_16x16x32_bf16 v[140:143], a[248:251], v[48:51], v[140:143]\n"
  "v_cvt_pk_bf16_f32 v147, v134, v135\n"
  "v_mfma_f32_16x16x32_bf16 v[140:143], a[252:255], v[56:59], v[140:143]\n"
  "s_waitcnt lgkmcnt(4)\n"
  "buffer_load_dwordx4 v199, %[srdK], s62 offen offset:1024 lds\n"
  "s_branch L_attn_in%=\n"
  "L_attn_top%=:\n"
  "v_mfma_f32_16x16x32_bf16 a[96:99], v[68:71], v[148:151], a[96:99]\n"
  "v_exp_f32 v128, v128\n"
  "v_mfma_f32_16x16x32_bf16 a[100:103], v[76:79], v[148:151], a[100:103]\n"
  "ds_read_b64_tr_b16 v[60:61], v197 offset:1536\n"
  "v_mfma_f32_16x16x32_bf16 a[104:107], v[84:87], v[148:151], a[104:107]\n"
  "v_exp_f32 v129, v129\n"
  "v_mfma_f32_16x16x32_bf16 a[108:111], v[92:95], v[148:151], a[108:111]\n"
  "ds_read_b64_tr_b16 v[62:63], v197 offset:5632\n"
  "v_cvt_pk_bf16_f32 v144, v128, v129\n"
  "v_mfma_f32_16x16x32_bf16 a[112:115], v[100:103], v[148:151], a[112:115]\n"
  "v_exp_f32 v130, v130\n"
  "v_mfma_f32_16x16x32_bf16 a[116:119], v[108:111], v[148:151], a[116:119]\n"
  "s_cmp_eq_u32 s63, 4\n"
  "s_cselect_b64 s[68:69], %[pP1], s[68:69]\n"
  "ds_read_b128 a[232:235], v194 offset:8192\n"
  "v_mfma_f32_16x16x32_bf16 a[120:123], v[116:119], v[148:151], a[120:123]\n"
  "v_exp_f32 v131, v131\n"
  "v_mfma_f32_16x16x32_bf16 a[124:127], v[124:127], v[148:151], a[124:127]\n"
  "v_cvt_pk_bf16_f32 v145, v130, v131\n"
  "v_mfma_f32_16x16x32_bf16 a[140:143], v[168:171], v[148:151], a[140:143]\n"
  "v_exp_f32 v132, v132\n"
  "v_mfma_f32_16x16x32_bf16 v[136:139], a[192:195], v[32:35], v[156:159]\n"
  "ds_read_b64_tr_b16 v[68:69], v196 offset:8192\n"
  "v_mfma_f32_16x16x32_bf16 v[136:139], a[196:199], v[40:43], v[136:139]\n"
  "v_exp_f32 v133, v133\n"
  "v_mfma_f32_16x16x32_bf16 v[136:139], a[200:203], v[48:51], v[136:139]\n"
  "ds_read_b64_tr_b16 v[70:71], v196 offset:12288\n"
  "v_cvt_pk_bf16_f32 v146, v132, v133\n"
  "v_mfma_f32_16x16x32_bf16 v[136:139], a[204:207], v[56:59], v[136:139]\n"
  "v_exp_f32 v134, v134\n"
  "v_mfma_f32_16x16x32_bf16 v[140:143], a[208:211], v[32:35], v[156:159]\n"
  "ds_read_b128 a[236:239], v195 offset:8192\n"
  "v_mfma_f32_16x16x32_bf16 v[140:143], a[212:215], v[40:43], v[140:143]\n"
  "v_exp_f32 v135, v135\n"
  "v_mfma_f32_16x16x32_bf16 v[140:143], a[216:219], v[48:51], v[140:143]\n"
  "v_cvt_pk_bf16_f32 v147, v134, v135\n"
  "v_mfma_f32_16x16x32_bf16 v[140:143], a[220:223], v[56:59], v[140:143]\n"
  "s_waitcnt lgkmcnt(4)\n"
  "buffer_load_dwordx4 v203, %[srdV], s62 offen offset:1024 lds\n"
  "v_mfma_f32_16x16x32_bf16 a[0:3], v[4:7], v[144:147], a[0:3]\n"
  "v_exp_f32 v136, v136\n"
  "v_mfma_f32_16x16x32_bf16 a[4:7], v[12:15], v[144:147], a[4:7]\n"
  "ds_read_b64_tr_b16 v[76:77], v197 offset:8192\n"
  "v_mfma_f32_16x16x32_bf16 a[8:11], v[20:23], v[144:147], a[8:11]\n"
  "v_exp_f32 v137, v137\n"
  "v_mfma_f32_16x16x32_bf16 a[12:15], v[28:31], v[144:147], a[12:15]\n"
  "ds_read_b64_tr_b16 v[78:79], v197 offset:12288\n"
  "v_cvt_pk_bf16_f32 v148, v136, v137\n"
  "v_mfma_f32_16x16x32_bf16 a[16:19], v[36:39], v[144:147], a[16:19]\n"
  "v_exp_f32 v138, v138\n"
  "v_mfma_f32_16x16x32_bf16 a[20:23], v[44:47], v[144:147], a[20:23]\n"
  "s_cmp_eq_u32 s63, 5\n"
  "s_cselect_b64 s[68:69], %[pP2], s[68:69]\n"
  "ds_read_b128 a[240:243], v192 offset:12288\n"
  "v_mfma_f32_16x16x32_bf16 a[24:27], v[52:55], v[144:147], a[24:27]\n"
  "v_exp_f32 v139, v139\n"
  "v_mfma_f32_16x16x32_bf16 a[28:31], v[60:63], v[144:147], a[28:31]\n"
  "v_cvt_pk_bf16_f32 v149, v138, v139\n"
  "v_mfma_f32_16x16x32_bf16 a[128:131], v[168:171], v[144:147], a[128:131]\n"
  "v_exp_f32 v140, v140\n"
  "v_mfma_f32_16x16x32_bf16 v[128:131], a[192:195], v[64:67], v[160:163]\n"
  "ds_read_b64_tr_b16 v[84:85], v196 offset:8704\n"
  "v_mfma_f32_16x16x32_bf16 v[128:131], a[196:199], v[72:75], v[128:131]\n"
  "v_exp_f32 v141, v141\n"
  "v_mfma_f32_16x16x32_bf16 v[128:131], a[200:203], v[80:83], v[128:131]\n"
  "ds_read_b64_tr_b16 v[86:87], v196 offset:12800\n"
  "v_cvt_pk_bf16_f32 v150, v140, v141\n"
  "v_mfma_f32_16x16x32_bf16 v[128:131], a[204:207], v[88:91], v[128:131]\n"
  "v_exp_f32 v142, v142\n"
  "v_mfma_f32_16x16x32_bf16 v[132:135], a[208:211], v[64:67], v[160:163]\n"
  "ds_read_b128 a[244:247], v193 offset:12288\n"
  "v_mfma_f32_16x16x32_bf16 v[132:135], a[212:215], v[72:75], v[132:135]\n"
  "v_exp_f32 v143, v143\n"
  "v_mfma_f32_16x16x32_bf16 v[132:135], a[216:219], v[80:83], v[132:135]\n"
  "v_cvt_pk_bf16_f32 v151, v142, v143\n"
  "v_mfma_f32_16x16x32_bf16 v[132:135], a[220:223], v[88:91], v[132:135]\n"
  "buffer_load_dwordx4 v202, %[srdV], s62 offen offset:2048 lds\n"
  "v_mfma_f32_16x16x32_bf16 a[32:35], v[4:7], v[148:151], a[32:35]\n"
  "v_exp_f32 v128, v128\n"
  "v_mfma_f32_16x16x32_bf16 a[36:39], v[12:15], v[148:151], a[36:39]\n"
  "ds_read_b64_tr_b16 v[92:93], v197 offset:8704\n"
  "v_mfma_f32_16x16x32_bf16 a[40:43], v[20:23], v[148:151], a[40:43]\n"
  "v_exp_f32 v129, v129\n"
  "v_mfma_f32_16x16x32_bf16 a[44:47], v[28:31], v[148:151], a[44:47]\n"
  "ds_read_b64_tr_b16 v[94:95], v197 offset:12800\n"
  "v_cvt_pk_bf16_f32 v144, v128, v129\n"
  "v_mfma_f32_16x16x32_bf16 a[48:51], v[36:39], v[148:151], a[48:51]\n"
  "v_exp_f32 v130, v130\n"
  "v_mfma_f32_16x16x32_bf16 a[52:55], v[44:47], v[148:151], a[52:55]\n"
  "s_cmp_eq_u32 s63, 6\n"
  "s_cselect_b64 s[68:69], %[pP3], s[68:69]\n"
  "ds_read_b128 a[248:251], v194 offset:12288\n"
  "v_mfma_f32_16x16x32_bf16 a[56:59], v[52:55], v[148:151], a[56:59]\n"
  "v_exp_f32 v131, v131\n"
  "v_mfma_f32_16x16x32_bf16 a[60:63], v[60:63], v[148:151], a[60:63]\n"
  "v_cvt_pk_bf16_f32 v145, v130, v131\n"
  "v_mfma_f32_16x16x32_bf16 a[132:135], v[168:171], v[148:151], a[132:135]\n"
  "v_exp_f32 v132, v132\n"
  "v_mfma_f32_16x16x32_bf16 v[136:139], a[192:195], v[96:99], v[164:167]\n"
  "ds_read_b64_tr_b16 v[100:101], v196 offset:9216\n"
  "v_mfma_f32_16x16x32_bf16 v[136:139], a[196:199], v[104:107], v[136:139]\n"
  "v_exp_f32 v133, v133\n"
  "v_mfma_f32_16x16x32_bf16 v[136:139], a[200:203], v[112:115], v[136:139]\n"
  "ds_read_b64_tr_b16 v[102:103], v196 offset:13312\n"
  "v_cvt_pk_bf16_f32 v146, v132, v133\n"
  "v_mfma_f32_16x16x32_bf16 v[136:139], a[204:207], v[120:123], v[136:139]\n"
  "v_exp_f32 v134, v134\n"
  "v_mfma_f32_16x16x32_bf16 v[140:143], a[208:211], v[96:99], v[164:167]\n"
  "ds_read_b128 a[252:255], v195 offset:12288\n"
  "v_mfma_f32_16x16x32_bf16 v[140:143], a[212:215], v[104:107], v[140:143]\n"
  "v_exp_f32 v135, v135\n"
  "v_mfma_f32_16x16x32_bf16 v[140:143], a[216:219], v[112:115], v[140:143]\n"
  "s_waitcnt vmcnt(8)\n"
  "s_barrier\n"
  "v_cvt_pk_bf16_f32 v147, v134, v135\n"
  "v_mfma_f32_16x16x32_bf16 v[140:143], a[220:223], v[120:123], v[140:143]\n"
  "buffer_load_dwordx4 v203, %[srdV], s62 offen offset:3072 lds\n"
  "v_mfma_f32_16x16x32_bf16 a[64:67], v[4:7], v[144:147], a[64:67]\n"
  "v_exp_f32 v136, v136\n"
  "v_mfma_f32_16x16x32_bf16 a[68:71], v[12:15], v[144:147], a[68:71]\n"
  "ds_read_b64_tr_b16 v[108:109], v197 offset:9216\n"
  "v_mfma_f32_16x16x32_bf16 a[72:75], v[20:23], v[144:147], a[72:75]\n"
  "v_exp_f32 v137, v137\n"
  "v_mfma_f32_16x16x32_bf16 a[76:79], v[28:31], v[144:147], a[76:79]\n"
  "ds_read_b64_tr_b16 v[110:111], v197 offset:13312\n"
  "v_cvt_pk_bf16_f32 v148, v136, v137\n"
  "v_mfma_f32_16x16x32_bf16 a[80:83], v[36:39], v[144:147], a[80:83]\n"
  "v_exp_f32 v138, v138\n"
  "v_mfma_f32_16x16x32_bf16 a[84:87], v[44:47], v[144:147], a[84:87]\n"
  "s_cmp_eq_u32 s63, 7\n"
  "s_cselect_b64 s[68:69], %[pP4], s[68:69]\n"
  "ds_read_b128 a[192:195], v192 offset:16384\n"
  "v_mfma_f32_16x16x32_bf16 a[88:91], v[52:55], v[144:147], a[88:91]\n"
  "v_exp_f32 v139, v139\n"
  "v_mfma_f32_16x16x32_bf16 a[92:95], v[60:63], v[144:147], a[92:95]\n"
  "v_cvt_pk_bf16_f32 v149, v138, v139\n"
  "v_mfma_f32_16x16x32_bf16 a[136:139], v[168:171], v[144:147], a[136:139]\n"
  "s_waitcnt lgkmcnt(3)\n"
  "v_exp_f32 v140, v140\n"
  "v_mfma_f32_16x16x32_bf16 v[128:131], a[224:227], v[0:3], v[152:155]\n"
  "ds_read_b64_tr_b16 v[116:117], v196 offset:9728\n"
  "v_mfma_f32_16x16x32_bf16 v[128:131], a[228:231], v[8:11], v[128:131]\n"
  "v_exp_f32 v141, v141\n"
  "v_mfma_f32_16x16x32_bf16 v[128:131], a[232:235], v[16:19], v[128:131]\n"
  "ds_read_b64_tr_b16 v[118:119], v196 offset:13824\n"
  "v_cvt_pk_bf16_f32 v150, v140, v141\n"
  "v_mfma_f32_16x16x32_bf16 v[128:131], a[236:239], v[24:27], v[128:131]\n"
  "s_add_u32 s62, s62, 16384\n"
  "v_exp_f32 v142, v142\n"
  "v_mfma_f32_16x16x32_bf16 v[132:135], a[240:243], v[0:3], v[152:155]\n"
  "ds_read_b128 a[196:199], v193 offset:16384\n"
  "v_mfma_f32_16x16x32_bf16 v[132:135], a[244:247], v[8:11], v[132:135]\n"
  "s_add_u32 m0, %[sM0], 49152\n"
  "v_exp_f32 v143, v143\n"
  "v_mfma_f32_16x16x32_bf16 v[132:135], a[248:251], v[16:19], v[132:135]\n"
  "v_cvt_pk_bf16_f32 v151, v142, v143\n"
  "v_mfma_f32_16x16x32_bf16 v[132:135], a[252:255], v[24:27], v[132:135]\n"
  "buffer_load_dwordx4 v198, %[srdK], s62 offen offset:0 lds\n"
  "v_mfma_f32_16x16x32_bf16 a[96:99], v[4:7], v[148:151], a[96:99]\n"
  "v_exp_f32 v128, v128\n"
  "v_mfma_f32_16x16x32_bf16 a[100:103], v[12:15], v[148:151], a[100:103]\n"
  "ds_read_b64_tr_b16 v[124:125], v197 offset:9728\n"
  "v_mfma_f32_16x16x32_bf16 a[104:107], v[20:23], v[148:151], a[104:107]\n"
  "v_exp_f32 v129, v129\n"
  "v_mfma_f32_16x16x32_bf16 a[108:111], v[28:31], v[148:151], a[108:111]\n"
  "ds_read_b64_tr_b16 v[126:127], v197 offset:13824\n"
  "v_cvt_pk_bf16_f32 v144, v128, v129\n"
  "v_mfma_f32_16x16x32_bf16 a[112:115], v[36:39], v[148:151], a[112:115]\n"
  "v_exp_f32 v130, v130\n"
  "v_mfma_f32_16x16x32_bf16 a[116:119], v[44:47], v[148:151], a[116:119]\n"
  "ds_read_b128 a[200:203], v194 offset:16384\n"
  "v_mfma_f32_16x16x32_bf16 a[120:123], v[52:55], v[148:151], a[120:123]\n"
  "v_exp_f32 v131, v131\n"
  "v_mfma_f32_16x16x32_bf16 a[124:127], v[60:63], v[148:151], a[124:127]\n"
  "v_cvt_pk_bf16_f32 v145, v130, v131\n"
  "v_mfma_f32_16x16x32_bf16 a[140:143], v[168:171], v[148:151], a[140:143]\n"
  "v_exp_f32 v132, v132\n"
  "v_mfma_f32_16x16x32_bf16 v[136:139], a[224:227], v[32:35], v[156:159]\n"
  "ds_read_b64_tr_b16 v[4:5], v196 offset:16384\n"
  "v_mfma_f32_16x16x32_bf16 v[136:139], a[228:231], v[40:43], v[136:139]\n"
  "v_exp_f32 v133, v133\n"
  "v_mfma_f32_16x16x32_bf16 v[136:139], a[232:235], v[48:51], v[136:139]\n"
  "ds_read_b64_tr_b16 v[6:7], v196 offset:20480\n"
  "v_cvt_pk_bf16_f32 v146, v132, v133\n"
  "v_mfma_f32_16x16x32_bf16 v[136:139], a[236:239], v[56:59], v[136:139]\n"
  "v_exp_f32 v134, v134\n"
  "v_mfma_f32_16x16x32_bf16 v[140:143], a[240:243], v[32:35], v[156:159]\n"
  "ds_read_b128 a[204:207], v195 offset:16384\n"
  "v_mfma_f32_16x16x32_bf16 v[140:143], a[244:247], v[40:43], v[140:143]\n"
  "v_exp_f32 v135, v135\n"
  "v_mfma_f32_16x16x32_bf16 v[140:143], a[248:251], v[48:51], v[140:143]\n"
  "v_cvt_pk_bf16_f32 v147, v134, v135\n"
  "v_mfma_f32_16x16x32_bf16 v[140:143], a[252:255], v[56:59], v[140:143]\n"
  "s_waitcnt lgkmcnt(4)\n"
  "buffer_load_dwordx4 v199, %[srdK], s62 offen offset:1024 lds\n"
  "L_attn_in%=:\n"
  "v_mfma_f32_16x16x32_bf16 a[0:3], v[68:71], v[144:147], a[0:3]\n"
  "v_exp_f32 v136, v136\n"
  "v_mfma_f32_16x16x32_bf16 a[4:7], v[76:79], v[144:147], a[4:7]\n"
  "ds_read_b64_tr_b16 v[12:13], v197 offset:16384\n"
  "v_mfma_f32_16x16x32_bf16 a[8:11], v[84:87], v[144:147], a[8:11]\n"
  "v_exp_f32 v137, v137\n"
  "v_mfma_f32_16x16x32_bf16 a[12:15], v[92:95], v[144:147], a[12:15]\n"
  "ds_read_b64_tr_b16 v[14:15], v197 offset:20480\n"
  "v_cvt_pk_bf16_f32 v148, v136, v137\n"
  "v_mfma_f32_16x16x32_bf16 a[16:19], v[100:103], v[144:147], a[16:19]\n"
  "v_exp_f32 v138, v138\n"
  "v_mfma_f32_16x16x32_bf16 a[20:23], v[108:111], v[144:147], a[20:23]\n"
  "ds_read_b128 a[208:211], v192 offset:20480\n"
  "v_mfma_f32_16x16x32_bf16 a[24:27], v[116:119], v[144:147], a[24:27]\n"
  "v_exp_f32 v139, v139\n"
  "v_mfma_f32_16x16x32_bf16 a[28:31], v[124:127], v[144:147], a[28:31]\n"
  "global_load_dwordx4 v[216:219], %[vL16], s[68:69] offset:0\n"
  "v_cvt_pk_bf16_f32 v149, v138, v139\n"
  "v_mfma_f32_16x16x32_bf16 a[128:131], v[168:171], v[144:147], a[128:131]\n"
  "v_exp_f32 v140, v140\n"
  "v_mfma_f32_16x16x32_bf16 v[128:131], a[224:227], v[64:67], v[160:163]\n"
  "ds_read_b64_tr_b16 v[20:21], v196 offset:16896\n"
  "v_mfma_f32_16x16x32_bf16 v[128:131], a[228:231], v[72:75], v[128:131]\n"
  "v_exp_f32 v141, v141\n"
  "v_mfma_f32_16x16x32_bf16 v[128:131], a[232:235], v[80:83], v[128:131]\n"
  "ds_read_b64_tr_b16 v[22:23], v196 offset:20992\n"
  "v_cvt_pk_bf16_f32 v150, v140, v141\n"
  "v_mfma_f32_16x16x32_bf16 v[128:131], a[236:239], v[88:91], v[128:131]\n"
  "v_exp_f32 v142, v142\n"
  "v_mfma_f32_16x16x32_bf16 v[132:135], a[240:243], v[64:67], v[160:163]\n"
  "ds_read_b128 a[212:215], v193 offset:20480\n"
  "v_mfma_f32_16x16x32_bf16 v[132:135], a[244:247], v[72:75], v[132:135]\n"
  "v_exp_f32 v143, v143\n"
  "v_mfma_f32_16x16x32_bf16 v[132:135], a[248:251], v[80:83], v[132:135]\n"
  "v_cvt_pk_bf16_f32 v151, v142, v143\n"
  "v_mfma_f32_16x16x32_bf16 v[132:135], a[252:255], v[88:91], v[132:135]\n"
  "buffer_load_dwordx4 v200, %[srdK], s62 offen offset:2048 lds\n"
  "v_mfma_f32_16x16x32_bf16 a[32:35], v[68:71], v[148:151], a[32:35]\n"
  "v_exp_f32 v128, v128\n"
  "v_mfma_f32_16x16x32_bf16 a[36:39], v[76:79], v[148:151], a[36:39]\n"
  "ds_read_b64_tr_b16 v[28:29], v197 offset:16896\n"
  "v_mfma_f32_16x16x32_bf16 a[40:43], v[84:87], v[148:151], a[40:43]\n"
  "v_exp_f32 v129, v129\n"
  "v_mfma_f32_16x16x32_bf16 a[44:47], v[92:95], v[148:151], a[44:47]\n"
  "ds_read_b64_tr_b16 v[30:31], v197 offset:20992\n"
  "v_cvt_pk_bf16_f32 v144, v128, v129\n"
  "v_mfma_f32_16x16x32_bf16 a[48:51], v[100:103], v[148:151], a[48:51]\n"
  "v_exp_f32 v130, v130\n"
  "v_mfma_f32_16x16x32_bf16 a[52:55], v[108:111], v[148:151], a[52:55]\n"
  "ds_read_b128 a[216:219], v194 offset:20480\n"
  "v_mfma_f32_16x16x32_bf16 a[56:59], v[116:119], v[148:151], a[56:59]\n"
  "v_exp_f32 v131, v131\n"
  "v_mfma_f32_16x16x32_bf16 a[60:63], v[124:127], v[148:151], a[60:63]\n"
  "v_cvt_pk_bf16_f32 v145, v130, v131\n"
  "v_mfma_f32_16x16x32_bf16 a[132:135], v[168:171], v[148:151], a[132:135]\n"
  "v_exp_f32 v132, v132\n"
  "v_mfma_f32_16x16x32_bf16 v[136:139], a[224:227], v[96:99], v[164:167]\n"
  "ds_read_b64_tr_b16 v[36:37], v196 offset:17408\n"
  "v_mfma_f32_16x16x32_bf16 v[136:139], a[228:231], v[104:107], v[136:139]\n"
  "v_exp_f32 v133, v133\n"
  "v_mfma_f32_16x16x32_bf16 v[136:139], a[232:235], v[112:115], v[136:139]\n"
  "ds_read_b64_tr_b16 v[38:39], v196 offset:21504\n"
  "v_cvt_pk_bf16_f32 v146, v132, v133\n"
  "v_mfma_f32_16x16x32_bf16 v[136:139], a[236:239], v[120:123], v[136:139]\n"
  "v_exp_f32 v134, v134\n"
  "v_mfma_f32_16x16x32_bf16 v[140:143], a[240:243], v[96:99], v[164:167]\n"
  "ds_read_b128 a[220:223], v195 offset:20480\n"
  "v_mfma_f32_16x16x32_bf16 v[140:143], a[244:247], v[104:107], v[140:143]\n"
  "v_exp_f32 v135, v135\n"
  "v_mfma_f32_16x16x32_bf16 v[140:143], a[248:251], v[112:115], v[140:143]\n"
  "v_cvt_pk_bf16_f32 v147, v134, v135\n"
  "v_mfma_f32_16x16x32_bf16 v[140:143], a[252:255], v[120:123], v[140:143]\n"
  "buffer_load_dwordx4 v201, %[srdK], s62 offen offset:3072 lds\n"
  "v_mfma_f32_16x16x32_bf16 a[64:67], v[68:71], v[144:147], a[64:67]\n"
  "v_exp_f32 v136, v136\n"
  "v_mfma_f32_16x16x32_bf16 a[68:71], v[76:79], v[144:147], a[68:71]\n"
  "ds_read_b64_tr_b16 v[44:45], v197 offset:17408\n"
  "v_mfma_f32_16x16x32_bf16 a[72:75], v[84:87], v[144:147], a[72:75]\n"
  "v_exp_f32 v137, v137\n"
  "v_mfma_f32_16x16x32_bf16 a[76:79], v[92:95], v[144:147], a[76:79]\n"
  "ds_read_b64_tr_b16 v[46:47], v197 offset:21504\n"
  "v_cvt_pk_bf16_f32 v148, v136, v137\n"
  "v_mfma_f32_16x16x32_bf16 a[80:83], v[100:103], v[144:147], a[80:83]\n"
  "v_exp_f32 v138, v138\n"
  "v_mfma_f32_16x16x32_bf16 a[84:87], v[108:111], v[144:147], a[84:87]\n"
  "ds_read_b128 a[224:227], v192 offset:24576\n"
  "v_mfma_f32_16x16x32_bf16 a[88:91], v[116:119], v[144:147], a[88:91]\n"
  "v_exp_f32 v139, v139\n"
  "v_mfma_f32_16x16x32_bf16 a[92:95], v[124:127], v[144:147], a[92:95]\n"
  "v_cvt_pk_bf16_f32 v149, v138, v139\n"
  "v_mfma_f32_16x16x32_bf16 a[136:139], v[168:171], v[144:147], a[136:139]\n"
  "s_waitcnt lgkmcnt(3)\n"
  "v_exp_f32 v140, v140\n"
  "v_mfma_f32_16x16x32_bf16 v[128:131], a[192:195], v[0:3], v[152:155]\n"
  "ds_read_b64_tr_b16 v[52:53], v196 offset:17920\n"
  "v_mfma_f32_16x16x32_bf16 v[128:131], a[196:199], v[8:11], v[128:131]\n"
  "v_exp_f32 v141, v141\n"
  "v_mfma_f32_16x16x32_bf16 v[128:131], a[200:203], v[16:19], v[128:131]\n"
  "ds_read_b64_tr_b16 v[54:55], v196 offset:22016\n"
  "v_cvt_pk_bf16_f32 v150, v140, v141\n"
  "v_mfma_f32_16x16x32_bf16 v[128:131], a[204:207], v[24:27], v[128:131]\n"
  "v_exp_f32 v142, v142\n"
  "v_mfma_f32_16x16x32_bf16 v[132:135], a[208:211], v[0:3], v[152:155]\n"
  "ds_read_b128 a[228:231], v193 offset:24576\n"
  "v_mfma_f32_16x16x32_bf16 v[132:135], a[212:215], v[8:11], v[132:135]\n"
  "s_add_u32 m0, %[sM0], 114688\n"
  "v_exp_f32 v143, v143\n"
  "v_mfma_f32_16x16x32_bf16 v[132:135], a[216:219], v[16:19], v[132:135]\n"
  "v_cvt_pk_bf16_f32 v151, v142, v143\n"
  "v_mfma_f32_16x16x32_bf16 v[132:135], a[220:223], v[24:27], v[132:135]\n"
  "buffer_load_dwordx4 v202, %[srdV], s62 offen offset:0 lds\n"
  "v_mfma_f32_16x16x32_bf16 a[96:99], v[68:71], v[148:151], a[96:99]\n"
  "v_exp_f32 v128, v128\n"
  "v_mfma_f32_16x16x32_bf16 a[100:103], v[76:79], v[148:151], a[100:103]\n"
  "ds_read_b64_tr_b16 v[60:61], v197 offset:17920\n"
  "v_mfma_f32_16x16x32_bf16 a[104:107], v[84:87], v[148:151], a[104:107]\n"
  "v_exp_f32 v129, v129\n"
  "v_mfma_f32_16x16x32_bf16 a[108:111], v[92:95], v[148:151], a[108:111]\n"
  "ds_read_b64_tr_b16 v[62:63], v197 offset:22016\n"
  "v_cvt_pk_bf16_f32 v144, v128, v129\n"
  "v_mfma_f32_16x16x32_bf16 a[112:115], v[100:103], v[148:151], a[112:115]\n"
  "v_exp_f32 v130, v130\n"
  "v_mfma_f32_16x16x32_bf16 a[116:119], v[108:111], v[148:151], a[116:119]\n"
  "ds_read_b128 a[232:235], v194 offset:24576\n"
  "v_mfma_f32_16x16x32_bf16 a[120:123], v[116:119], v[148:151], a[120:123]\n"
  "v_exp_f32 v131, v131\n"
  "v_mfma_f32_16x16x32_bf16 a[124:127], v[124:127], v[148:151], a[124:127]\n"
  "v_cvt_pk_bf16_f32 v145, v130, v131\n"
  "v_mfma_f32_16x16x32_bf16 a[140:143], v[168:171], v[148:151], a[140:143]\n"
  "v_exp_f32 v132, v132\n"
  "v_mfma_f32_16x16x32_bf16 v[136:139], a[192:195], v[32:35], v[156:159]\n"
  "ds_read_b64_tr_b16 v[68:69], v196 offset:24576\n"
  "v_mfma_f32_16x16x32_bf16 v[136:139], a[196:199], v[40:43], v[136:139]\n"
  "v_exp_f32 v133, v133\n"
  "v_mfma_f32_16x16x32_bf16 v[136:139], a[200:203], v[48:51], v[136:139]\n"
  "ds_read_b64_tr_b16 v[70:71], v196 offset:28672\n"
  "v_cvt_pk_bf16_f32 v146, v132, v133\n"
  "v_mfma_f32_16x16x32_bf16 v[136:139], a[204:207], v[56:59], v[136:139]\n"
  "v_exp_f32 v134, v134\n"
  "v_mfma_f32_16x16x32_bf16 v[140:143], a[208:211], v[32:35], v[156:159]\n"
  "ds_read_b128 a[236:239], v195 offset:24576\n"
  "v_mfma_f32_16x16x32_bf16 v[140:143], a[212:215], v[40:43], v[140:143]\n"
  "v_exp_f32 v135, v135\n"
  "v_mfma_f32_16x16x32_bf16 v[140:143], a[216:219], v[48:51], v[140:143]\n"
  "v_cvt_pk_bf16_f32 v147, v134, v135\n"
  "v_mfma_f32_16x16x32_bf16 v[140:143], a[220:223], v[56:59], v[140:143]\n"
  "s_waitcnt lgkmcnt(4)\n"
  "buffer_load_dwordx4 v203, %[srdV], s62 offen offset:1024 lds\n"
  "v_mfma_f32_16x16x32_bf16 a[0:3], v[4:7], v[144:147], a[0:3]\n"
  "v_exp_f32 v136, v136\n"
  "v_mfma_f32_16x16x32_bf16 a[4:7], v[12:15], v[144:147], a[4:7]\n"
  "ds_read_b64_tr_b16 v[76:77], v197 offset:24576\n"
  "v_mfma_f32_16x16x32_bf16 a[8:11], v[20:23], v[144:147], a[8:11]\n"
  "v_exp_f32 v137, v137\n"
  "v_mfma_f32_16x16x32_bf16 a[12:15], v[28:31], v[144:147], a[12:15]\n"
  "ds_read_b64_tr_b16 v[78:79], v197 offset:28672\n"
  "v_cvt_pk_bf16_f32 v148, v136, v137\n"
  "v_mfma_f32_16x16x32_bf16 a[16:19], v[36:39], v[144:147], a[16:19]\n"
  "v_exp_f32 v138, v138\n"
  "v_mfma_f32_16x16x32_bf16 a[20:23], v[44:47], v[144:147], a[20:23]\n"
  "ds_read_b128 a[240:243], v192 offset:28672\n"
  "v_mfma_f32_16x16x32_bf16 a[24:27], v[52:55], v[144:147], a[24:27]\n"
  "v_exp_f32 v139, v139\n"
  "v_mfma_f32_16x16x32_bf16 a[28:31], v[60:63], v[144:147], a[28:31]\n"
  "v_cvt_pk_bf16_f32 v149, v138, v139\n"
  "v_mfma_f32_16x16x32_bf16 a[128:131], v[168:171], v[144:147], a[128:131]\n"
  "v_exp_f32 v140, v140\n"
  "v_mfma_f32_16x16x32_bf16 v[128:131], a[192:195], v[64:67], v[160:163]\n"
  "ds_read_b64_tr_b16 v[84:85], v196 offset:25088\n"
  "v_mfma_f32_16x16x32_bf16 v[128:131], a[196:199], v[72:75], v[128:131]\n"
  "v_exp_f32 v141, v141\n"
  "v_mfma_f32_16x16x32_bf16 v[128:131], a[200:203], v[80:83], v[128:131]\n"
  "ds_read_b64_tr_b16 v[86:87], v196 offset:29184\n"
  "v_cvt_pk_bf16_f32 v150, v140, v141\n"
  "v_mfma_f32_16x16x32_bf16 v[128:131], a[204:207], v[88:91], v[128:131]\n"
  "v_exp_f32 v142, v142\n"
  "v_mfma_f32_16x16x32_bf16 v[132:135], a[208:211], v[64:67], v[160:163]\n"
  "ds_read_b128 a[244:247], v193 offset:28672\n"
  "v_mfma_f32_16x16x32_bf16 v[132:135], a[212:215], v[72:75], v[132:135]\n"
  "v_exp_f32 v143, v143\n"
  "v_mfma_f32_16x16x32_bf16 v[132:135], a[216:219], v[80:83], v[132:135]\n"
  "v_cvt_pk_bf16_f32 v151, v142, v143\n"
  "v_mfma_f32_16x16x32_bf16 v[132:135], a[220:223], v[88:91], v[132:135]\n"
  "buffer_load_dwordx4 v202, %[srdV], s62 offen offset:2048 lds\n"
  "v_mfma_f32_16x16x32_bf16 a[32:35], v[4:7], v[148:151], a[32:35]\n"
  "v_exp_f32 v128, v128\n"
  "v_mfma_f32_16x16x32_bf16 a[36:39], v[12:15], v[148:151], a[36:39]\n"
  "ds_read_b64_tr_b16 v[92:93], v197 offset:25088\n"
  "v_mfma_f32_16x16x32_bf16 a[40:43], v[20:23], v[148:151], a[40:43]\n"
  "v_exp_f32 v129, v129\n"
  "v_mfma_f32_16x16x32_bf16 a[44:47], v[28:31], v[148:151], a[44:47]\n"
  "ds_read_b64_tr_b16 v[94:95], v197 offset:29184\n"
  "v_cvt_pk_bf16_f32 v144, v128, v129\n"
  "v_mfma_f32_16x16x32_bf16 a[48:51], v[36:39], v[148:151], a[48:51]\n"
  "v_exp_f32 v130, v130\n"
  "v_mfma_f32_16x16x32_bf16 a[52:55], v[44:47], v[148:151], a[52:55]\n"
  "ds_read_b128 a[248:251], v194 offset:28672\n"
  "v_mfma_f32_16x16x32_bf16 a[56:59], v[52:55], v[148:151], a[56:59]\n"
  "v_exp_f32 v131, v131\n"
  "v_mfma_f32_16x16x32_bf16 a[60:63], v[60:63], v[148:151], a[60:63]\n"
  "v_cvt_pk_bf16_f32 v145, v130, v131\n"
  "v_mfma_f32_16x16x32_bf16 a[132:135], v[168:171], v[148:151], a[132:135]\n"
  "v_exp_f32 v132, v132\n"
  "v_mfma_f32_16x16x32_bf16 v[136:139], a[192:195], v[96:99], v[164:167]\n"
  "ds_read_b64_tr_b16 v[100:101], v196 offset:25600\n"
  "v_mfma_f32_16x16x32_bf16 v[136:139], a[196:199], v[104:107], v[136:139]\n"
  "v_exp_f32 v133, v133\n"
  "v_mfma_f32_16x16x32_bf16 v[136:139], a[200:203], v[112:115], v[136:139]\n"
  "ds_read_b64_tr_b16 v[102:103], v196 offset:29696\n"
  "v_cvt_pk_bf16_f32 v146, v132, v133\n"
  "v_mfma_f32_16x16x32_bf16 v[136:139], a[204:207], v[120:123], v[136:139]\n"
  "v_exp_f32 v134, v134\n"
  "v_mfma_f32_16x16x32_bf16 v[140:143], a[208:211], v[96:99], v[164:167]\n"
  "ds_read_b128 a[252:255], v195 offset:28672\n"
  "v_mfma_f32_16x16x32_bf16 v[140:143], a[212:215], v[104:107], v[140:143]\n"
  "v_exp_f32 v135, v135\n"
  "v_mfma_f32_16x16x32_bf16 v[140:143], a[216:219], v[112:115], v[140:143]\n"
  "s_waitcnt vmcnt(8)\n"
  "s_barrier\n"
  "v_cvt_pk_bf16_f32 v147, v134, v135\n"
  "v_mfma_f32_16x16x32_bf16 v[140:143], a[220:223], v[120:123], v[140:143]\n"
  "buffer_load_dwordx4 v203, %[srdV], s62 offen offset:3072 lds\n"
  "v_mfma_f32_16x16x32_bf16 a[64:67], v[4:7], v[144:147], a[64:67]\n"
  "v_exp_f32 v136, v136\n"
  "v_mfma_f32_16x16x32_bf16 a[68:71], v[12:15], v[144:147], a[68:71]\n"
  "ds_read_b64_tr_b16 v[108:109], v197 offset:25600\n"
  "v_mfma_f32_16x16x32_bf16 a[72:75], v[20:23], v[144:147], a[72:75]\n"
  "v_exp_f32 v137, v137\n"
  "v_mfma_f32_16x16x32_bf16 a[76:79], v[28:31], v[144:147], a[76:79]\n"
  "ds_read_b64_tr_b16 v[110:111], v197 offset:29696\n"
  "v_cvt_pk_bf16_f32 v148, v136, v137\n"
  "v_mfma_f32_16x16x32_bf16 a[80:83], v[36:39], v[144:147], a[80:83]\n"
  "v_exp_f32 v138, v138\n"
  "v_mfma_f32_16x16x32_bf16 a[84:87], v[44:47], v[144:147], a[84:87]\n"
  "ds_read_b128 a[192:195], v192 offset:32768\n"
  "v_mfma_f32_16x16x32_bf16 a[88:91], v[52:55], v[144:147], a[88:91]\n"
  "v_exp_f32 v139, v139\n"
  "v_mfma_f32_16x16x32_bf16 a[92:95], v[60:63], v[144:147], a[92:95]\n"
  "v_cvt_pk_bf16_f32 v149, v138, v139\n"
  "v_mfma_f32_16x16x32_bf16 a[136:139], v[168:171], v[144:147], a[136:139]\n"
  "s_waitcnt lgkmcnt(3)\n"
  "v_exp_f32 v140, v140\n"
  "v_mfma_f32_16x16x32_bf16 v[128:131], a[224:227], v[0:3], v[152:155]\n"
  "ds_read_b64_tr_b16 v[116:117], v196 offset:26112\n"
  "v_mfma_f32_16x16x32_bf16 v[128:131], a[228:231], v[8:11], v[128:131]\n"
  "v_exp_f32 v141, v141\n"
  "v_mfma_f32_16x16x32_bf16 v[128:131], a[232:235], v[16:19], v[128:131]\n"
  "ds_read_b64_tr_b16 v[118:119], v196 offset:30208\n"
  "v_cvt_pk_bf16_f32 v150, v140, v141\n"
  "v_mfma_f32_16x16x32_bf16 v[128:131], a[236:239], v[24:27], v[128:131]\n"
  "s_add_u32 s62, s62, 16384\n"
  "v_exp_f32 v142, v142\n"
  "v_mfma_f32_16x16x32_bf16 v[132:135], a[240:243], v[0:3], v[152:155]\n"
  "ds_read_b128 a[196:199], v193 offset:32768\n"
  "v_mfma_f32_16x16x32_bf16 v[132:135], a[244:247], v[8:11], v[132:135]\n"
  "s_add_u32 m0, %[sM0], 0\n"
  "v_exp_f32 v143, v143\n"
  "v_mfma_f32_16x16x32_bf16 v[132:135], a[248:251], v[16:19], v[132:135]\n"
  "v_cvt_pk_bf16_f32 v151, v142, v143\n"
  "v_mfma_f32_16x16x32_bf16 v[132:135], a[252:255], v[24:27], v[132:135]\n"
  "buffer_load_dwordx4 v198, %[srdK], s62 offen offset:0 lds\n"
  "v_mfma_f32_16x16x32_bf16 a[96:99], v[4:7], v[148:151], a[96:99]\n"
  "v_exp_f32 v128, v128\n"
  "v_mfma_f32_16x16x32_bf16 a[100:103], v[12:15], v[148:151], a[100:103]\n"
  "ds_read_b64_tr_b16 v[124:125], v197 offset:26112\n"
  "v_mfma_f32_16x16x32_bf16 a[104:107], v[20:23], v[148:151], a[104:107]\n"
  "v_exp_f32 v129, v129\n"
  "v_mfma_f32_16x16x32_bf16 a[108:111], v[28:31], v[148:151], a[108:111]\n"
  "ds_read_b64_tr_b16 v[126:127], v197 offset:30208\n"
  "v_cvt_pk_bf16_f32 v144, v128, v129\n"
  "v_mfma_f32_16x16x32_bf16 a[112:115], v[36:39], v[148:151], a[112:115]\n"
  "v_exp_f32 v130, v130\n"
  "v_mfma_f32_16x16x32_bf16 a[116:119], v[44:47], v[148:151], a[116:119]\n"
  "ds_read_b128 a[200:203], v194 offset:32768\n"
  "v_mfma_f32_16x16x32_bf16 a[120:123], v[52:55], v[148:151], a[120:123]\n"
  "v_exp_f32 v131, v131\n"
  "v_mfma_f32_16x16x32_bf16 a[124:127], v[60:63], v[148:151], a[124:127]\n"
  "v_cvt_pk_bf16_f32 v145, v130, v131\n"
  "v_mfma_f32_16x16x32_bf16 a[140:143], v[168:171], v[148:151], a[140:143]\n"
  "v_exp_f32 v132, v132\n"
  "v_mfma_f32_16x16x32_bf16 v[136:139], a[224:227], v[32:35], v[156:159]\n"
  "ds_read_b64_tr_b16 v[4:5], v196 offset:32768\n"
  "v_mfma_f32_16x16x32_bf16 v[136:139], a[228:231], v[40:43], v[136:139]\n"
  "v_exp_f32 v133, v133\n"
  "v_mfma_f32_16x16x32_bf16 v[136:139], a[232:235], v[48:51], v[136:139]\n"
  "ds_read_b64_tr_b16 v[6:7], v196 offset:36864\n"
  "v_cvt_pk_bf16_f32 v146, v132, v133\n"
  "v_mfma_f32_16x16x32_bf16 v[136:139], a[236:239], v[56:59], v[136:139]\n"
  "v_exp_f32 v134, v134\n"
  "v_mfma_f32_16x16x32_bf16 v[140:143], a[240:243], v[32:35], v[156:159]\n"
  "ds_read_b128 a[204:207], v195 offset:32768\n"
  "v_mfma_f32_16x16x32_bf16 v[140:143], a[244:247], v[40:43], v[140:143]\n"
  "v_exp_f32 v135, v135\n"
  "v_mfma_f32_16x16x32_bf16 v[140:143], a[248:251], v[48:51], v[140:143]\n"
  "v_cvt_pk_bf16_f32 v147, v134, v135\n"
  "v_mfma_f32_16x16x32_bf16 v[140:143], a[252:255], v[56:59], v[140:143]\n"
  "s_waitcnt lgkmcnt(4)\n"
  "buffer_load_dwordx4 v199, %[srdK], s62 offen offset:1024 lds\n"
  "v_mfma_f32_16x16x32_bf16 a[0:3], v[68:71], v[144:147], a[0:3]\n"
  "v_exp_f32 v136, v136\n"
  "v_mfma_f32_16x16x32_bf16 a[4:7], v[76:79], v[144:147], a[4:7]\n"
  "ds_read_b64_tr_b16 v[12:13], v197 offset:32768\n"
  "v_mfma_f32_16x16x32_bf16 a[8:11], v[84:87], v[144:147], a[8:11]\n"
  "v_exp_f32 v137, v137\n"
  "v_mfma_f32_16x16x32_bf16 a[12:15], v[92:95], v[144:147], a[12:15]\n"
  "ds_read_b64_tr_b16 v[14:15], v197 offset:36864\n"
  "v_cvt_pk_bf16_f32 v148, v136, v137\n"
  "v_mfma_f32_16x16x32_bf16 a[16:19], v[100:103], v[144:147], a[16:19]\n"
  "v_exp_f32 v138, v138\n"
  "v_mfma_f32_16x16x32_bf16 a[20:23], v[108:111], v[144:147], a[20:23]\n"
  "ds_read_b128 a[208:211], v192 offset:36864\n"
  "v_mfma_f32_16x16x32_bf16 a[24:27], v[116:119], v[144:147], a[24:27]\n"
  "v_exp_f32 v139, v139\n"
  "v_mfma_f32_16x16x32_bf16 a[28:31], v[124:127], v[144:147], a[28:31]\n"
  "global_load_dwordx4 v[216:219], %[vL16], s[68:69] offset:1024\n"
  "v_cvt_pk_bf16_f32 v149, v138, v139\n"
  "v_mfma_f32_16x16x32_bf16 a[128:131], v[168:171], v[144:147], a[128:131]\n"
  "v_exp_f32 v140, v140\n"
  "v_mfma_f32_16x16x32_bf16 v[128:131], a[224:227], v[64:67], v[160:163]\n"
  "ds_read_b64_tr_b16 v[20:21], v196 offset:33280\n"
  "v_mfma_f32_16x16x32_bf16 v[128:131], a[228:231], v[72:75], v[128:131]\n"
  "v_exp_f32 v141, v141\n"
  "v_mfma_f32_16x16x32_bf16 v[128:131], a[232:235], v[80:83], v[128:131]\n"
  "ds_read_b64_tr_b16 v[22:23], v196 offset:37376\n"
  "v_cvt_pk_bf16_f32 v150, v140, v141\n"
  "v_mfma_f32_16x16x32_bf16 v[128:131], a[236:239], v[88:91], v[128:131]\n"
  "v_exp_f32 v142, v142\n"
  "v_mfma_f32_16x16x32_bf16 v[132:135], a[240:243], v[64:67], v[160:163]\n"
  "ds_read_b128 a[212:215], v193 offset:36864\n"
  "v_mfma_f32_16x16x32_bf16 v[132:135], a[244:247], v[72:75], v[132:135]\n"
  "v_exp_f32 v143, v143\n"
  "v_mfma_f32_16x16x32_bf16 v[132:135], a[248:251], v[80:83], v[132:135]\n"
  "v_cvt_pk_bf16_f32 v151, v142, v143\n"
  "v_mfma_f32_16x16x32_bf16 v[132:135], a[252:255], v[88:91], v[132:135]\n"
  "buffer_load_dwordx4 v200, %[srdK], s62 offen offset:2048 lds\n"
  "v_mfma_f32_16x16x32_bf16 a[32:35], v[68:71], v[148:151], a[32:35]\n"
  "v_exp_f32 v128, v128\n"
  "v_mfma_f32_16x16x32_bf16 a[36:39], v[76:79], v[148:151], a[36:39]\n"
  "ds_read_b64_tr_b16 v[28:29], v197 offset:33280\n"
  "v_mfma_f32_16x16x32_bf16 a[40:43], v[84:87], v[148:151], a[40:43]\n"
  "v_exp_f32 v129, v129\n"
  "v_mfma_f32_16x16x32_bf16 a[44:47], v[92:95], v[148:151], a[44:47]\n"
  "ds_read_b64_tr_b16 v[30:31], v197 offset:37376\n"
  "v_cvt_pk_bf16_f32 v144, v128, v129\n"
  "v_mfma_f32_16x16x32_bf16 a[48:51], v[100:103], v[148:151], a[48:51]\n"
  "v_exp_f32 v130, v130\n"
  "v_mfma_f32_16x16x32_bf16 a[52:55], v[108:111], v[148:151], a[52:55]\n"
  "ds_read_b128 a[216:219], v194 offset:36864\n"
  "v_mfma_f32_16x16x32_bf16 a[56:59], v[116:119], v[148:151], a[56:59]\n"
  "v_exp_f32 v131, v131\n"
  "v_mfma_f32_16x16x32_bf16 a[60:63], v[124:127], v[148:151], a[60:63]\n"
  "v_cvt_pk_bf16_f32 v145, v130, v131\n"
  "v_mfma_f32_16x16x32_bf16 a[132:135], v[168:171], v[148:151], a[132:135]\n"
  "v_exp_f32 v132, v132\n"
  "v_mfma_f32_16x16x32_bf16 v[136:139], a[224:227], v[96:99], v[164:167]\n"
  "ds_read_b64_tr_b16 v[36:37], v196 offset:33792\n"
  "v_mfma_f32_16x16x32_bf16 v[136:139], a[228:231], v[104:107], v[136:139]\n"
  "v_exp_f32 v133, v133\n"
  "v_mfma_f32_16x16x32_bf16 v[136:139], a[232:235], v[112:115], v[136:139]\n"
  "ds_read_b64_tr_b16 v[38:39], v196 offset:37888\n"
  "v_cvt_pk_bf16_f32 v146, v132, v133\n"
  "v_mfma_f32_16x16x32_bf16 v[136:139], a[236:239], v[120:123], v[136:139]\n"
  "v_exp_f32 v134, v134\n"
  "v_mfma_f32_16x16x32_bf16 v[140:143], a[240:243], v[96:99], v[164:167]\n"
  "ds_read_b128 a[220:223], v195 offset:36864\n"
  "v_mfma_f32_16x16x32_bf16 v[140:143], a[244:247], v[104:107], v[140:143]\n"
  "v_exp_f32 v135, v135\n"
  "v_mfma_f32_16x16x32_bf16 v[140:143], a[248:251], v[112:115], v[140:143]\n"
  "v_cvt_pk_bf16_f32 v147, v134, v135\n"
  "v_mfma_f32_16x16x32_bf16 v[140:143], a[252:255], v[120:123], v[140:143]\n"
  "buffer_load_dwordx4 v201, %[srdK], s62 offen offset:3072 lds\n"
  "v_mfma_f32_16x16x32_bf16 a[64:67], v[68:71], v[144:147], a[64:67]\n"
  "v_exp_f32 v136, v136\n"
  "v_mfma_f32_16x16x32_bf16 a[68:71], v[76:79], v[144:147], a[68:71]\n"
  "ds_read_b64_tr_b16 v[44:45], v197 offset:33792\n"
  "v_mfma_f32_16x16x32_bf16 a[72:75], v[84:87], v[144:147], a[72:75]\n"
  "v_exp_f32 v137, v137\n"
  "v_mfma_f32_16x16x32_bf16 a[76:79], v[92:95], v[144:147], a[76:79]\n"
  "ds_read_b64_tr_b16 v[46:47], v197 offset:37888\n"
  "v_cvt_pk_bf16_f32 v148, v136, v137\n"
  "v_mfma_f32_16x16x32_bf16 a[80:83], v[100:103], v[144:147], a[80:83]\n"
  "v_exp_f32 v138, v138\n"
  "v_mfma_f32_16x16x32_bf16 a[84:87], v[108:111], v[144:147], a[84:87]\n"
  "ds_read_b128 a[224:227], v192 offset:40960\n"
  "v_mfma_f32_16x16x32_bf16 a[88:91], v[116:119], v[144:147], a[88:91]\n"
  "v_exp_f32 v139, v139\n"
  "v_mfma_f32_16x16x32_bf16 a[92:95], v[124:127], v[144:147], a[92:95]\n"
  "v_cvt_pk_bf16_f32 v149, v138, v139\n"
  "v_mfma_f32_16x16x32_bf16 a[136:139], v[168:171], v[144:147], a[136:139]\n"
  "s_waitcnt lgkmcnt(3)\n"
  "v_exp_f32 v140, v140\n"
  "v_mfma_f32_16x16x32_bf16 v[128:131], a[192:195], v[0:3], v[152:155]\n"
  "ds_read_b64_tr_b16 v[52:53], v196 offset:34304\n"
  "v_mfma_f32_16x16x32_bf16 v[128:131], a[196:199], v[8:11], v[128:131]\n"
  "v_exp_f32 v141, v141\n"
  "v_mfma_f32_16x16x32_bf16 v[128:131], a[200:203], v[16:19], v[128:131]\n"
  "ds_read_b64_tr_b16 v[54:55], v196 offset:38400\n"
  "v_cvt_pk_bf16_f32 v150, v140, v141\n"
  "v_mfma_f32_16x16x32_bf16 v[128:131], a[204:207], v[24:27], v[128:131]\n"
  "v_exp_f32 v142, v142\n"
  "v_mfma_f32_16x16x32_bf16 v[132:135], a[208:211], v[0:3], v[152:155]\n"
  "ds_read_b128 a[228:231], v193 offset:40960\n"
  "v_mfma_f32_16x16x32_bf16 v[132:135], a[212:215], v[8:11], v[132:135]\n"
  "s_add_u32 m0, %[sM0], 65536\n"
  "v_exp_f32 v143, v143\n"
  "v_mfma_f32_16x16x32_bf16 v[132:135], a[216:219], v[16:19], v[132:135]\n"
  "v_cvt_pk_bf16_f32 v151, v142, v143\n"
  "v_mfma_f32_16x16x32_bf16 v[132:135], a[220:223], v[24:27], v[132:135]\n"
  "buffer_load_dwordx4 v202, %[srdV], s62 offen offset:0 lds\n"
  "v_mfma_f32_16x16x32_bf16 a[96:99], v[68:71], v[148:151], a[96:99]\n"
  "v_exp_f32 v128, v128\n"
  "v_mfma_f32_16x16x32_bf16 a[100:103], v[76:79], v[148:151], a[100:103]\n"
  "ds_read_b64_tr_b16 v[60:61], v197 offset:34304\n"
  "v_mfma_f32_16x16x32_bf16 a[104:107], v[84:87], v[148:151], a[104:107]\n"
  "v_exp_f32 v129, v129\n"
  "v_mfma_f32_16x16x32_bf16 a[108:111], v[92:95], v[148:151], a[108:111]\n"
  "ds_read_b64_tr_b16 v[62:63], v197 offset:38400\n"
  "v_cvt_pk_bf16_f32 v144, v128, v129\n"
  "v_mfma_f32_16x16x32_bf16 a[112:115], v[100:103], v[148:151], a[112:115]\n"
  "v_exp_f32 v130, v130\n"
  "v_mfma_f32_16x16x32_bf16 a[116:119], v[108:111], v[148:151], a[116:119]\n"
  "ds_read_b128 a[232:235], v194 offset:40960\n"
  "v_mfma_f32_16x16x32_bf16 a[120:123], v[116:119], v[148:151], a[120:123]\n"
  "v_exp_f32 v131, v131\n"
  "v_mfma_f32_16x16x32_bf16 a[124:127], v[124:127], v[148:151], a[124:127]\n"
  "v_cvt_pk_bf16_f32 v145, v130, v131\n"
  "v_mfma_f32_16x16x32_bf16 a[140:143], v[168:171], v[148:151], a[140:143]\n"
  "v_exp_f32 v132, v132\n"
  "v_mfma_f32_16x16x32_bf16 v[136:139], a[192:195], v[32:35], v[156:159]\n"
  "ds_read_b64_tr_b16 v[68:69], v196 offset:40960\n"
  "v_mfma_f32_16x16x32_bf16 v[136:139], a[196:199], v[40:43], v[136:139]\n"
  "v_exp_f32 v133, v133\n"
  "v_mfma_f32_16x16x32_bf16 v[136:139], a[200:203], v[48:51], v[136:139]\n"
  "ds_read_b64_tr_b16 v[70:71], v196 offset:45056\n"
  "v_cvt_pk_bf16_f32 v146, v132, v133\n"
  "v_mfma_f32_16x16x32_bf16 v[136:139], a[204:207], v[56:59], v[136:139]\n"
  "v_exp_f32 v134, v134\n"
  "v_mfma_f32_16x16x32_bf16 v[140:143], a[208:211], v[32:35], v[156:159]\n"
  "ds_read_b128 a[236:239], v195 offset:40960\n"
  "v_mfma_f32_16x16x32_bf16 v[140:143], a[212:215], v[40:43], v[140:143]\n"
  "v_exp_f32 v135, v135\n"
  "v_mfma_f32_16x16x32_bf16 v[140:143], a[216:219], v[48:51], v[140:143]\n"
  "v_cvt_pk_bf16_f32 v147, v134, v135\n"
  "v_mfma_f32_16x16x32_bf16 v[140:143], a[220:223], v[56:59], v[140:143]\n"
  "s_waitcnt lgkmcnt(4)\n"
  "buffer_load_dwordx4 v203, %[srdV], s62 offen offset:1024 lds\n"
  "v_mfma_f32_16x16x32_bf16 a[0:3], v[4:7], v[144:147], a[0:3]\n"
  "v_exp_f32 v136, v136\n"
  "v_mfma_f32_16x16x32_bf16 a[4:7], v[12:15], v[144:147], a[4:7]\n"
  "ds_read_b64_tr_b16 v[76:77], v197 offset:40960\n"
  "v_mfma_f32_16x16x32_bf16 a[8:11], v[20:23], v[144:147], a[8:11]\n"
  "v_exp_f32 v137, v137\n"
  "v_mfma_f32_16x16x32_bf16 a[12:15], v[28:31], v[144:147], a[12:15]\n"
  "ds_read_b64_tr_b16 v[78:79], v197 offset:45056\n"
  "v_cvt_pk_bf16_f32 v148, v136, v137\n"
  "v_mfma_f32_16x16x32_bf16 a[16:19], v[36:39], v[144:147], a[16:19]\n"
  "v_exp_f32 v138, v138\n"
  "v_mfma_f32_16x16x32_bf16 a[20:23], v[44:47], v[144:147], a[20:23]\n"
  "ds_read_b128 a[240:243], v192 offset:45056\n"
  "v_mfma_f32_16x16x32_bf16 a[24:27], v[52:55], v[144:147], a[24:27]\n"
  "v_exp_f32 v139, v139\n"
  "v_mfma_f32_16x16x32_bf16 a[28:31], v[60:63], v[144:147], a[28:31]\n"
  "v_cvt_pk_bf16_f32 v149, v138, v139\n"
  "v_mfma_f32_16x16x32_bf16 a[128:131], v[168:171], v[144:147], a[128:131]\n"
  "v_exp_f32 v140, v140\n"
  "v_mfma_f32_16x16x32_bf16 v[128:131], a[192:195], v[64:67], v[160:163]\n"
  "ds_read_b64_tr_b16 v[84:85], v196 offset:41472\n"
  "v_mfma_f32_16x16x32_bf16 v[128:131], a[196:199], v[72:75], v[128:131]\n"
  "v_exp_f32 v141, v141\n"
  "v_mfma_f32_16x16x32_bf16 v[128:131], a[200:203], v[80:83], v[128:131]\n"
  "ds_read_b64_tr_b16 v[86:87], v196 offset:45568\n"
  "v_cvt_pk_bf16_f32 v150, v140, v141\n"
  "v_mfma_f32_16x16x32_bf16 v[128:131], a[204:207], v[88:91], v[128:131]\n"
  "v_exp_f32 v142, v142\n"
  "v_mfma_f32_16x16x32_bf16 v[132:135], a[208:211], v[64:67], v[160:163]\n"
  "ds_read_b128 a[244:247], v193 offset:45056\n"
  "v_mfma_f32_16x16x32_bf16 v[132:135], a[212:215], v[72:75], v[132:135]\n"
  "v_exp_f32 v143, v143\n"
  "v_mfma_f32_16x16x32_bf16 v[132:135], a[216:219], v[80:83], v[132:135]\n"
  "v_cvt_pk_bf16_f32 v151, v142, v143\n"
  "v_mfma_f32_16x16x32_bf16 v[132:135], a[220:223], v[88:91], v[132:135]\n"
  "buffer_load_dwordx4 v202, %[srdV], s62 offen offset:2048 lds\n"
  "v_mfma_f32_16x16x32_bf16 a[32:35], v[4:7], v[148:151], a[32:35]\n"
  "v_exp_f32 v128, v128\n"
  "v_mfma_f32_16x16x32_bf16 a[36:39], v[12:15], v[148:151], a[36:39]\n"
  "ds_read_b64_tr_b16 v[92:93], v197 offset:41472\n"
  "v_mfma_f32_16x16x32_bf16 a[40:43], v[20:23], v[148:151], a[40:43]\n"
  "v_exp_f32 v129, v129\n"
  "v_mfma_f32_16x16x32_bf16 a[44:47], v[28:31], v[148:151], a[44:47]\n"
  "ds_read_b64_tr_b16 v[94:95], v197 offset:45568\n"
  "v_cvt_pk_bf16_f32 v144, v128, v129\n"
  "v_mfma_f32_16x16x32_bf16 a[48:51], v[36:39], v[148:151], a[48:51]\n"
  "v_exp_f32 v130, v130\n"
  "v_mfma_f32_16x16x32_bf16 a[52:55], v[44:47], v[148:151], a[52:55]\n"
  "ds_read_b128 a[248:251], v194 offset:45056\n"
  "v_mfma_f32_16x16x32_bf16 a[56:59], v[52:55], v[148:151], a[56:59]\n"
  "v_exp_f32 v131, v131\n"
  "v_mfma_f32_16x16x32_bf16 a[60:63], v[60:63], v[148:151], a[60:63]\n"
  "v_cvt_pk_bf16_f32 v145, v130, v131\n"
  "v_mfma_f32_16x16x32_bf16 a[132:135], v[168:171], v[148:151], a[132:135]\n"
  "v_exp_f32 v132, v132\n"
  "v_mfma_f32_16x16x32_bf16 v[136:139], a[192:195], v[96:99], v[164:167]\n"
  "ds_read_b64_tr_b16 v[100:101], v196 offset:41984\n"
  "v_mfma_f32_16x16x32_bf16 v[136:139], a[196:199], v[104:107], v[136:139]\n"
  "v_exp_f32 v133, v133\n"
  "v_mfma_f32_16x16x32_bf16 v[136:139], a[200:203], v[112:115], v[136:139]\n"
  "ds_read_b64_tr_b16 v[102:103], v196 offset:46080\n"
  "v_cvt_pk_bf16_f32 v146, v132, v133\n"
  "v_mfma_f32_16x16x32_bf16 v[136:139], a[204:207], v[120:123], v[136:139]\n"
  "v_exp_f32 v134, v134\n"
  "v_mfma_f32_16x16x32_bf16 v[140:143], a[208:211], v[96:99], v[164:167]\n"
  "ds_read_b128 a[252:255], v195 offset:45056\n"
  "v_mfma_f32_16x16x32_bf16 v[140:143], a[212:215], v[104:107], v[140:143]\n"
  "v_exp_f32 v135, v135\n"
  "v_mfma_f32_16x16x32_bf16 v[140:143], a[216:219], v[112:115], v[140:143]\n"
  "s_waitcnt vmcnt(8)\n"
  "s_barrier\n"
  "v_cvt_pk_bf16_f32 v147, v134, v135\n"
  "v_mfma_f32_16x16x32_bf16 v[140:143], a[220:223], v[120:123], v[140:143]\n"
  "buffer_load_dwordx4 v203, %[srdV], s62 offen offset:3072 lds\n"
  "v_mfma_f32_16x16x32_bf16 a[64:67], v[4:7], v[144:147], a[64:67]\n"
  "v_exp_f32 v136, v136\n"
  "v_mfma_f32_16x16x32_bf16 a[68:71], v[12:15], v[144:147], a[68:71]\n"
  "ds_read_b64_tr_b16 v[108:109], v197 offset:41984\n"
  "v_mfma_f32_16x16x32_bf16 a[72:75], v[20:23], v[144:147], a[72:75]\n"
  "v_exp_f32 v137, v137\n"
  "v_mfma_f32_16x16x32_bf16 a[76:79], v[28:31], v[144:147], a[76:79]\n"
  "ds_read_b64_tr_b16 v[110:111], v197 offset:46080\n"
  "v_cvt_pk_bf16_f32 v148, v136, v137\n"
  "v_mfma_f32_16x16x32_bf16 a[80:83], v[36:39], v[144:147], a[80:83]\n"
  "v_exp_f32 v138, v138\n"
  "v_mfma_f32_16x16x32_bf16 a[84:87], v[44:47], v[144:147], a[84:87]\n"
  "ds_read_b128 a[192:195], v192 offset:49152\n"
  "v_mfma_f32_16x16x32_bf16 a[88:91], v[52:55], v[144:147], a[88:91]\n"
  "v_exp_f32 v139, v139\n"
  "v_mfma_f32_16x16x32_bf16 a[92:95], v[60:63], v[144:147], a[92:95]\n"
  "v_cvt_pk_bf16_f32 v149, v138, v139\n"
  "v_mfma_f32_16x16x32_bf16 a[136:139], v[168:171], v[144:147], a[136:139]\n"
  "s_waitcnt lgkmcnt(3)\n"
  "v_exp_f32 v140, v140\n"
  "v_mfma_f32_16x16x32_bf16 v[128:131], a[224:227], v[0:3], v[152:155]\n"
  "ds_read_b64_tr_b16 v[116:117], v196 offset:42496\n"
  "v_mfma_f32_16x16x32_bf16 v[128:131], a[228:231], v[8:11], v[128:131]\n"
  "v_exp_f32 v141, v141\n"
  "v_mfma_f32_16x16x32_bf16 v[128:131], a[232:235], v[16:19], v[128:131]\n"
  "ds_read_b64_tr_b16 v[118:119], v196 offset:46592\n"
  "v_cvt_pk_bf16_f32 v150, v140, v141\n"
  "v_mfma_f32_16x16x32_bf16 v[128:131], a[236:239], v[24:27], v[128:131]\n"
  "s_add_u32 s62, s62, 16384\n"
  "v_exp_f32 v142, v142\n"
  "v_mfma_f32_16x16x32_bf16 v[132:135], a[240:243], v[0:3], v[152:155]\n"
  "ds_read_b128 a[196:199], v193 offset:49152\n"
  "v_mfma_f32_16x16x32_bf16 v[132:135], a[244:247], v[8:11], v[132:135]\n"
  "s_add_u32 m0, %[sM0], 16384\n"
  "v_exp_f32 v143, v143\n"
  "v_mfma_f32_16x16x32_bf16 v[132:135], a[248:251], v[16:19], v[132:135]\n"
  "v_cvt_pk_bf16_f32 v151, v142, v143\n"
  "v_mfma_f32_16x16x32_bf16 v[132:135], a[252:255], v[24:27], v[132:135]\n"
  "buffer_load_dwordx4 v198, %[srdK], s62 offen offset:0 lds\n"
  "v_mfma_f32_16x16x32_bf16 a[96:99], v[4:7], v[148:151], a[96:99]\n"
  "v_exp_f32 v128, v128\n"
  "v_mfma_f32_16x16x32_bf16 a[100:103], v[12:15], v[148:151], a[100:103]\n"
  "ds_read_b64_tr_b16 v[124:125], v197 offset:42496\n"
  "v_mfma_f32_16x16x32_bf16 a[104:107], v[20:23], v[148:151], a[104:107]\n"
  "v_exp_f32 v129, v129\n"
  "v_mfma_f32_16x16x32_bf16 a[108:111], v[28:31], v[148:151], a[108:111]\n"
  "ds_read_b64_tr_b16 v[126:127], v197 offset:46592\n"
  "v_cvt_pk_bf16_f32 v144, v128, v129\n"
  "v_mfma_f32_16x16x32_bf16 a[112:115], v[36:39], v[148:151], a[112:115]\n"
  "v_exp_f32 v130, v130\n"
  "v_mfma_f32_16x16x32_bf16 a[116:119], v[44:47], v[148:151], a[116:119]\n"
  "ds_read_b128 a[200:203], v194 offset:49152\n"
  "v_mfma_f32_16x16x32_bf16 a[120:123], v[52:55], v[148:151], a[120:123]\n"
  "v_exp_f32 v131, v131\n"
  "v_mfma_f32_16x16x32_bf16 a[124:127], v[60:63], v[148:151], a[124:127]\n"
  "v_cvt_pk_bf16_f32 v145, v130, v131\n"
  "v_mfma_f32_16x16x32_bf16 a[140:143], v[168:171], v[148:151], a[140:143]\n"
  "v_exp_f32 v132, v132\n"
  "v_mfma_f32_16x16x32_bf16 v[136:139], a[224:227], v[32:35], v[156:159]\n"
  "ds_read_b64_tr_b16 v[4:5], v196 offset:49152\n"
  "v_mfma_f32_16x16x32_bf16 v[136:139], a[228:231], v[40:43], v[136:139]\n"
  "v_exp_f32 v133, v133\n"
  "v_mfma_f32_16x16x32_bf16 v[136:139], a[232:235], v[48:51], v[136:139]\n"
  "ds_read_b64_tr_b16 v[6:7], v196 offset:53248\n"
  "v_cvt_pk_bf16_f32 v146, v132, v133\n"
  "v_mfma_f32_16x16x32_bf16 v[136:139], a[236:239], v[56:59], v[136:139]\n"
  "v_exp_f32 v134, v134\n"
  "v_mfma_f32_16x16x32_bf16 v[140:143], a[240:243], v[32:35], v[156:159]\n"
  "ds_read_b128 a[204:207], v195 offset:49152\n"
  "v_mfma_f32_16x16x32_bf16 v[140:143], a[244:247], v[40:43], v[140:143]\n"
  "v_exp_f32 v135, v135\n"
  "v_mfma_f32_16x16x32_bf16 v[140:143], a[248:251], v[48:51], v[140:143]\n"
  "v_cvt_pk_bf16_f32 v147, v134, v135\n"
  "v_mfma_f32_16x16x32_bf16 v[140:143], a[252:255], v[56:59], v[140:143]\n"
  "s_waitcnt lgkmcnt(4)\n"
  "buffer_load_dwordx4 v199, %[srdK], s62 offen offset:1024 lds\n"
  "v_mfma_f32_16x16x32_bf16 a[0:3], v[68:71], v[144:147], a[0:3]\n"
  "v_exp_f32 v136, v136\n"
  "v_mfma_f32_16x16x32_bf16 a[4:7], v[76:79], v[144:147], a[4:7]\n"
  "ds_read_b64_tr_b16 v[12:13], v197 offset:49152\n"
  "v_mfma_f32_16x16x32_bf16 a[8:11], v[84:87], v[144:147], a[8:11]\n"
  "v_exp_f32 v137, v137\n"
  "v_mfma_f32_16x16x32_bf16 a[12:15], v[92:95], v[144:147], a[12:15]\n"
  "ds_read_b64_tr_b16 v[14:15], v197 offset:53248\n"
  "v_cvt_pk_bf16_f32 v148, v136, v137\n"
  "v_mfma_f32_16x16x32_bf16 a[16:19], v[100:103], v[144:147], a[16:19]\n"
  "v_exp_f32 v138, v138\n"
  "v_mfma_f32_16x16x32_bf16 a[20:23], v[108:111], v[144:147], a[20:23]\n"
  "ds_read_b128 a[208:211], v192 offset:53248\n"
  "v_mfma_f32_16x16x32_bf16 a[24:27], v[116:119], v[144:147], a[24:27]\n"
  "v_exp_f32 v139, v139\n"
  "v_mfma_f32_16x16x32_bf16 a[28:31], v[124:127], v[144:147], a[28:31]\n"
  "global_load_dwordx4 v[216:219], %[vL16], s[68:69] offset:2048\n"
  "v_cvt_pk_bf16_f32 v149, v138, v139\n"
  "v_mfma_f32_16x16x32_bf16 a[128:131], v[168:171], v[144:147], a[128:131]\n"
  "v_exp_f32 v140, v140\n"
  "v_mfma_f32_16x16x32_bf16 v[128:131], a[224:227], v[64:67], v[160:163]\n"
  "ds_read_b64_tr_b16 v[20:21], v196 offset:49664\n"
  "v_mfma_f32_16x16x32_bf16 v[128:131], a[228:231], v[72:75], v[128:131]\n"
  "v_exp_f32 v141, v141\n"
  "v_mfma_f32_16x16x32_bf16 v[128:131], a[232:235], v[80:83], v[128:131]\n"
  "ds_read_b64_tr_b16 v[22:23], v196 offset:53760\n"
  "v_cvt_pk_bf16_f32 v150, v140, v141\n"
  "v_mfma_f32_16x16x32_bf16 v[128:131], a[236:239], v[88:91], v[128:131]\n"
  "v_exp_f32 v142, v142\n"
  "v_mfma_f32_16x16x32_bf16 v[132:135], a[240:243], v[64:67], v[160:163]\n"
  "ds_read_b128 a[212:215], v193 offset:53248\n"
  "v_mfma_f32_16x16x32_bf16 v[132:135], a[244:247], v[72:75], v[132:135]\n"
  "v_exp_f32 v143, v143\n"
  "v_mfma_f32_16x16x32_bf16 v[132:135], a[248:251], v[80:83], v[132:135]\n"
  "v_cvt_pk_bf16_f32 v151, v142, v143\n"
  "v_mfma_f32_16x16x32_bf16 v[132:135], a[252:255], v[88:91], v[132:135]\n"
  "buffer_load_dwordx4 v200, %[srdK], s62 offen offset:2048 lds\n"
  "v_mfma_f32_16x16x32_bf16 a[32:35], v[68:71], v[148:151], a[32:35]\n"
  "v_exp_f32 v128, v128\n"
  "v_mfma_f32_16x16x32_bf16 a[36:39], v[76:79], v[148:151], a[36:39]\n"
  "ds_read_b64_tr_b16 v[28:29], v197 offset:49664\n"
  "v_mfma_f32_16x16x32_bf16 a[40:43], v[84:87], v[148:151], a[40:43]\n"
  "v_exp_f32 v129, v129\n"
  "v_mfma_f32_16x16x32_bf16 a[44:47], v[92:95], v[148:151], a[44:47]\n"
  "ds_read_b64_tr_b16 v[30:31], v197 offset:53760\n"
  "v_cvt_pk_bf16_f32 v144, v128, v129\n"
  "v_mfma_f32_16x16x32_bf16 a[48:51], v[100:103], v[148:151], a[48:51]\n"
  "v_exp_f32 v130, v130\n"
  "v_mfma_f32_16x16x32_bf16 a[52:55], v[108:111], v[148:151], a[52:55]\n"
  "ds_read_b128 a[216:219], v194 offset:53248\n"
  "v_mfma_f32_16x16x32_bf16 a[56:59], v[116:119], v[148:151], a[56:59]\n"
  "v_exp_f32 v131, v131\n"
  "v_mfma_f32_16x16x32_bf16 a[60:63], v[124:127], v[148:151], a[60:63]\n"
  "v_cvt_pk_bf16_f32 v145, v130, v131\n"
  "v_mfma_f32_16x16x32_bf16 a[132:135], v[168:171], v[148:151], a[132:135]\n"
  "v_exp_f32 v132, v132\n"
  "v_mfma_f32_16x16x32_bf16 v[136:139], a[224:227], v[96:99], v[164:167]\n"
  "ds_read_b64_tr_b16 v[36:37], v196 offset:50176\n"
  "v_mfma_f32_16x16x32_bf16 v[136:139], a[228:231], v[104:107], v[136:139]\n"
  "v_exp_f32 v133, v133\n"
  "v_mfma_f32_16x16x32_bf16 v[136:139], a[232:235], v[112:115], v[136:139]\n"
  "ds_read_b64_tr_b16 v[38:39], v196 offset:54272\n"
  "v_cvt_pk_bf16_f32 v146, v132, v133\n"
  "v_mfma_f32_16x16x32_bf16 v[136:139], a[236:239], v[120:123], v[136:139]\n"
  "v_exp_f32 v134, v134\n"
  "v_mfma_f32_16x16x32_bf16 v[140:143], a[240:243], v[96:99], v[164:167]\n"
  "ds_read_b128 a[220:223], v195 offset:53248\n"
  "v_mfma_f32_16x16x32_bf16 v[140:143], a[244:247], v[104:107], v[140:143]\n"
  "v_exp_f32 v135, v135\n"
  "v_mfma_f32_16x16x32_bf16 v[140:143], a[248:251], v[112:115], v[140:143]\n"
  "v_cvt_pk_bf16_f32 v147, v134, v135\n"
  "v_mfma_f32_16x16x32_bf16 v[140:143], a[252:255], v[120:123], v[140:143]\n"
  "buffer_load_dwordx4 v201, %[srdK], s62 offen offset:3072 lds\n"
  "v_mfma_f32_16x16x32_bf16 a[64:67], v[68:71], v[144:147], a[64:67]\n"
  "v_exp_f32 v136, v136\n"
  "v_mfma_f32_16x16x32_bf16 a[68:71], v[76:79], v[144:147], a[68:71]\n"
  "ds_read_b64_tr_b16 v[44:45], v197 offset:50176\n"
  "v_mfma_f32_16x16x32_bf16 a[72:75], v[84:87], v[144:147], a[72:75]\n"
  "v_exp_f32 v137, v137\n"
  "v_mfma_f32_16x16x32_bf16 a[76:79], v[92:95], v[144:147], a[76:79]\n"
  "ds_read_b64_tr_b16 v[46:47], v197 offset:54272\n"
  "v_cvt_pk_bf16_f32 v148, v136, v137\n"
  "v_mfma_f32_16x16x32_bf16 a[80:83], v[100:103], v[144:147], a[80:83]\n"
  "v_exp_f32 v138, v138\n"
  "v_mfma_f32_16x16x32_bf16 a[84:87], v[108:111], v[144:147], a[84:87]\n"
  "ds_read_b128 a[224:227], v192 offset:57344\n"
  "v_mfma_f32_16x16x32_bf16 a[88:91], v[116:119], v[144:147], a[88:91]\n"
  "v_exp_f32 v139, v139\n"
  "v_mfma_f32_16x16x32_bf16 a[92:95], v[124:127], v[144:147], a[92:95]\n"
  "v_cvt_pk_bf16_f32 v149, v138, v139\n"
  "v_mfma_f32_16x16x32_bf16 a[136:139], v[168:171], v[144:147], a[136:139]\n"
  "s_waitcnt lgkmcnt(3)\n"
  "v_exp_f32 v140, v140\n"
  "v_mfma_f32_16x16x32_bf16 v[128:131], a[192:195], v[0:3], v[152:155]\n"
  "ds_read_b64_tr_b16 v[52:53], v196 offset:50688\n"
  "v_mfma_f32_16x16x32_bf16 v[128:131], a[196:199], v[8:11], v[128:131]\n"
  "v_exp_f32 v141, v141\n"
  "v_mfma_f32_16x16x32_bf16 v[128:131], a[200:203], v[16:19], v[128:131]\n"
  "ds_read_b64_tr_b16 v[54:55], v196 offset:54784\n"
  "v_cvt_pk_bf16_f32 v150, v140, v141\n"
  "v_mfma_f32_16x16x32_bf16 v[128:131], a[204:207], v[24:27], v[128:131]\n"
  "v_exp_f32 v142, v142\n"
  "v_mfma_f32_16x16x32_bf16 v[132:135], a[208:211], v[0:3], v[152:155]\n"
  "ds_read_b128 a[228:231], v193 offset:57344\n"
  "v_mfma_f32_16x16x32_bf16 v[132:135], a[212:215], v[8:11], v[132:135]\n"
  "s_add_u32 m0, %[sM0], 81920\n"
  "v_exp_f32 v143, v143\n"
  "v_mfma_f32_16x16x32_bf16 v[132:135], a[216:219], v[16:19], v[132:135]\n"
  "v_cvt_pk_bf16_f32 v151, v142, v143\n"
  "v_mfma_f32_16x16x32_bf16 v[132:135], a[220:223], v[24:27], v[132:135]\n"
  "buffer_load_dwordx4 v202, %[srdV], s62 offen offset:0 lds\n"
  "v_mfma_f32_16x16x32_bf16 a[96:99], v[68:71], v[148:151], a[96:99]\n"
  "v_exp_f32 v128, v128\n"
  "v_mfma_f32_16x16x32_bf16 a[100:103], v[76:79], v[148:151], a[100:103]\n"
  "ds_read_b64_tr_b16 v[60:61], v197 offset:50688\n"
  "v_mfma_f32_16x16x32_bf16 a[104:107], v[84:87], v[148:151], a[104:107]\n"
  "v_exp_f32 v129, v129\n"
  "v_mfma_f32_16x16x32_bf16 a[108:111], v[92:95], v[148:151], a[108:111]\n"
  "ds_read_b64_tr_b16 v[62:63], v197 offset:54784\n"
  "v_cvt_pk_bf16_f32 v144, v128, v129\n"
  "v_mfma_f32_16x16x32_bf16 a[112:115], v[100:103], v[148:151], a[112:115]\n"
  "v_exp_f32 v130, v130\n"
  "v_mfma_f32_16x16x32_bf16 a[116:119], v[108:111], v[148:151], a[116:119]\n"
  "ds_read_b128 a[232:235], v194 offset:57344\n"
  "v_mfma_f32_16x16x32_bf16 a[120:123], v[116:119], v[148:151], a[120:123]\n"
  "v_exp_f32 v131, v131\n"
  "v_mfma_f32_16x16x32_bf16 a[124:127], v[124:127], v[148:151], a[124:127]\n"
  "v_cvt_pk_bf16_f32 v145, v130, v131\n"
  "v_mfma_f32_16x16x32_bf16 a[140:143], v[168:171], v[148:151], a[140:143]\n"
  "v_exp_f32 v132, v132\n"
  "v_mfma_f32_16x16x32_bf16 v[136:139], a[192:195], v[32:35], v[156:159]\n"
  "ds_read_b64_tr_b16 v[68:69], v196 offset:57344\n"
  "v_mfma_f32_16x16x32_bf16 v[136:139], a[196:199], v[40:43], v[136:139]\n"
  "v_exp_f32 v133, v133\n"
  "v_mfma_f32_16x16x32_bf16 v[136:139], a[200:203], v[48:51], v[136:139]\n"
  "ds_read_b64_tr_b16 v[70:71], v196 offset:61440\n"
  "v_cvt_pk_bf16_f32 v146, v132, v133\n"
  "v_mfma_f32_16x16x32_bf16 v[136:139], a[204:207], v[56:59], v[136:139]\n"
  "v_exp_f32 v134, v134\n"
  "v_mfma_f32_16x16x32_bf16 v[140:143], a[208:211], v[32:35], v[156:159]\n"
  "ds_read_b128 a[236:239], v195 offset:57344\n"
  "v_mfma_f32_16x16x32_bf16 v[140:143], a[212:215], v[40:43], v[140:143]\n"
  "v_exp_f32 v135, v135\n"
  "v_mfma_f32_16x16x32_bf16 v[140:143], a[216:219], v[48:51], v[140:143]\n"
  "v_cvt_pk_bf16_f32 v147, v134, v135\n"
  "v_mfma_f32_16x16x32_bf16 v[140:143], a[220:223], v[56:59], v[140:143]\n"
  "s_waitcnt lgkmcnt(4)\n"
  "buffer_load_dwordx4 v203, %[srdV], s62 offen offset:1024 lds\n"
  "v_mfma_f32_16x16x32_bf16 a[0:3], v[4:7], v[144:147], a[0:3]\n"
  "v_exp_f32 v136, v136\n"
  "v_mfma_f32_16x16x32_bf16 a[4:7], v[12:15], v[144:147], a[4:7]\n"
  "ds_read_b64_tr_b16 v[76:77], v197 offset:57344\n"
  "v_mfma_f32_16x16x32_bf16 a[8:11], v[20:23], v[144:147], a[8:11]\n"
  "v_exp_f32 v137, v137\n"
  "v_mfma_f32_16x16x32_bf16 a[12:15], v[28:31], v[144:147], a[12:15]\n"
  "ds_read_b64_tr_b16 v[78:79], v197 offset:61440\n"
  "v_cvt_pk_bf16_f32 v148, v136, v137\n"
  "v_mfma_f32_16x16x32_bf16 a[16:19], v[36:39], v[144:147], a[16:19]\n"
  "v_exp_f32 v138, v138\n"
  "v_mfma_f32_16x16x32_bf16 a[20:23], v[44:47], v[144:147], a[20:23]\n"
  "ds_read_b128 a[240:243], v192 offset:61440\n"
  "v_mfma_f32_16x16x32_bf16 a[24:27], v[52:55], v[144:147], a[24:27]\n"
  "v_exp_f32 v139, v139\n"
  "v_mfma_f32_16x16x32_bf16 a[28:31], v[60:63], v[144:147], a[28:31]\n"
  "v_cvt_pk_bf16_f32 v149, v138, v139\n"
  "v_mfma_f32_16x16x32_bf16 a[128:131], v[168:171], v[144:147], a[128:131]\n"
  "v_exp_f32 v140, v140\n"
  "v_mfma_f32_16x16x32_bf16 v[128:131], a[192:195], v[64:67], v[160:163]\n"
  "ds_read_b64_tr_b16 v[84:85], v196 offset:57856\n"
  "v_mfma_f32_16x16x32_bf16 v[128:131], a[196:199], v[72:75], v[128:131]\n"
  "v_exp_f32 v141, v141\n"
  "v_mfma_f32_16x16x32_bf16 v[128:131], a[200:203], v[80:83], v[128:131]\n"
  "ds_read_b64_tr_b16 v[86:87], v196 offset:61952\n"
  "v_cvt_pk_bf16_f32 v150, v140, v141\n"
  "v_mfma_f32_16x16x32_bf16 v[128:131], a[204:207], v[88:91], v[128:131]\n"
  "v_exp_f32 v142, v142\n"
  "v_mfma_f32_16x16x32_bf16 v[132:135], a[208:211], v[64:67], v[160:163]\n"
  "ds_read_b128 a[244:247], v193 offset:61440\n"
  "v_mfma_f32_16x16x32_bf16 v[132:135], a[212:215], v[72:75], v[132:135]\n"
  "v_exp_f32 v143, v143\n"
  "v_mfma_f32_16x16x32_bf16 v[132:135], a[216:219], v[80:83], v[132:135]\n"
  "v_cvt_pk_bf16_f32 v151, v142, v143\n"
  "v_mfma_f32_16x16x32_bf16 v[132:135], a[220:223], v[88:91], v[132:135]\n"
  "buffer_load_dwordx4 v202, %[srdV], s62 offen offset:2048 lds\n"
  "v_mfma_f32_16x16x32_bf16 a[32:35], v[4:7], v[148:151], a[32:35]\n"
  "v_exp_f32 v128, v128\n"
  "v_mfma_f32_16x16x32_bf16 a[36:39], v[12:15], v[148:151], a[36:39]\n"
  "ds_read_b64_tr_b16 v[92:93], v197 offset:57856\n"
  "v_mfma_f32_16x16x32_bf16 a[40:43], v[20:23], v[148:151], a[40:43]\n"
  "v_exp_f32 v129, v129\n"
  "v_mfma_f32_16x16x32_bf16 a[44:47], v[28:31], v[148:151], a[44:47]\n"
  "ds_read_b64_tr_b16 v[94:95], v197 offset:61952\n"
  "v_cvt_pk_bf16_f32 v144, v128, v129\n"
  "v_mfma_f32_16x16x32_bf16 a[48:51], v[36:39], v[148:151], a[48:51]\n"
  "v_exp_f32 v130, v130\n"
  "v_mfma_f32_16x16x32_bf16 a[52:55], v[44:47], v[148:151], a[52:55]\n"
  "ds_read_b128 a[248:251], v194 offset:61440\n"
  "v_mfma_f32_16x16x32_bf16 a[56:59], v[52:55], v[148:151], a[56:59]\n"
  "v_exp_f32 v131, v131\n"
  "v_mfma_f32_16x16x32_bf16 a[60:63], v[60:63], v[148:151], a[60:63]\n"
  "v_cvt_pk_bf16_f32 v145, v130, v131\n"
  "v_mfma_f32_16x16x32_bf16 a[132:135], v[168:171], v[148:151], a[132:135]\n"
  "v_exp_f32 v132, v132\n"
  "v_mfma_f32_16x16x32_bf16 v[136:139], a[192:195], v[96:99], v[164:167]\n"
  "ds_read_b64_tr_b16 v[100:101], v196 offset:58368\n"
  "v_mfma_f32_16x16x32_bf16 v[136:139], a[196:199], v[104:107], v[136:139]\n"
  "v_exp_f32 v133, v133\n"
  "v_mfma_f32_16x16x32_bf16 v[136:139], a[200:203], v[112:115], v[136:139]\n"
  "ds_read_b64_tr_b16 v[102:103], v196 offset:62464\n"
  "v_cvt_pk_bf16_f32 v146, v132, v133\n"
  "v_mfma_f32_16x16x32_bf16 v[136:139], a[204:207], v[120:123], v[136:139]\n"
  "v_exp_f32 v134, v134\n"
  "v_mfma_f32_16x16x32_bf16 v[140:143], a[208:211], v[96:99], v[164:167]\n"
  "ds_read_b128 a[252:255], v195 offset:61440\n"
  "v_mfma_f32_16x16x32_bf16 v[140:143], a[212:215], v[104:107], v[140:143]\n"
  "v_exp_f32 v135, v135\n"
  "v_mfma_f32_16x16x32_bf16 v[140:143], a[216:219], v[112:115], v[140:143]\n"
  "s_waitcnt vmcnt(8)\n"
  "s_barrier\n"
  "v_cvt_pk_bf16_f32 v147, v134, v135\n"
  "v_mfma_f32_16x16x32_bf16 v[140:143], a[220:223], v[120:123], v[140:143]\n"
  "buffer_load_dwordx4 v203, %[srdV], s62 offen offset:3072 lds\n"
  "v_mfma_f32_16x16x32_bf16 a[64:67], v[4:7], v[144:147], a[64:67]\n"
  "v_exp_f32 v136, v136\n"
  "v_mfma_f32_16x16x32_bf16 a[68:71], v[12:15], v[144:147], a[68:71]\n"
  "ds_read_b64_tr_b16 v[108:109], v197 offset:58368\n"
  "v_mfma_f32_16x16x32_bf16 a[72:75], v[20:23], v[144:147], a[72:75]\n"
  "v_exp_f32 v137, v137\n"
  "v_mfma_f32_16x16x32_bf16 a[76:79], v[28:31], v[144:147], a[76:79]\n"
  "ds_read_b64_tr_b16 v[110:111], v197 offset:62464\n"
  "v_cvt_pk_bf16_f32 v148, v136, v137\n"
  "v_mfma_f32_16x16x32_bf16 a[80:83], v[36:39], v[144:147], a[80:83]\n"
  "v_exp_f32 v138, v138\n"
  "v_mfma_f32_16x16x32_bf16 a[84:87], v[44:47], v[144:147], a[84:87]\n"
  "ds_read_b128 a[192:195], v192 offset:0\n"
  "v_mfma_f32_16x16x32_bf16 a[88:91], v[52:55], v[144:147], a[88:91]\n"
  "v_exp_f32 v139, v139\n"
  "v_mfma_f32_16x16x32_bf16 a[92:95], v[60:63], v[144:147], a[92:95]\n"
  "v_cvt_pk_bf16_f32 v149, v138, v139\n"
  "v_mfma_f32_16x16x32_bf16 a[136:139], v[168:171], v[144:147], a[136:139]\n"
  "s_waitcnt lgkmcnt(3)\n"
  "v_exp_f32 v140, v140\n"
  "v_mfma_f32_16x16x32_bf16 v[128:131], a[224:227], v[0:3], v[152:155]\n"
  "ds_read_b64_tr_b16 v[116:117], v196 offset:58880\n"
  "v_mfma_f32_16x16x32_bf16 v[128:131], a[228:231], v[8:11], v[128:131]\n"
  "v_exp_f32 v141, v141\n"
  "v_mfma_f32_16x16x32_bf16 v[128:131], a[232:235], v[16:19], v[128:131]\n"
  "ds_read_b64_tr_b16 v[118:119], v196 offset:62976\n"
  "v_cvt_pk_bf16_f32 v150, v140, v141\n"
  "v_mfma_f32_16x16x32_bf16 v[128:131], a[236:239], v[24:27], v[128:131]\n"
  "s_add_u32 s62, s62, 16384\n"
  "v_exp_f32 v142, v142\n"
  "v_mfma_f32_16x16x32_bf16 v[132:135], a[240:243], v[0:3], v[152:155]\n"
  "ds_read_b128 a[196:199], v193 offset:0\n"
  "v_mfma_f32_16x16x32_bf16 v[132:135], a[244:247], v[8:11], v[132:135]\n"
  "s_add_u32 m0, %[sM0], 32768\n"
  "v_exp_f32 v143, v143\n"
  "v_mfma_f32_16x16x32_bf16 v[132:135], a[248:251], v[16:19], v[132:135]\n"
  "v_cvt_pk_bf16_f32 v151, v142, v143\n"
  "v_mfma_f32_16x16x32_bf16 v[132:135], a[252:255], v[24:27], v[132:135]\n"
  "buffer_load_dwordx4 v198, %[srdK], s62 offen offset:0 lds\n"
  "v_mfma_f32_16x16x32_bf16 a[96:99], v[4:7], v[148:151], a[96:99]\n"
  "v_exp_f32 v128, v128\n"
  "v_mfma_f32_16x16x32_bf16 a[100:103], v[12:15], v[148:151], a[100:103]\n"
  "ds_read_b64_tr_b16 v[124:125], v197 offset:58880\n"
  "v_mfma_f32_16x16x32_bf16 a[104:107], v[20:23], v[148:151], a[104:107]\n"
  "v_exp_f32 v129, v129\n"
  "v_mfma_f32_16x16x32_bf16 a[108:111], v[28:31], v[148:151], a[108:111]\n"
  "ds_read_b64_tr_b16 v[126:127], v197 offset:62976\n"
  "v_cvt_pk_bf16_f32 v144, v128, v129\n"
  "v_mfma_f32_16x16x32_bf16 a[112:115], v[36:39], v[148:151], a[112:115]\n"
  "v_exp_f32 v130, v130\n"
  "v_mfma_f32_16x16x32_bf16 a[116:119], v[44:47], v[148:151], a[116:119]\n"
  "ds_read_b128 a[200:203], v194 offset:0\n"
  "v_mfma_f32_16x16x32_bf16 a[120:123], v[52:55], v[148:151], a[120:123]\n"
  "v_exp_f32 v131, v131\n"
  "v_mfma_f32_16x16x32_bf16 a[124:127], v[60:63], v[148:151], a[124:127]\n"
  "v_cvt_pk_bf16_f32 v145, v130, v131\n"
  "v_mfma_f32_16x16x32_bf16 a[140:143], v[168:171], v[148:151], a[140:143]\n"
  "v_exp_f32 v132, v132\n"
  "v_mfma_f32_16x16x32_bf16 v[136:139], a[224:227], v[32:35], v[156:159]\n"
  "ds_read_b64_tr_b16 v[4:5], v196 offset:0\n"
  "v_mfma_f32_16x16x32_bf16 v[136:139], a[228:231], v[40:43], v[136:139]\n"
  "v_exp_f32 v133, v133\n"
  "v_mfma_f32_16x16x32_bf16 v[136:139], a[232:235], v[48:51], v[136:139]\n"
  "ds_read_b64_tr_b16 v[6:7], v196 offset:4096\n"
  "v_cvt_pk_bf16_f32 v146, v132, v133\n"
  "v_mfma_f32_16x16x32_bf16 v[136:139], a[236:239], v[56:59], v[136:139]\n"
  "v_exp_f32 v134, v134\n"
  "v_mfma_f32_16x16x32_bf16 v[140:143], a[240:243], v[32:35], v[156:159]\n"
  "ds_read_b128 a[204:207], v195 offset:0\n"
  "v_mfma_f32_16x16x32_bf16 v[140:143], a[244:247], v[40:43], v[140:143]\n"
  "v_exp_f32 v135, v135\n"
  "v_mfma_f32_16x16x32_bf16 v[140:143], a[248:251], v[48:51], v[140:143]\n"
  "v_cvt_pk_bf16_f32 v147, v134, v135\n"
  "v_mfma_f32_16x16x32_bf16 v[140:143], a[252:255], v[56:59], v[140:143]\n"
  "s_waitcnt lgkmcnt(4)\n"
  "buffer_load_dwordx4 v199, %[srdK], s62 offen offset:1024 lds\n"
  "v_mfma_f32_16x16x32_bf16 a[0:3], v[68:71], v[144:147], a[0:3]\n"
  "v_exp_f32 v136, v136\n"
  "v_mfma_f32_16x16x32_bf16 a[4:7], v[76:79], v[144:147], a[4:7]\n"
  "ds_read_b64_tr_b16 v[12:13], v197 offset:0\n"
  "v_mfma_f32_16x16x32_bf16 a[8:11], v[84:87], v[144:147], a[8:11]\n"
  "v_exp_f32 v137, v137\n"
  "v_mfma_f32_16x16x32_bf16 a[12:15], v[92:95], v[144:147], a[12:15]\n"
  "ds_read_b64_tr_b16 v[14:15], v197 offset:4096\n"
  "v_cvt_pk_bf16_f32 v148, v136, v137\n"
  "v_mfma_f32_16x16x32_bf16 a[16:19], v[100:103], v[144:147], a[16:19]\n"
  "v_exp_f32 v138, v138\n"
  "v_mfma_f32_16x16x32_bf16 a[20:23], v[108:111], v[144:147], a[20:23]\n"
  "ds_read_b128 a[208:211], v192 offset:4096\n"
  "v_mfma_f32_16x16x32_bf16 a[24:27], v[116:119], v[144:147], a[24:27]\n"
  "v_exp_f32 v139, v139\n"
  "v_mfma_f32_16x16x32_bf16 a[28:31], v[124:127], v[144:147], a[28:31]\n"
  "global_load_dwordx4 v[216:219], %[vL16], s[68:69] offset:3072\n"
  "v_cvt_pk_bf16_f32 v149, v138, v139\n"
  "v_mfma_f32_16x16x32_bf16 a[128:131], v[168:171], v[144:147], a[128:131]\n"
  "v_exp_f32 v140, v140\n"
  "v_mfma_f32_16x16x32_bf16 v[128:131], a[224:227], v[64:67], v[160:163]\n"
  "ds_read_b64_tr_b16 v[20:21], v196 offset:512\n"
  "v_mfma_f32_16x16x32_bf16 v[128:131], a[228:231], v[72:75], v[128:131]\n"
  "v_exp_f32 v141, v141\n"
  "v_mfma_f32_16x16x32_bf16 v[128:131], a[232:235], v[80:83], v[128:131]\n"
  "ds_read_b64_tr_b16 v[22:23], v196 offset:4608\n"
  "v_cvt_pk_bf16_f32 v150, v140, v141\n"
  "v_mfma_f32_16x16x32_bf16 v[128:131], a[236:239], v[88:91], v[128:131]\n"
  "v_exp_f32 v142, v142\n"
  "v_mfma_f32_16x16x32_bf16 v[132:135], a[240:243], v[64:67], v[160:163]\n"
  "ds_read_b128 a[212:215], v193 offset:4096\n"
  "v_mfma_f32_16x16x32_bf16 v[132:135], a[244:247], v[72:75], v[132:135]\n"
  "v_exp_f32 v143, v143\n"
  "v_mfma_f32_16x16x32_bf16 v[132:135], a[248:251], v[80:83], v[132:135]\n"
  "v_cvt_pk_bf16_f32 v151, v142, v143\n"
  "v_mfma_f32_16x16x32_bf16 v[132:135], a[252:255], v[88:91], v[132:135]\n"
  "buffer_load_dwordx4 v200, %[srdK], s62 offen offset:2048 lds\n"
  "v_mfma_f32_16x16x32_bf16 a[32:35], v[68:71], v[148:151], a[32:35]\n"
  "v_exp_f32 v128, v128\n"
  "v_mfma_f32_16x16x32_bf16 a[36:39], v[76:79], v[148:151], a[36:39]\n"
  "ds_read_b64_tr_b16 v[28:29], v197 offset:512\n"
  "v_mfma_f32_16x16x32_bf16 a[40:43], v[84:87], v[148:151], a[40:43]\n"
  "v_exp_f32 v129, v129\n"
  "v_mfma_f32_16x16x32_bf16 a[44:47], v[92:95], v[148:151], a[44:47]\n"
  "ds_read_b64_tr_b16 v[30:31], v197 offset:4608\n"
  "v_cvt_pk_bf16_f32 v144, v128, v129\n"
  "v_mfma_f32_16x16x32_bf16 a[48:51], v[100:103], v[148:151], a[48:51]\n"
  "v_exp_f32 v130, v130\n"
  "v_mfma_f32_16x16x32_bf16 a[52:55], v[108:111], v[148:151], a[52:55]\n"
  "ds_read_b128 a[216:219], v194 offset:4096\n"
  "v_mfma_f32_16x16x32_bf16 a[56:59], v[116:119], v[148:151], a[56:59]\n"
  "v_exp_f32 v131, v131\n"
  "v_mfma_f32_16x16x32_bf16 a[60:63], v[124:127], v[148:151], a[60:63]\n"
  "v_cvt_pk_bf16_f32 v145, v130, v131\n"
  "v_mfma_f32_16x16x32_bf16 a[132:135], v[168:171], v[148:151], a[132:135]\n"
  "v_exp_f32 v132, v132\n"
  "v_mfma_f32_16x16x32_bf16 v[136:139], a[224:227], v[96:99], v[164:167]\n"
  "ds_read_b64_tr_b16 v[36:37], v196 offset:1024\n"
  "v_mfma_f32_16x16x32_bf16 v[136:139], a[228:231], v[104:107], v[136:139]\n"
  "v_exp_f32 v133, v133\n"
  "v_mfma_f32_16x16x32_bf16 v[136:139], a[232:235], v[112:115], v[136:139]\n"
  "ds_read_b64_tr_b16 v[38:39], v196 offset:5120\n"
  "v_cvt_pk_bf16_f32 v146, v132, v133\n"
  "v_mfma_f32_16x16x32_bf16 v[136:139], a[236:239], v[120:123], v[136:139]\n"
  "v_exp_f32 v134, v134\n"
  "v_mfma_f32_16x16x32_bf16 v[140:143], a[240:243], v[96:99], v[164:167]\n"
  "ds_read_b128 a[220:223], v195 offset:4096\n"
  "v_mfma_f32_16x16x32_bf16 v[140:143], a[244:247], v[104:107], v[140:143]\n"
  "v_exp_f32 v135, v135\n"
  "v_mfma_f32_16x16x32_bf16 v[140:143], a[248:251], v[112:115], v[140:143]\n"
  "v_cvt_pk_bf16_f32 v147, v134, v135\n"
  "v_mfma_f32_16x16x32_bf16 v[140:143], a[252:255], v[120:123], v[140:143]\n"
  "buffer_load_dwordx4 v201, %[srdK], s62 offen offset:3072 lds\n"
  "s_add_u32 s63, s63, 1\n"
  "s_cmp_eq_u32 s63, 8\n"
  "s_cbranch_scc1 L_attn_exit%=\n"
  "v_mfma_f32_16x16x32_bf16 a[64:67], v[68:71], v[144:147], a[64:67]\n"
  "v_exp_f32 v136, v136\n"
  "v_mfma_f32_16x16x32_bf16 a[68:71], v[76:79], v[144:147], a[68:71]\n"
  "ds_read_b64_tr_b16 v[44:45], v197 offset:1024\n"
  "v_mfma_f32_16x16x32_bf16 a[72:75], v[84:87], v[144:147], a[72:75]\n"
  "v_exp_f32 v137, v137\n"
  "v_mfma_f32_16x16x32_bf16 a[76:79], v[92:95], v[144:147], a[76:79]\n"
  "ds_read_b64_tr_b16 v[46:47], v197 offset:5120\n"
  "v_cvt_pk_bf16_f32 v148, v136, v137\n"
  "v_mfma_f32_16x16x32_bf16 a[80:83], v[100:103], v[144:147], a[80:83]\n"
  "v_exp_f32 v138, v138\n"
  "v_mfma_f32_16x16x32_bf16 a[84:87], v[108:111], v[144:147], a[84:87]\n"
  "ds_read_b128 a[224:227], v192 offset:8192\n"
  "v_mfma_f32_16x16x32_bf16 a[88:91], v[116:119], v[144:147], a[88:91]\n"
  "v_exp_f32 v139, v139\n"
  "v_mfma_f32_16x16x32_bf16 a[92:95], v[124:127], v[144:147], a[92:95]\n"
  "v_cvt_pk_bf16_f32 v149, v138, v139\n"
  "v_mfma_f32_16x16x32_bf16 a[136:139], v[168:171], v[144:147], a[136:139]\n"
  "s_waitcnt lgkmcnt(3)\n"
  "v_exp_f32 v140, v140\n"
  "v_mfma_f32_16x16x32_bf16 v[128:131], a[192:195], v[0:3], v[152:155]\n"
  "ds_read_b64_tr_b16 v[52:53], v196 offset:1536\n"
  "v_mfma_f32_16x16x32_bf16 v[128:131], a[196:199], v[8:11], v[128:131]\n"
  "v_exp_f32 v141, v141\n"
  "v_mfma_f32_16x16x32_bf16 v[128:131], a[200:203], v[16:19], v[128:131]\n"
  "ds_read_b64_tr_b16 v[54:55], v196 offset:5632\n"
  "v_cvt_pk_bf16_f32 v150, v140, v141\n"
  "v_mfma_f32_16x16x32_bf16 v[128:131], a[204:207], v[24:27], v[128:131]\n"
  "v_exp_f32 v142, v142\n"
  "v_mfma_f32_16x16x32_bf16 v[132:135], a[208:211], v[0:3], v[152:155]\n"
  "ds_read_b128 a[228:231], v193 offset:8192\n"
  "v_mfma_f32_16x16x32_bf16 v[132:135], a[212:215], v[8:11], v[132:135]\n"
  "s_add_u32 m0, %[sM0], 98304\n"
  "v_exp_f32 v143, v143\n"
  "v_mfma_f32_16x16x32_bf16 v[132:135], a[216:219], v[16:19], v[132:135]\n"
  "v_cvt_pk_bf16_f32 v151, v142, v143\n"
  "v_mfma_f32_16x16x32_bf16 v[132:135], a[220:223], v[24:27], v[132:135]\n"
  "buffer_load_dwordx4 v202, %[srdV], s62 offen offset:0 lds\n"
  "s_branch L_attn_top%=\n"
  "L_attn_exit%=:\n"
  "v_mfma_f32_16x16x32_bf16 a[64:67], v[68:71], v[144:147], a[64:67]\n"
  "v_exp_f32 v136, v136\n"
  "v_mfma_f32_16x16x32_bf16 a[68:71], v[76:79], v[144:147], a[68:71]\n"
  "v_mfma_f32_16x16x32_bf16 a[72:75], v[84:87], v[144:147], a[72:75]\n"
  "v_exp_f32 v137, v137\n"
  "v_mfma_f32_16x16x32_bf16 a[76:79], v[92:95], v[144:147], a[76:79]\n"
  "v_cvt_pk_bf16_f32 v148, v136, v137\n"
  "v_mfma_f32_16x16x32_bf16 a[80:83], v[100:103], v[144:147], a[80:83]\n"
  "v_exp_f32 v138, v138\n"
  "v_mfma_f32_16x16x32_bf16 a[84:87], v[108:111], v[144:147], a[84:87]\n"
  "v_mfma_f32_16x16x32_bf16 a[88:91], v[116:119], v[144:147], a[88:91]\n"
  "v_exp_f32 v139, v139\n"
  "v_mfma_f32_16x16x32_bf16 a[92:95], v[124:127], v[144:147], a[92:95]\n"
  "v_cvt_pk_bf16_f32 v149, v138, v139\n"
  "v_mfma_f32_16x16x32_bf16 a[136:139], v[168:171], v[144:147], a[136:139]\n"
  "v_exp_f32 v140, v140\n"
  "v_exp_f32 v141, v141\n"
  "s_nop 0\n"
  "v_cvt_pk_bf16_f32 v150, v140, v141\n"
  "v_exp_f32 v142, v142\n"
  "v_exp_f32 v143, v143\n"
  "s_nop 0\n"
  "v_cvt_pk_bf16_f32 v151, v142, v143\n"
  "s_nop 1\n"
  "v_mfma_f32_16x16x32_bf16 a[96:99], v[68:71], v[148:151], a[96:99]\n"
  "v_mfma_f32_16x16x32_bf16 a[100:103], v[76:79], v[148:151], a[100:103]\n"
  "v_mfma_f32_16x16x32_bf16 a[104:107], v[84:87], v[148:151], a[104:107]\n"
  "v_mfma_f32_16x16x32_bf16 a[108:111], v[92:95], v[148:151], a[108:111]\n"
  "v_mfma_f32_16x16x32_bf16 a[112:115], v[100:103], v[148:151], a[112:115]\n"
  "v_mfma_f32_16x16x32_bf16 a[116:119], v[108:111], v[148:151], a[116:119]\n"
  "v_mfma_f32_16x16x32_bf16 a[120:123], v[116:119], v[148:151], a[120:123]\n"
  "v_mfma_f32_16x16x32_bf16 a[124:127], v[124:127], v[148:151], a[124:127]\n"
  "v_mfma_f32_16x16x32_bf16 a[140:143], v[168:171], v[148:151], a[140:143]\n"
  "v_accvgpr_read_b32 v128, a128\n"
  "v_accvgpr_read_b32 v129, a132\n"
  "v_accvgpr_read_b32 v130, a136\n"
  "s_nop 4\n"
  "v_accvgpr_read_b32 v131, a140\n"
  "v_mov_b32 v132, v128\n"
  "v_mov_b32 v133, v129\n"
  "v_mov_b32 v134, v130\n"
  "v_mov_b32 v135, v131\n"
  "v_permlane16_swap_b32 v128, v132\n"
  "v_permlane16_swap_b32 v129, v133\n"
  "v_permlane16_swap_b32 v130, v134\n"
  "v_permlane16_swap_b32 v131, v135\n"
  "v_add_f32 v128, v128, v132\n"
  "v_add_f32 v129, v129, v133\n"
  "v_add_f32 v130, v130, v134\n"
  "v_add_f32 v131, v131, v135\n"
  "v_mov_b32 v132, v128\n"
  "v_mov_b32 v133, v129\n"
  "v_mov_b32 v134, v130\n"
  "v_mov_b32 v135, v131\n"
  "v_permlane32_swap_b32 v128, v132\n"
  "v_permlane32_swap_b32 v129, v133\n"
  "v_permlane32_swap_b32 v130, v134\n"
  "v_permlane32_swap_b32 v131, v135\n"
  "v_add_f32 v128, v128, v132\n"
  "v_add_f32 v129, v129, v133\n"
  "v_add_f32 v130, v130, v134\n"
  "v_add_f32 v131, v131, v135\n"
  "s_mov_b32 s60, 0x71800000\n"
  "v_add_f32 v208, v128, v129\n"
  "v_add_f32 v209, v130, v131\n"
  "v_add_f32 v208, v208, v209\n"
  "v_cmp_ngt_f32 %[bad], s60, v208\n"
  "v_rcp_f32 v136, v128\n"
  "v_rcp_f32 v138, v129\n"
  "v_rcp_f32 v140, v130\n"
  "v_rcp_f32 v142, v131\n"
  "s_add_u32 s60, %[sOoff], 0\n"
  "v_accvgpr_read_b32 v144, a0\n"
  "v_accvgpr_read_b32 v145, a1\n"
  "v_accvgpr_read_b32 v146, a2\n"
  "v_accvgpr_read_b32 v147, a3\n"
  "v_accvgpr_read_b32 v148, a4\n"
  "v_accvgpr_read_b32 v149, a5\n"
  "v_accvgpr_read_b32 v150, a6\n"
  "v_accvgpr_read_b32 v151, a7\n"
  "v_pk_mul_f32 v[144:145], v[144:145], v[136:137] op_sel_hi:[1,0]\n"
  "v_pk_mul_f32 v[146:147], v[146:147], v[136:137] op_sel_hi:[1,0]\n"
  "v_pk_mul_f32 v[148:149], v[148:149], v[136:137] op_sel_hi:[1,0]\n"
  "v_pk_mul_f32 v[150:151], v[150:151], v[136:137] op_sel_hi:[1,0]\n"
  "v_pk_fma_f32 v[212:213], v[144:145], 0, v[212:213] op_sel_hi:[1,0,1]\n"
  "v_pk_fma_f32 v[212:213], v[146:147], 0, v[212:213] op_sel_hi:[1,0,1]\n"
  "v_pk_fma_f32 v[212:213], v[148:149], 0, v[212:213] op_sel_hi:[1,0,1]\n"
  "v_pk_fma_f32 v[212:213], v[150:151], 0, v[212:213] op_sel_hi:[1,0,1]\n"
  "v_cvt_pk_bf16_f32 v144, v144, v145\n"
  "v_cvt_pk_bf16_f32 v145, v146, v147\n"
  "v_cvt_pk_bf16_f32 v146, v148, v149\n"
  "v_cvt_pk_bf16_f32 v147, v150, v151\n"
  "buffer_store_dwordx4 v[144:147], %[vL16], %[srdO], s60 offen offset:0 sc1\n"
  "v_accvgpr_read_b32 v152, a8\n"
  "v_accvgpr_read_b32 v153, a9\n"
  "v_accvgpr_read_b32 v154, a10\n"
  "v_accvgpr_read_b32 v155, a11\n"
  "v_accvgpr_read_b32 v156, a12\n"
  "v_accvgpr_read_b32 v157, a13\n"
  "v_accvgpr_read_b32 v158, a14\n"
  "v_accvgpr_read_b32 v159, a15\n"
  "v_pk_mul_f32 v[152:153], v[152:153], v[136:137] op_sel_hi:[1,0]\n"
  "v_pk_mul_f32 v[154:155], v[154:155], v[136:137] op_sel_hi:[1,0]\n"
  "v_pk_mul_f32 v[156:157], v[156:157], v[136:137] op_sel_hi:[1,0]\n"
  "v_pk_mul_f32 v[158:159], v[158:159], v[136:137] op_sel_hi:[1,0]\n"
  "v_pk_fma_f32 v[212:213], v[152:153], 0, v[212:213] op_sel_hi:[1,0,1]\n"
  "v_pk_fma_f32 v[212:213], v[154:155], 0, v[212:213] op_sel_hi:[1,0,1]\n"
  "v_pk_fma_f32 v[212:213], v[156:157], 0, v[212:213] op_sel_hi:[1,0,1]\n"
  "v_pk_fma_f32 v[212:213], v[158:159], 0, v[212:213] op_sel_hi:[1,0,1]\n"
  "v_cvt_pk_bf16_f32 v152, v152, v153\n"
  "v_cvt_pk_bf16_f32 v153, v154, v155\n"
  "v_cvt_pk_bf16_f32 v154, v156, v157\n"
  "v_cvt_pk_bf16_f32 v155, v158, v159\n"
  "buffer_store_dwordx4 v[152:155], %[vL16], %[srdO], s60 offen offset:1024 sc1\n"
  "v_accvgpr_read_b32 v160, a16\n"
  "v_accvgpr_read_b32 v161, a17\n"
  "v_accvgpr_read_b32 v162, a18\n"
  "v_accvgpr_read_b32 v163, a19\n"
  "v_accvgpr_read_b32 v164, a20\n"
  "v_accvgpr_read_b32 v165, a21\n"
  "v_accvgpr_read_b32 v166, a22\n"
  "v_accvgpr_read_b32 v167, a23\n"
  "v_pk_mul_f32 v[160:161], v[160:161], v[136:137] op_sel_hi:[1,0]\n"
  "v_pk_mul_f32 v[162:163], v[162:163], v[136:137] op_sel_hi:[1,0]\n"
  "v_pk_mul_f32 v[164:165], v[164:165], v[136:137] op_sel_hi:[1,0]\n"
  "v_pk_mul_f32 v[166:167], v[166:167], v[136:137] op_sel_hi:[1,0]\n"
  "v_pk_fma_f32 v[212:213], v[160:161], 0, v[212:213] op_sel_hi:[1,0,1]\n"
  "v_pk_fma_f32 v[212:213], v[162:163], 0, v[212:213] op_sel_hi:[1,0,1]\n"
  "v_pk_fma_f32 v[212:213], v[164:165], 0, v[212:213] op_sel_hi:[1,0,1]\n"
  "v_pk_fma_f32 v[212:213], v[166:167], 0, v[212:213] op_sel_hi:[1,0,1]\n"
  "v_cvt_pk_bf16_f32 v160, v160, v161\n"
  "v_cvt_pk_bf16_f32 v161, v162, v163\n"
  "v_cvt_pk_bf16_f32 v162, v164, v165\n"
  "v_cvt_pk_bf16_f32 v163, v166, v167\n"
  "buffer_store_dwordx4 v[160:163], %[vL16], %[srdO], s60 offen offset:2048 sc1\n"
  "v_accvgpr_read_b32 v168, a24\n"
  "v_accvgpr_read_b32 v169, a25\n"
  "v_accvgpr_read_b32 v170, a26\n"
  "v_accvgpr_read_b32 v171, a27\n"
  "v_accvgpr_read_b32 v172, a28\n"
  "v_accvgpr_read_b32 v173, a29\n"
  "v_accvgpr_read_b32 v174, a30\n"
  "v_accvgpr_read_b32 v175, a31\n"
  "v_pk_mul_f32 v[168:169], v[168:169], v[136:137] op_sel_hi:[1,0]\n"
  "v_pk_mul_f32 v[170:171], v[170:171], v[136:137] op_sel_hi:[1,0]\n"
  "v_pk_mul_f32 v[172:173], v[172:173], v[136:137] op_sel_hi:[1,0]\n"
  "v_pk_mul_f32 v[174:175], v[174:175], v[136:137] op_sel_hi:[1,0]\n"
  "v_pk_fma_f32 v[212:213], v[168:169], 0, v[212:213] op_sel_hi:[1,0,1]\n"
  "v_pk_fma_f32 v[212:213], v[170:171], 0, v[212:213] op_sel_hi:[1,0,1]\n"
  "v_pk_fma_f32 v[212:213], v[172:173], 0, v[212:213] op_sel_hi:[1,0,1]\n"
  "v_pk_fma_f32 v[212:213], v[174:175], 0, v[212:213] op_sel_hi:[1,0,1]\n"
  "v_cvt_pk_bf16_f32 v168, v168, v169\n"
  "v_cvt_pk_bf16_f32 v169, v170, v171\n"
  "v_cvt_pk_bf16_f32 v170, v172, v173\n"
  "v_cvt_pk_bf16_f32 v171, v174, v175\n"
  "buffer_store_dwordx4 v[168:171], %[vL16], %[srdO], s60 offen offset:3072 sc1\n"
  "s_add_u32 s61, %[sOoff], 131072\n"
  "v_accvgpr_read_b32 v176, a32\n"
  "v_accvgpr_read_b32 v177, a33\n"
  "v_accvgpr_read_b32 v178, a34\n"
  "v_accvgpr_read_b32 v179, a35\n"
  "v_accvgpr_read_b32 v180, a36\n"
  "v_accvgpr_read_b32 v181, a37\n"
  "v_accvgpr_read_b32 v182, a38\n"
  "v_accvgpr_read_b32 v183, a39\n"
  "v_pk_mul_f32 v[176:177], v[176:177], v[138:139] op_sel_hi:[1,0]\n"
  "v_pk_mul_f32 v[178:179], v[178:179], v[138:139] op_sel_hi:[1,0]\n"
  "v_pk_mul_f32 v[180:181], v[180:181], v[138:139] op_sel_hi:[1,0]\n"
  "v_pk_mul_f32 v[182:183], v[182:183], v[138:139] op_sel_hi:[1,0]\n"
  "v_pk_fma_f32 v[212:213], v[176:177], 0, v[212:213] op_sel_hi:[1,0,1]\n"
  "v_pk_fma_f32 v[212:213], v[178:179], 0, v[212:213] op_sel_hi:[1,0,1]\n"
  "v_pk_fma_f32 v[212:213], v[180:181], 0, v[212:213] op_sel_hi:[1,0,1]\n"
  "v_pk_fma_f32 v[212:213], v[182:183], 0, v[212:213] op_sel_hi:[1,0,1]\n"
  "v_cvt_pk_bf16_f32 v176, v176, v177\n"
  "v_cvt_pk_bf16_f32 v177, v178, v179\n"
  "v_cvt_pk_bf16_f32 v178, v180, v181\n"
  "v_cvt_pk_bf16_f32 v179, v182, v183\n"
  "buffer_store_dwordx4 v[176:179], %[vL16], %[srdO], s61 offen offset:0 sc1\n"
  "v_accvgpr_read_b32 v184, a40\n"
  "v_accvgpr_read_b32 v185, a41\n"
  "v_accvgpr_read_b32 v186, a42\n"
  "v_accvgpr_read_b32 v187, a43\n"
  "v_accvgpr_read_b32 v188, a44\n"
  "v_accvgpr_read_b32 v189, a45\n"
  "v_accvgpr_read_b32 v190, a46\n"
  "v_accvgpr_read_b32 v191, a47\n"
  "v_pk_mul_f32 v[184:185], v[184:185], v[138:139] op_sel_hi:[1,0]\n"
  "v_pk_mul_f32 v[186:187], v[186:187], v[138:139] op_sel_hi:[1,0]\n"
  "v_pk_mul_f32 v[188:189], v[188:189], v[138:139] op_sel_hi:[1,0]\n"
  "v_pk_mul_f32 v[190:191], v[190:191], v[138:139] op_sel_hi:[1,0]\n"
  "v_pk_fma_f32 v[212:213], v[184:185], 0, v[212:213] op_sel_hi:[1,0,1]\n"
  "v_pk_fma_f32 v[212:213], v[186:187], 0, v[212:213] op_sel_hi:[1,0,1]\n"
  "v_pk_fma_f32 v[212:213], v[188:189], 0, v[212:213] op_sel_hi:[1,0,1]\n"
  "v_pk_fma_f32 v[212:213], v[190:191], 0, v[212:213] op_sel_hi:[1,0,1]\n"
  "v_cvt_pk_bf16_f32 v184, v184, v185\n"
  "v_cvt_pk_bf16_f32 v185, v186, v187\n"
  "v_cvt_pk_bf16_f32 v186, v188, v189\n"
  "v_cvt_pk_bf16_f32 v187, v190, v191\n"
  "buffer_store_dwordx4 v[184:187], %[vL16], %[srdO], s61 offen offset:1024 sc1\n"
  "v_accvgpr_read_b32 v144, a48\n"
  "v_accvgpr_read_b32 v145, a49\n"
  "v_accvgpr_read_b32 v146, a50\n"
  "v_accvgpr_read_b32 v147, a51\n"
  "v_accvgpr_read_b32 v148, a52\n"
  "v_accvgpr_read_b32 v149, a53\n"
  "v_accvgpr_read_b32 v150, a54\n"
  "v_accvgpr_read_b32 v151, a55\n"
  "v_pk_mul_f32 v[144:145], v[144:145], v[138:139] op_sel_hi:[1,0]\n"
  "v_pk_mul_f32 v[146:147], v[146:147], v[138:139] op_sel_hi:[1,0]\n"
  "v_pk_mul_f32 v[148:149], v[148:149], v[138:139] op_sel_hi:[1,0]\n"
  "v_pk_mul_f32 v[150:151], v[150:151], v[138:139] op_sel_hi:[1,0]\n"
  "v_pk_fma_f32 v[212:213], v[144:145], 0, v[212:213] op_sel_hi:[1,0,1]\n"
  "v_pk_fma_f32 v[212:213], v[146:147], 0, v[212:213] op_sel_hi:[1,0,1]\n"
  "v_pk_fma_f32 v[212:213], v[148:149], 0, v[212:213] op_sel_hi:[1,0,1]\n"
  "v_pk_fma_f32 v[212:213], v[150:151], 0, v[212:213] op_sel_hi:[1,0,1]\n"
  "v_cvt_pk_bf16_f32 v144, v144, v145\n"
  "v_cvt_pk_bf16_f32 v145, v146, v147\n"
  "v_cvt_pk_bf16_f32 v146, v148, v149\n"
  "v_cvt_pk_bf16_f32 v147, v150, v151\n"
  "buffer_store_dwordx4 v[144:147], %[vL16], %[srdO], s61 offen offset:2048 sc1\n"
  "v_accvgpr_read_b32 v152, a56\n"
  "v_accvgpr_read_b32 v153, a57\n"
  "v_accvgpr_read_b32 v154, a58\n"
  "v_accvgpr_read_b32 v155, a59\n"
  "v_accvgpr_read_b32 v156, a60\n"
  "v_accvgpr_read_b32 v157, a61\n"
  "v_accvgpr_read_b32 v158, a62\n"
  "v_accvgpr_read_b32 v159, a63\n"
  "v_pk_mul_f32 v[152:153], v[152:153], v[138:139] op_sel_hi:[1,0]\n"
  "v_pk_mul_f32 v[154:155], v[154:155], v[138:139] op_sel_hi:[1,0]\n"
  "v_pk_mul_f32 v[156:157], v[156:157], v[138:139] op_sel_hi:[1,0]\n"
  "v_pk_mul_f32 v[158:159], v[158:159], v[138:139] op_sel_hi:[1,0]\n"
  "v_pk_fma_f32 v[212:213], v[152:153], 0, v[212:213] op_sel_hi:[1,0,1]\n"
  "v_pk_fma_f32 v[212:213], v[154:155], 0, v[212:213] op_sel_hi:[1,0,1]\n"
  "v_pk_fma_f32 v[212:213], v[156:157], 0, v[212:213] op_sel_hi:[1,0,1]\n"
  "v_pk_fma_f32 v[212:213], v[158:159], 0, v[212:213] op_sel_hi:[1,0,1]\n"
  "v_cvt_pk_bf16_f32 v152, v152, v153\n"
  "v_cvt_pk_bf16_f32 v153, v154, v155\n"
  "v_cvt_pk_bf16_f32 v154, v156, v157\n"
  "v_cvt_pk_bf16_f32 v155, v158, v159\n"
  "buffer_store_dwordx4 v[152:155], %[vL16], %[srdO], s61 offen offset:3072 sc1\n"
  "s_add_u32 s60, %[sOoff], 262144\n"
  "v_accvgpr_read_b32 v160, a64\n"
  "v_accvgpr_read_b32 v161, a65\n"
  "v_accvgpr_read_b32 v162, a66\n"
  "v_accvgpr_read_b32 v163, a67\n"
  "v_accvgpr_read_b32 v164, a68\n"
  "v_accvgpr_read_b32 v165, a69\n"
  "v_accvgpr_read_b32 v166, a70\n"
  "v_accvgpr_read_b32 v167, a71\n"
  "v_pk_mul_f32 v[160:161], v[160:161], v[140:141] op_sel_hi:[1,0]\n"
  "v_pk_mul_f32 v[162:163], v[162:163], v[140:141] op_sel_hi:[1,0]\n"
  "v_pk_mul_f32 v[164:165], v[164:165], v[140:141] op_sel_hi:[1,0]\n"
  "v_pk_mul_f32 v[166:167], v[166:167], v[140:141] op_sel_hi:[1,0]\n"
  "v_pk_fma_f32 v[212:213], v[160:161], 0, v[212:213] op_sel_hi:[1,0,1]\n"
  "v_pk_fma_f32 v[212:213], v[162:163], 0, v[212:213] op_sel_hi:[1,0,1]\n"
  "v_pk_fma_f32 v[212:213], v[164:165], 0, v[212:213] op_sel_hi:[1,0,1]\n"
  "v_pk_fma_f32 v[212:213], v[166:167], 0, v[212:213] op_sel_hi:[1,0,1]\n"
  "v_cvt_pk_bf16_f32 v160, v160, v161\n"
  "v_cvt_pk_bf16_f32 v161, v162, v163\n"
  "v_cvt_pk_bf16_f32 v162, v164, v165\n"
  "v_cvt_pk_bf16_f32 v163, v166, v167\n"
  "buffer_store_dwordx4 v[160:163], %[vL16], %[srdO], s60 offen offset:0 sc1\n"
  "v_accvgpr_read_b32 v168, a72\n"
  "v_accvgpr_read_b32 v169, a73\n"
  "v_accvgpr_read_b32 v170, a74\n"
  "v_accvgpr_read_b32 v171, a75\n"
  "v_accvgpr_read_b32 v172, a76\n"
  "v_accvgpr_read_b32 v173, a77\n"
  "v_accvgpr_read_b32 v174, a78\n"
  "v_accvgpr_read_b32 v175, a79\n"
  "v_pk_mul_f32 v[168:169], v[168:169], v[140:141] op_sel_hi:[1,0]\n"
  "v_pk_mul_f32 v[170:171], v[170:171], v[140:141] op_sel_hi:[1,0]\n"
  "v_pk_mul_f32 v[172:173], v[172:173], v[140:141] op_sel_hi:[1,0]\n"
  "v_pk_mul_f32 v[174:175], v[174:175], v[140:141] op_sel_hi:[1,0]\n"
  "v_pk_fma_f32 v[212:213], v[168:169], 0, v[212:213] op_sel_hi:[1,0,1]\n"
  "v_pk_fma_f32 v[212:213], v[170:171], 0, v[212:213] op_sel_hi:[1,0,1]\n"
  "v_pk_fma_f32 v[212:213], v[172:173], 0, v[212:213] op_sel_hi:[1,0,1]\n"
  "v_pk_fma_f32 v[212:213], v[174:175], 0, v[212:213] op_sel_hi:[1,0,1]\n"
  "v_cvt_pk_bf16_f32 v168, v168, v169\n"
  "v_cvt_pk_bf16_f32 v169, v170, v171\n"
  "v_cvt_pk_bf16_f32 v170, v172, v173\n"
  "v_cvt_pk_bf16_f32 v171, v174, v175\n"
  "buffer_store_dwordx4 v[168:171], %[vL16], %[srdO], s60 offen offset:1024 sc1\n"
  "v_accvgpr_read_b32 v176, a80\n"
  "v_accvgpr_read_b32 v177, a81\n"
  "v_accvgpr_read_b32 v178, a82\n"
  "v_accvgpr_read_b32 v179, a83\n"
  "v_accvgpr_read_b32 v180, a84\n"
  "v_accvgpr_read_b32 v181, a85\n"
  "v_accvgpr_read_b32 v182, a86\n"
  "v_accvgpr_read_b32 v183, a87\n"
  "v_pk_mul_f32 v[176:177], v[176:177], v[140:141] op_sel_hi:[1,0]\n"
  "v_pk_mul_f32 v[178:179], v[178:179], v[140:141] op_sel_hi:[1,0]\n"
  "v_pk_mul_f32 v[180:181], v[180:181], v[140:141] op_sel_hi:[1,0]\n"
  "v_pk_mul_f32 v[182:183], v[182:183], v[140:141] op_sel_hi:[1,0]\n"
  "v_pk_fma_f32 v[212:213], v[176:177], 0, v[212:213] op_sel_hi:[1,0,1]\n"
  "v_pk_fma_f32 v[212:213], v[178:179], 0, v[212:213] op_sel_hi:[1,0,1]\n"
  "v_pk_fma_f32 v[212:213], v[180:181], 0, v[212:213] op_sel_hi:[1,0,1]\n"
  "v_pk_fma_f32 v[212:213], v[182:183], 0, v[212:213] op_sel_hi:[1,0,1]\n"
  "v_cvt_pk_bf16_f32 v176, v176, v177\n"
  "v_cvt_pk_bf16_f32 v177, v178, v179\n"
  "v_cvt_pk_bf16_f32 v178, v180, v181\n"
  "v_cvt_pk_bf16_f32 v179, v182, v183\n"
  "buffer_store_dwordx4 v[176:179], %[vL16], %[srdO], s60 offen offset:2048 sc1\n"
  "v_accvgpr_read_b32 v184, a88\n"
  "v_accvgpr_read_b32 v185, a89\n"
  "v_accvgpr_read_b32 v186, a90\n"
  "v_accvgpr_read_b32 v187, a91\n"
  "v_accvgpr_read_b32 v188, a92\n"
  "v_accvgpr_read_b32 v189, a93\n"
  "v_accvgpr_read_b32 v190, a94\n"
  "v_accvgpr_read_b32 v191, a95\n"
  "v_pk_mul_f32 v[184:185], v[184:185], v[140:141] op_sel_hi:[1,0]\n"
  "v_pk_mul_f32 v[186:187], v[186:187], v[140:141] op_sel_hi:[1,0]\n"
  "v_pk_mul_f32 v[188:189], v[188:189], v[140:141] op_sel_hi:[1,0]\n"
  "v_pk_mul_f32 v[190:191], v[190:191], v[140:141] op_sel_hi:[1,0]\n"
  "v_pk_fma_f32 v[212:213], v[184:185], 0, v[212:213] op_sel_hi:[1,0,1]\n"
  "v_pk_fma_f32 v[212:213], v[186:187], 0, v[212:213] op_sel_hi:[1,0,1]\n"
  "v_pk_fma_f32 v[212:213], v[188:189], 0, v[212:213] op_sel_hi:[1,0,1]\n"
  "v_pk_fma_f32 v[212:213], v[190:191], 0, v[212:213] op_sel_hi:[1,0,1]\n"
  "v_cvt_pk_bf16_f32 v184, v184, v185\n"
  "v_cvt_pk_bf16_f32 v185, v186, v187\n"
  "v_cvt_pk_bf16_f32 v186, v188, v189\n"
  "v_cvt_pk_bf16_f32 v187, v190, v191\n"
  "buffer_store_dwordx4 v[184:187], %[vL16], %[srdO], s60 offen offset:3072 sc1\n"
  "s_add_u32 s61, %[sOoff], 393216\n"
  "v_accvgpr_read_b32 v144, a96\n"
  "v_accvgpr_read_b32 v145, a97\n"
  "v_accvgpr_read_b32 v146, a98\n"
  "v_accvgpr_read_b32 v147, a99\n"
  "v_accvgpr_read_b32 v148, a100\n"
  "v_accvgpr_read_b32 v149, a101\n"
  "v_accvgpr_read_b32 v150, a102\n"
  "v_accvgpr_read_b32 v151, a103\n"
  "v_pk_mul_f32 v[144:145], v[144:145], v[142:143] op_sel_hi:[1,0]\n"
  "v_pk_mul_f32 v[146:147], v[146:147], v[142:143] op_sel_hi:[1,0]\n"
  "v_pk_mul_f32 v[148:149], v[148:149], v[142:143] op_sel_hi:[1,0]\n"
  "v_pk_mul_f32 v[150:151], v[150:151], v[142:143] op_sel_hi:[1,0]\n"
  "v_pk_fma_f32 v[212:213], v[144:145], 0, v[212:213] op_sel_hi:[1,0,1]\n"
  "v_pk_fma_f32 v[212:213], v[146:147], 0, v[212:213] op_sel_hi:[1,0,1]\n"
  "v_pk_fma_f32 v[212:213], v[148:149], 0, v[212:213] op_sel_hi:[1,0,1]\n"
  "v_pk_fma_f32 v[212:213], v[150:151], 0, v[212:213] op_sel_hi:[1,0,1]\n"
  "v_cvt_pk_bf16_f32 v144, v144, v145\n"
  "v_cvt_pk_bf16_f32 v145, v146, v147\n"
  "v_cvt_pk_bf16_f32 v146, v148, v149\n"
  "v_cvt_pk_bf16_f32 v147, v150, v151\n"
  "buffer_store_dwordx4 v[144:147], %[vL16], %[srdO], s61 offen offset:0 sc1\n"
  "v_accvgpr_read_b32 v152, a104\n"
  "v_accvgpr_read_b32 v153, a105\n"
  "v_accvgpr_read_b32 v154, a106\n"
  "v_accvgpr_read_b32 v155, a107\n"
  "v_accvgpr_read_b32 v156, a108\n"
  "v_accvgpr_read_b32 v157, a109\n"
  "v_accvgpr_read_b32 v158, a110\n"
  "v_accvgpr_read_b32 v159, a111\n"
  "v_pk_mul_f32 v[152:153], v[152:153], v[142:143] op_sel_hi:[1,0]\n"
  "v_pk_mul_f32 v[154:155], v[154:155], v[142:143] op_sel_hi:[1,0]\n"
  "v_pk_mul_f32 v[156:157], v[156:157], v[142:143] op_sel_hi:[1,0]\n"
  "v_pk_mul_f32 v[158:159], v[158:159], v[142:143] op_sel_hi:[1,0]\n"
  "v_pk_fma_f32 v[212:213], v[152:153], 0, v[212:213] op_sel_hi:[1,0,1]\n"
  "v_pk_fma_f32 v[212:213], v[154:155], 0, v[212:213] op_sel_hi:[1,0,1]\n"
  "v_pk_fma_f32 v[212:213], v[156:157], 0, v[212:213] op_sel_hi:[1,0,1]\n"
  "v_pk_fma_f32 v[212:213], v[158:159], 0, v[212:213] op_sel_hi:[1,0,1]\n"
  "v_cvt_pk_bf16_f32 v152, v152, v153\n"
  "v_cvt_pk_bf16_f32 v153, v154, v155\n"
  "v_cvt_pk_bf16_f32 v154, v156, v157\n"
  "v_cvt_pk_bf16_f32 v155, v158, v159\n"
  "buffer_store_dwordx4 v[152:155], %[vL16], %[srdO], s61 offen offset:1024 sc1\n"
  "v_accvgpr_read_b32 v160, a112\n"
  "v_accvgpr_read_b32 v161, a113\n"
  "v_accvgpr_read_b32 v162, a114\n"
  "v_accvgpr_read_b32 v163, a115\n"
  "v_accvgpr_read_b32 v164, a116\n"
  "v_accvgpr_read_b32 v165, a117\n"
  "v_accvgpr_read_b32 v166, a118\n"
  "v_accvgpr_read_b32 v167, a119\n"
  "v_pk_mul_f32 v[160:161], v[160:161], v[142:143] op_sel_hi:[1,0]\n"
  "v_pk_mul_f32 v[162:163], v[162:163], v[142:143] op_sel_hi:[1,0]\n"
  "v_pk_mul_f32 v[164:165], v[164:165], v[142:143] op_sel_hi:[1,0]\n"
  "v_pk_mul_f32 v[166:167], v[166:167], v[142:143] op_sel_hi:[1,0]\n"
  "v_pk_fma_f32 v[212:213], v[160:161], 0, v[212:213] op_sel_hi:[1,0,1]\n"
  "v_pk_fma_f32 v[212:213], v[162:163], 0, v[212:213] op_sel_hi:[1,0,1]\n"
  "v_pk_fma_f32 v[212:213], v[164:165], 0, v[212:213] op_sel_hi:[1,0,1]\n"
  "v_pk_fma_f32 v[212:213], v[166:167], 0, v[212:213] op_sel_hi:[1,0,1]\n"
  "v_cvt_pk_bf16_f32 v160, v160, v161\n"
  "v_cvt_pk_bf16_f32 v161, v162, v163\n"
  "v_cvt_pk_bf16_f32 v162, v164, v165\n"
  "v_cvt_pk_bf16_f32 v163, v166, v167\n"
  "buffer_store_dwordx4 v[160:163], %[vL16], %[srdO], s61 offen offset:2048 sc1\n"
  "v_accvgpr_read_b32 v168, a120\n"
  "v_accvgpr_read_b32 v169, a121\n"
  "v_accvgpr_read_b32 v170, a122\n"
  "v_accvgpr_read_b32 v171, a123\n"
  "v_accvgpr_read_b32 v172, a124\n"
  "v_accvgpr_read_b32 v173, a125\n"
  "v_accvgpr_read_b32 v174, a126\n"
  "v_accvgpr_read_b32 v175, a127\n"
  "v_pk_mul_f32 v[168:169], v[168:169], v[142:143] op_sel_hi:[1,0]\n"
  "v_pk_mul_f32 v[170:171], v[170:171], v[142:143] op_sel_hi:[1,0]\n"
  "v_pk_mul_f32 v[172:173], v[172:173], v[142:143] op_sel_hi:[1,0]\n"
  "v_pk_mul_f32 v[174:175], v[174:175], v[142:143] op_sel_hi:[1,0]\n"
  "v_pk_fma_f32 v[212:213], v[168:169], 0, v[212:213] op_sel_hi:[1,0,1]\n"
  "v_pk_fma_f32 v[212:213], v[170:171], 0, v[212:213] op_sel_hi:[1,0,1]\n"
  "v_pk_fma_f32 v[212:213], v[172:173], 0, v[212:213] op_sel_hi:[1,0,1]\n"
  "v_pk_fma_f32 v[212:213], v[174:175], 0, v[212:213] op_sel_hi:[1,0,1]\n"
  "v_cvt_pk_bf16_f32 v168, v168, v169\n"
  "v_cvt_pk_bf16_f32 v169, v170, v171\n"
  "v_cvt_pk_bf16_f32 v170, v172, v173\n"
  "v_cvt_pk_bf16_f32 v171, v174, v175\n"
  "buffer_store_dwordx4 v[168:171], %[vL16], %[srdO], s61 offen offset:3072 sc1\n"
  "v_cmp_u_f32 vcc, v212, v213\n"
  "s_nop 0\n"
  "s_or_b64 %[bad], %[bad], vcc\n"
  "s_waitcnt vmcnt(0) lgkmcnt(0)\n"

    : [bad] "=&s"(bad)
    : [srdH] "s"(srdH), [srdW] "s"(srdW), [srdK] "s"(srdK), [srdV] "s"(srdV), [srdO] "s"(srdO),
      [sHoff] "s"(sHoff), [sWoff] "s"(sWoff), [sKV] "s"(sKV), [sM0] "s"(sM0), [sOoff] "s"(sOoff), [pP0] "s"(pP0), [pP1] "s"(pP1), [pP2] "s"(pP2), [pP3] "s"(pP3), [pP4] "s"(pP4),
      [vA0] "v"(vA0), [vB0] "v"(vB0), [vVb] "v"(vVb), [vVd] "v"(vVd), [vWb] "v"(vWb), [vWd] "v"(vWd), [vL16] "v"(vL16)
    : "memory", "vcc", "scc", "s60", "s61", "s62", "s63", "s64", "s65", "s66", "s67", "s68", "s69",
      CLOB_0_99("v"), CLOB_100_239("v"), CLOB_0_99("a"), CLOB_100_239("a"), CLOB_240_255("a"));
  if (bad != 0ull) attn_slow(hfrag, wq, kc, vc, ofrag, head, rb0);
  asm volatile("s_waitcnt vmcnt(0)" ::: "memory");
  if ((vL16 >> 4) == 0u) __hip_atomic_fetch_add((GAS unsigned*)cnt + (xcd * 8u + (r & 3u) * 2u + (wid >> 1)) * 32u, 1u, __ATOMIC_RELAXED, __HIP_MEMORY_SCOPE_AGENT);
  const unsigned sp = xcd >> 1, qt = (xcd & 1u) * 8u + (r >> 2), nt = r & 3u, crb0 = qt * 8u + wid * 2u;
  const __amdgpu_buffer_rsrc_t srdWo = __builtin_amdgcn_make_buffer_rsrc((void*)wo, (short)0, NH * DH * DM * 2, 0x00020000);
  const __amdgpu_buffer_rsrc_t srdP = __builtin_amdgcn_make_buffer_rsrc((void*)part, (short)0, 4 * SEQ * DM * 2, 0x00020000);
  const unsigned cOoff = crb0 * 131072u + sp * 32768u, cWoff = sp * 1048576u + wid * 16384u + nt * 256u, cPoff = (sp * 128u + crb0) * 16384u + nt * 4096u;
  const unsigned* pCnt = cnt + (xcd * 8u + (r >> 2)) * 32u;
  const unsigned esz = b < (unsigned)NEB ? DM * FF * 2 : 0, f0 = ((b & 7u) * 14u + (b >> 3)) * 16u;
  const __amdgpu_buffer_rsrc_t srdG = RSRC(wg, esz), srdU = RSRC(wu, esz), srdDn = RSRC(wd, esz);
  const unsigned ln2 = vL16 >> 4;
  const unsigned vGU = (ln2 >> 1) * (FF * 2u) + 16u * (ln2 & 1u), vDn = ((ln2 >> 1) & 15u) * 1024u + (ln2 >> 5) * 32u + 16u * (ln2 & 1u);
  const unsigned sGoff = 128u * wid * (FF * 2u) + f0 * 2u, sDnoff = f0 * 1024u + 256u * wid;
  asm volatile(
  "s_nop 7\n"
  "s_barrier\n"
  "s_add_u32 m0, %[sM0], 0\n"
  "s_add_u32 s60, %[sWoff], 0\n"
  "buffer_load_dwordx4 %[vWd], %[srdW], s60 offen lds\n"
  "s_add_u32 m0, %[sM0], 1024\n"
  "s_add_u32 s61, %[sWoff], 128\n"
  "buffer_load_dwordx4 %[vWd], %[srdW], s61 offen lds\n"
  "s_add_u32 m0, %[sM0], 2048\n"
  "s_add_u32 s60, %[sWoff], 8192\n"
  "buffer_load_dwordx4 %[vWd], %[srdW], s60 offen lds\n"
  "s_add_u32 m0, %[sM0], 3072\n"
  "s_add_u32 s61, %[sWoff], 8320\n"
  "buffer_load_dwordx4 %[vWd], %[srdW], s61 offen lds\n"
  "s_add_u32 m0, %[sM0], 16384\n"
  "s_add_u32 s60, %[sWoff], 65536\n"
  "buffer_load_dwordx4 %[vWd], %[srdW], s60 offen lds\n"
  "s_add_u32 m0, %[sM0], 17408\n"
  "s_add_u32 s61, %[sWoff], 65664\n"
  "buffer_load_dwordx4 %[vWd], %[srdW], s61 offen lds\n"
  "s_add_u32 m0, %[sM0], 18432\n"
  "s_add_u32 s60, %[sWoff], 73728\n"
  "buffer_load_dwordx4 %[vWd], %[srdW], s60 offen lds\n"
  "s_add_u32 m0, %[sM0], 19456\n"
  "s_add_u32 s61, %[sWoff], 73856\n"
  "buffer_load_dwordx4 %[vWd], %[srdW], s61 offen lds\n"
  "s_add_u32 m0, %[sM0], 32768\n"
  "s_add_u32 s60, %[sWoff], 131072\n"
  "buffer_load_dwordx4 %[vWd], %[srdW], s60 offen lds\n"
  "s_add_u32 m0, %[sM0], 33792\n"
  "s_add_u32 s61, %[sWoff], 131200\n"
  "buffer_load_dwordx4 %[vWd], %[srdW], s61 offen lds\n"
  "s_add_u32 m0, %[sM0], 34816\n"
  "s_add_u32 s60, %[sWoff], 139264\n"
  "buffer_load_dwordx4 %[vWd], %[srdW], s60 offen lds\n"
  "s_add_u32 m0, %[sM0], 35840\n"
  "s_add_u32 s61, %[sWoff], 139392\n"
  "buffer_load_dwordx4 %[vWd], %[srdW], s61 offen lds\n"
  "s_add_u32 m0, %[sM0], 49152\n"
  "s_add_u32 s60, %[sWoff], 196608\n"
  "buffer_load_dwordx4 %[vWd], %[srdW], s60 offen lds\n"
  "s_add_u32 m0, %[sM0], 50176\n"
  "s_add_u32 s61, %[sWoff], 196736\n"
  "buffer_load_dwordx4 %[vWd], %[srdW], s61 offen lds\n"
  "s_add_u32 m0, %[sM0], 51200\n"
  "s_add_u32 s60, %[sWoff], 204800\n"
  "buffer_load_dwordx4 %[vWd], %[srdW], s60 offen lds\n"
  "s_add_u32 m0, %[sM0], 52224\n"
  "s_add_u32 s61, %[sWoff], 204928\n"
  "buffer_load_dwordx4 %[vWd], %[srdW], s61 offen lds\n"
  "s_add_u32 m0, %[sM0], 65536\n"
  "s_add_u32 s60, %[sWoff], 262144\n"
  "buffer_load_dwordx4 %[vWd], %[srdW], s60 offen lds\n"
  "s_add_u32 m0, %[sM0], 66560\n"
  "s_add_u32 s61, %[sWoff], 262272\n"
  "buffer_load_dwordx4 %[vWd], %[srdW], s61 offen lds\n"
  "s_add_u32 m0, %[sM0], 67584\n"
  "s_add_u32 s60, %[sWoff], 270336\n"
  "buffer_load_dwordx4 %[vWd], %[srdW], s60 offen lds\n"
  "s_add_u32 m0, %[sM0], 68608\n"
  "s_add_u32 s61, %[sWoff], 270464\n"
  "buffer_load_dwordx4 %[vWd], %[srdW], s61 offen lds\n"
  "s_add_u32 m0, %[sM0], 81920\n"
  "s_add_u32 s60, %[sWoff], 327680\n"
  "buffer_load_dwordx4 %[vWd], %[srdW], s60 offen lds\n"
  "s_add_u32 m0, %[sM0], 82944\n"
  "s_add_u32 s61, %[sWoff], 327808\n"
  "buffer_load_dwordx4 %[vWd], %[srdW], s61 offen lds\n"
  "s_add_u32 m0, %[sM0], 83968\n"
  "s_add_u32 s60, %[sWoff], 335872\n"
  "buffer_load_dwordx4 %[vWd], %[srdW], s60 offen lds\n"
  "s_add_u32 m0, %[sM0], 84992\n"
  "s_add_u32 s61, %[sWoff], 336000\n"
  "buffer_load_dwordx4 %[vWd], %[srdW], s61 offen lds\n"
  "v_add_u32 v128, 0, %[vWb]\n"
  "v_xor_b32 v129, 32, v128\n"
  "v_add_u32 v130, 65536, %[vWb]\n"
  "v_xor_b32 v131, 32, v130\n"
  "v_add_u32 v132, 131072, %[vWb]\n"
  "v_xor_b32 v133, 32, v132\n"
  "s_cmp_lg_u32 %[sWid], 0\n"
  "s_cbranch_scc1 L_op_go%=\n"
  "v_mov_b32 v0, 0\n"
  "s_mov_b32 s62, 0\n"
  "L_op_poll%=:\n"
  "global_load_dword v1, v0, %[pCnt] sc1\n"
  "s_waitcnt vmcnt(0)\n"
  "v_readfirstlane_b32 s63, v1\n"
  "s_add_u32 s62, s62, 1\n"
  "s_cmp_ge_u32 s63, 16\n"
  "s_cbranch_scc1 L_op_go%=\n"
  "s_sleep 1\n"
  "s_cmp_lt_u32 s62, 0x100000\n"
  "s_cbranch_scc1 L_op_poll%=\n"
  "L_op_go%=:\n"
  "s_waitcnt vmcnt(20)\n"
  "s_barrier\n"
  "s_add_u32 s62, %[sOoff], 0\n"
  "buffer_load_dwordx4 a[0:3], %[vL16], %[srdO], s62 offen offset:0 sc1\n"
  "s_add_u32 s63, %[sOoff], 131072\n"
  "buffer_load_dwordx4 a[4:7], %[vL16], %[srdO], s63 offen offset:0 sc1\n"
  "buffer_load_dwordx4 a[8:11], %[vL16], %[srdO], s62 offen offset:1024 sc1\n"
  "buffer_load_dwordx4 a[12:15], %[vL16], %[srdO], s63 offen offset:1024 sc1\n"
  "buffer_load_dwordx4 a[16:19], %[vL16], %[srdO], s62 offen offset:2048 sc1\n"
  "buffer_load_dwordx4 a[20:23], %[vL16], %[srdO], s63 offen offset:2048 sc1\n"
  "buffer_load_dwordx4 a[24:27], %[vL16], %[srdO], s62 offen offset:3072 sc1\n"
  "buffer_load_dwordx4 a[28:31], %[vL16], %[srdO], s63 offen offset:3072 sc1\n"
  "s_add_u32 s62, %[sOoff], 4096\n"
  "buffer_load_dwordx4 a[32:35], %[vL16], %[srdO], s62 offen offset:0 sc1\n"
  "s_add_u32 s63, %[sOoff], 135168\n"
  "buffer_load_dwordx4 a[36:39], %[vL16], %[srdO], s63 offen offset:0 sc1\n"
  "buffer_load_dwordx4 a[40:43], %[vL16], %[srdO], s62 offen offset:1024 sc1\n"
  "buffer_load_dwordx4 a[44:47], %[vL16], %[srdO], s63 offen offset:1024 sc1\n"
  "ds_read_b64_tr_b16 v[64:65], v128 offset:0\n"
  "ds_read_b64_tr_b16 v[66:67], v128 offset:4096\n"
  "ds_read_b64_tr_b16 v[68:69], v129 offset:0\n"
  "ds_read_b64_tr_b16 v[70:71], v129 offset:4096\n"
  "ds_read_b64_tr_b16 v[72:73], v128 offset:512\n"
  "ds_read_b64_tr_b16 v[74:75], v128 offset:4608\n"
  "ds_read_b64_tr_b16 v[76:77], v129 offset:512\n"
  "ds_read_b64_tr_b16 v[78:79], v129 offset:4608\n"
  "ds_read_b64_tr_b16 v[80:81], v128 offset:1024\n"
  "ds_read_b64_tr_b16 v[82:83], v128 offset:5120\n"
  "ds_read_b64_tr_b16 v[84:85], v129 offset:1024\n"
  "ds_read_b64_tr_b16 v[86:87], v129 offset:5120\n"
  "ds_read_b64_tr_b16 v[88:89], v128 offset:1536\n"
  "ds_read_b64_tr_b16 v[90:91], v128 offset:5632\n"
  "ds_read_b64_tr_b16 v[92:93], v129 offset:1536\n"
  "ds_read_b64_tr_b16 v[94:95], v129 offset:5632\n"
  "s_waitcnt vmcnt(10)\n"
  "s_waitcnt lgkmcnt(14)\n"
  "v_mfma_f32_16x16x32_bf16 v[0:3], v[64:67], a[0:3], 0\n"
  "ds_read_b64_tr_b16 v[96:97], v128 offset:8192\n"
  "v_mfma_f32_16x16x32_bf16 v[32:35], v[64:67], a[4:7], 0\n"
  "s_waitcnt lgkmcnt(13)\n"
  "ds_read_b64_tr_b16 v[98:99], v128 offset:12288\n"
  "v_mfma_f32_16x16x32_bf16 v[4:7], v[68:71], a[0:3], 0\n"
  "ds_read_b64_tr_b16 v[100:101], v129 offset:8192\n"
  "buffer_load_dwordx4 a[48:51], %[vL16], %[srdO], s62 offen offset:2048 sc1\n"
  "v_mfma_f32_16x16x32_bf16 v[36:39], v[68:71], a[4:7], 0\n"
  "s_waitcnt lgkmcnt(13)\n"
  "ds_read_b64_tr_b16 v[102:103], v129 offset:12288\n"
  "v_mfma_f32_16x16x32_bf16 v[8:11], v[72:75], a[0:3], 0\n"
  "ds_read_b64_tr_b16 v[104:105], v128 offset:8704\n"
  "v_mfma_f32_16x16x32_bf16 v[40:43], v[72:75], a[4:7], 0\n"
  "s_waitcnt lgkmcnt(13)\n"
  "ds_read_b64_tr_b16 v[106:107], v128 offset:12800\n"
  "v_mfma_f32_16x16x32_bf16 v[12:15], v[76:79], a[0:3], 0\n"
  "ds_read_b64_tr_b16 v[108:109], v129 offset:8704\n"
  "buffer_load_dwordx4 a[52:55], %[vL16], %[srdO], s63 offen offset:2048 sc1\n"
  "v_mfma_f32_16x16x32_bf16 v[44:47], v[76:79], a[4:7], 0\n"
  "s_waitcnt lgkmcnt(13)\n"
  "ds_read_b64_tr_b16 v[110:111], v129 offset:12800\n"
  "v_mfma_f32_16x16x32_bf16 v[16:19], v[80:83], a[0:3], 0\n"
  "ds_read_b64_tr_b16 v[112:113], v128 offset:9216\n"
  "v_mfma_f32_16x16x32_bf16 v[48:51], v[80:83], a[4:7], 0\n"
  "s_waitcnt lgkmcnt(13)\n"
  "ds_read_b64_tr_b16 v[114:115], v128 offset:13312\n"
  "v_mfma_f32_16x16x32_bf16 v[20:23], v[84:87], a[0:3], 0\n"
  "ds_read_b64_tr_b16 v[116:117], v129 offset:9216\n"
  "buffer_load_dwordx4 a[56:59], %[vL16], %[srdO], s62 offen offset:3072 sc1\n"
  "v_mfma_f32_16x16x32_bf16 v[52:55], v[84:87], a[4:7], 0\n"
  "s_waitcnt lgkmcnt(13)\n"
  "ds_read_b64_tr_b16 v[118:119], v129 offset:13312\n"
  "v_mfma_f32_16x16x32_bf16 v[24:27], v[88:91], a[0:3], 0\n"
  "ds_read_b64_tr_b16 v[120:121], v128 offset:9728\n"
  "v_mfma_f32_16x16x32_bf16 v[56:59], v[88:91], a[4:7], 0\n"
  "s_waitcnt lgkmcnt(13)\n"
  "ds_read_b64_tr_b16 v[122:123], v128 offset:13824\n"
  "v_mfma_f32_16x16x32_bf16 v[28:31], v[92:95], a[0:3], 0\n"
  "ds_read_b64_tr_b16 v[124:125], v129 offset:9728\n"
  "buffer_load_dwordx4 a[60:63], %[vL16], %[srdO], s63 offen offset:3072 sc1\n"
  "v_mfma_f32_16x16x32_bf16 v[60:63], v[92:95], a[4:7], 0\n"
  "s_waitcnt lgkmcnt(13)\n"
  "s_waitcnt vmcnt(12)\n"
  "ds_read_b64_tr_b16 v[126:127], v129 offset:13824\n"
  "s_waitcnt vmcnt(32)\n"
  "s_barrier\n"
  "v_mfma_f32_16x16x32_bf16 v[0:3], v[96:99], a[8:11], v[0:3]\n"
  "s_add_u32 m0, %[sM0], 98304\n"
  "ds_read_b64_tr_b16 v[64:65], v128 offset:16384\n"
  "v_mfma_f32_16x16x32_bf16 v[32:35], v[96:99], a[12:15], v[32:35]\n"
  "s_waitcnt lgkmcnt(13)\n"
  "ds_read_b64_tr_b16 v[66:67], v128 offset:20480\n"
  "v_mfma_f32_16x16x32_bf16 v[4:7], v[100:103], a[8:11], v[4:7]\n"
  "s_add_u32 s60, %[sWoff], 393216\n"
  "ds_read_b64_tr_b16 v[68:69], v129 offset:16384\n"
  "buffer_load_dwordx4 %[vWd], %[srdW], s60 offen lds\n"
  "v_mfma_f32_16x16x32_bf16 v[36:39], v[100:103], a[12:15], v[36:39]\n"
  "s_waitcnt lgkmcnt(13)\n"
  "ds_read_b64_tr_b16 v[70:71], v129 offset:20480\n"
  "v_mfma_f32_16x16x32_bf16 v[8:11], v[104:107], a[8:11], v[8:11]\n"
  "s_add_u32 m0, %[sM0], 99328\n"
  "ds_read_b64_tr_b16 v[72:73], v128 offset:16896\n"
  "v_mfma_f32_16x16x32_bf16 v[40:43], v[104:107], a[12:15], v[40:43]\n"
  "s_waitcnt lgkmcnt(13)\n"
  "ds_read_b64_tr_b16 v[74:75], v128 offset:20992\n"
  "v_mfma_f32_16x16x32_bf16 v[12:15], v[108:111], a[8:11], v[12:15]\n"
  "s_add_u32 s61, %[sWoff], 393344\n"
  "ds_read_b64_tr_b16 v[76:77], v129 offset:16896\n"
  "buffer_load_dwordx4 %[vWd], %[srdW], s61 offen lds\n"
  "v_mfma_f32_16x16x32_bf16 v[44:47], v[108:111], a[12:15], v[44:47]\n"
  "s_waitcnt lgkmcnt(13)\n"
  "ds_read_b64_tr_b16 v[78:79], v129 offset:20992\n"
  "v_mfma_f32_16x16x32_bf16 v[16:19], v[112:115], a[8:11], v[16:19]\n"
  "s_add_u32 m0, %[sM0], 100352\n"
  "ds_read_b64_tr_b16 v[80:81], v128 offset:17408\n"
  "v_mfma_f32_16x16x32_bf16 v[48:51], v[112:115], a[12:15], v[48:51]\n"
  "s_waitcnt lgkmcnt(13)\n"
  "ds_read_b64_tr_b16 v[82:83], v128 offset:21504\n"
  "v_mfma_f32_16x16x32_bf16 v[20:23], v[116:119], a[8:11], v[20:23]\n"
  "s_add_u32 s60, %[sWoff], 401408\n"
  "ds_read_b64_tr_b16 v[84:85], v129 offset:17408\n"
  "buffer_load_dwordx4 %[vWd], %[srdW], s60 offen lds\n"
  "v_mfma_f32_16x16x32_bf16 v[52:55], v[116:119], a[12:15], v[52:55]\n"
  "s_waitcnt lgkmcnt(13)\n"
  "ds_read_b64_tr_b16 v[86:87], v129 offset:21504\n"
  "v_mfma_f32_16x16x32_bf16 v[24:27], v[120:123], a[8:11], v[24:27]\n"
  "s_add_u32 m0, %[sM0], 101376\n"
  "ds_read_b64_tr_b16 v[88:89], v128 offset:17920\n"
  "v_mfma_f32_16x16x32_bf16 v[56:59], v[120:123], a[12:15], v[56:59]\n"
  "s_waitcnt lgkmcnt(13)\n"
  "ds_read_b64_tr_b16 v[90:91], v128 offset:22016\n"
  "v_mfma_f32_16x16x32_bf16 v[28:31], v[124:127], a[8:11], v[28:31]\n"
  "s_add_u32 s61, %[sWoff], 401536\n"
  "ds_read_b64_tr_b16 v[92:93], v129 offset:17920\n"
  "buffer_load_dwordx4 %[vWd], %[srdW], s61 offen lds\n"
  "v_mfma_f32_16x16x32_bf16 v[60:63], v[124:127], a[12:15], v[60:63]\n"
  "s_waitcnt lgkmcnt(13)\n"
  "s_waitcnt vmcnt(14)\n"
  "ds_read_b64_tr_b16 v[94:95], v129 offset:22016\n"
  "v_mfma_f32_16x16x32_bf16 v[0:3], v[64:67], a[16:19], v[0:3]\n"
  "ds_read_b64_tr_b16 v[96:97], v128 offset:24576\n"
  "v_mfma_f32_16x16x32_bf16 v[32:35], v[64:67], a[20:23], v[32:35]\n"
  "s_waitcnt lgkmcnt(13)\n"
  "ds_read_b64_tr_b16 v[98:99], v128 offset:28672\n"
  "v_mfma_f32_16x16x32_bf16 v[4:7], v[68:71], a[16:19], v[4:7]\n"
  "s_add_u32 s62, %[sOoff], 8192\n"
  "ds_read_b64_tr_b16 v[100:101], v129 offset:24576\n"
  "buffer_load_dwordx4 a[64:67], %[vL16], %[srdO], s62 offen offset:0 sc1\n"
  "v_mfma_f32_16x16x32_bf16 v[36:39], v[68:71], a[20:23], v[36:39]\n"
  "s_waitcnt lgkmcnt(13)\n"
  "ds_read_b64_tr_b16 v[102:103], v129 offset:28672\n"
  "v_mfma_f32_16x16x32_bf16 v[8:11], v[72:75], a[16:19], v[8:11]\n"
  "ds_read_b64_tr_b16 v[104:105], v128 offset:25088\n"
  "v_mfma_f32_16x16x32_bf16 v[40:43], v[72:75], a[20:23], v[40:43]\n"
  "s_waitcnt lgkmcnt(13)\n"
  "ds_read_b64_tr_b16 v[106:107], v128 offset:29184\n"
  "v_mfma_f32_16x16x32_bf16 v[12:15], v[76:79], a[16:19], v[12:15]\n"
  "s_add_u32 s63, %[sOoff], 139264\n"
  "ds_read_b64_tr_b16 v[108:109], v129 offset:25088\n"
  "buffer_load_dwordx4 a[68:71], %[vL16], %[srdO], s63 offen offset:0 sc1\n"
  "v_mfma_f32_16x16x32_bf16 v[44:47], v[76:79], a[20:23], v[44:47]\n"
  "s_waitcnt lgkmcnt(13)\n"
  "ds_read_b64_tr_b16 v[110:111], v129 offset:29184\n"
  "v_mfma_f32_16x16x32_bf16 v[16:19], v[80:83], a[16:19], v[16:19]\n"
  "ds_read_b64_tr_b16 v[112:113], v128 offset:25600\n"
  "v_mfma_f32_16x16x32_bf16 v[48:51], v[80:83], a[20:23], v[48:51]\n"
  "s_waitcnt lgkmcnt(13)\n"
  "ds_read_b64_tr_b16 v[114:115], v128 offset:29696\n"
  "v_mfma_f32_16x16x32_bf16 v[20:23], v[84:87], a[16:19], v[20:23]\n"
  "ds_read_b64_tr_b16 v[116:117], v129 offset:25600\n"
  "buffer_load_dwordx4 a[72:75], %[vL16], %[srdO], s62 offen offset:1024 sc1\n"
  "v_mfma_f32_16x16x32_bf16 v[52:55], v[84:87], a[20:23], v[52:55]\n"
  "s_waitcnt lgkmcnt(13)\n"
  "ds_read_b64_tr_b16 v[118:119], v129 offset:29696\n"
  "v_mfma_f32_16x16x32_bf16 v[24:27], v[88:91], a[16:19], v[24:27]\n"
  "ds_read_b64_tr_b16 v[120:121], v128 offset:26112\n"
  "v_mfma_f32_16x16x32_bf16 v[56:59], v[88:91], a[20:23], v[56:59]\n"
  "s_waitcnt lgkmcnt(13)\n"
  "ds_read_b64_tr_b16 v[122:123], v128 offset:30208\n"
  "v_mfma_f32_16x16x32_bf16 v[28:31], v[92:95], a[16:19], v[28:31]\n"
  "ds_read_b64_tr_b16 v[124:125], v129 offset:26112\n"
  "buffer_load_dwordx4 a[76:79], %[vL16], %[srdO], s63 offen offset:1024 sc1\n"
  "v_mfma_f32_16x16x32_bf16 v[60:63], v[92:95], a[20:23], v[60:63]\n"
  "s_waitcnt lgkmcnt(13)\n"
  "s_waitcnt vmcnt(16)\n"
  "ds_read_b64_tr_b16 v[126:127], v129 offset:30208\n"
  "s_waitcnt vmcnt(36)\n"
  "s_barrier\n"
  "v_mfma_f32_16x16x32_bf16 v[0:3], v[96:99], a[24:27], v[0:3]\n"
  "s_add_u32 m0, %[sM0], 114688\n"
  "ds_read_b64_tr_b16 v[64:65], v128 offset:32768\n"
  "v_mfma_f32_16x16x32_bf16 v[32:35], v[96:99], a[28:31], v[32:35]\n"
  "s_waitcnt lgkmcnt(13)\n"
  "ds_read_b64_tr_b16 v[66:67], v128 offset:36864\n"
  "v_mfma_f32_16x16x32_bf16 v[4:7], v[100:103], a[24:27], v[4:7]\n"
  "s_add_u32 s60, %[sWoff], 458752\n"
  "ds_read_b64_tr_b16 v[68:69], v129 offset:32768\n"
  "buffer_load_dwordx4 %[vWd], %[srdW], s60 offen lds\n"
  "v_mfma_f32_16x16x32_bf16 v[36:39], v[100:103], a[28:31], v[36:39]\n"
  "s_waitcnt lgkmcnt(13)\n"
  "ds_read_b64_tr_b16 v[70:71], v129 offset:36864\n"
  "v_mfma_f32_16x16x32_bf16 v[8:11], v[104:107], a[24:27], v[8:11]\n"
  "s_add_u32 m0, %[sM0], 115712\n"
  "ds_read_b64_tr_b16 v[72:73], v128 offset:33280\n"
  "v_mfma_f32_16x16x32_bf16 v[40:43], v[104:107], a[28:31], v[40:43]\n"
  "s_waitcnt lgkmcnt(13)\n"
  "ds_read_b64_tr_b16 v[74:75], v128 offset:37376\n"
  "v_mfma_f32_16x16x32_bf16 v[12:15], v[108:111], a[24:27], v[12:15]\n"
  "s_add_u32 s61, %[sWoff], 458880\n"
  "ds_read_b64_tr_b16 v[76:77], v129 offset:33280\n"
  "buffer_load_dwordx4 %[vWd], %[srdW], s61 offen lds\n"
  "v_mfma_f32_16x16x32_bf16 v[44:47], v[108:111], a[28:31], v[44:47]\n"
  "s_waitcnt lgkmcnt(13)\n"
  "ds_read_b64_tr_b16 v[78:79], v129 offset:37376\n"
  "v_mfma_f32_16x16x32_bf16 v[16:19], v[112:115], a[24:27], v[16:19]\n"
  "s_add_u32 m0, %[sM0], 116736\n"
  "ds_read_b64_tr_b16 v[80:81], v128 offset:33792\n"
  "v_mfma_f32_16x16x32_bf16 v[48:51], v[112:115], a[28:31], v[48:51]\n"
  "s_waitcnt lgkmcnt(13)\n"
  "ds_read_b64_tr_b16 v[82:83], v128 offset:37888\n"
  "v_mfma_f32_16x16x32_bf16 v[20:23], v[116:119], a[24:27], v[20:23]\n"
  "s_add_u32 s60, %[sWoff], 466944\n"
  "ds_read_b64_tr_b16 v[84:85], v129 offset:33792\n"
  "buffer_load_dwordx4 %[vWd], %[srdW], s60 offen lds\n"
  "v_mfma_f32_16x16x32_bf16 v[52:55], v[116:119], a[28:31], v[52:55]\n"
  "s_waitcnt lgkmcnt(13)\n"
  "ds_read_b64_tr_b16 v[86:87], v129 offset:37888\n"
  "v_mfma_f32_16x16x32_bf16 v[24:27], v[120:123], a[24:27], v[24:27]\n"
  "s_add_u32 m0, %[sM0], 117760\n"
  "ds_read_b64_tr_b16 v[88:89], v128 offset:34304\n"
  "v_mfma_f32_16x16x32_bf16 v[56:59], v[120:123], a[28:31], v[56:59]\n"
  "s_waitcnt lgkmcnt(13)\n"
  "ds_read_b64_tr_b16 v[90:91], v128 offset:38400\n"
  "v_mfma_f32_16x16x32_bf16 v[28:31], v[124:127], a[24:27], v[28:31]\n"
  "s_add_u32 s61, %[sWoff], 467072\n"
  "ds_read_b64_tr_b16 v[92:93], v129 offset:34304\n"
  "buffer_load_dwordx4 %[vWd], %[srdW], s61 offen lds\n"
  "v_mfma_f32_16x16x32_bf16 v[60:63], v[124:127], a[28:31], v[60:63]\n"
  "s_waitcnt lgkmcnt(13)\n"
  "s_waitcnt vmcnt(18)\n"
  "ds_read_b64_tr_b16 v[94:95], v129 offset:38400\n"
  "v_mfma_f32_16x16x32_bf16 v[0:3], v[64:67], a[32:35], v[0:3]\n"
  "ds_read_b64_tr_b16 v[96:97], v128 offset:40960\n"
  "v_mfma_f32_16x16x32_bf16 v[32:35], v[64:67], a[36:39], v[32:35]\n"
  "s_waitcnt lgkmcnt(13)\n"
  "ds_read_b64_tr_b16 v[98:99], v128 offset:45056\n"
  "v_mfma_f32_16x16x32_bf16 v[4:7], v[68:71], a[32:35], v[4:7]\n"
  "ds_read_b64_tr_b16 v[100:101], v129 offset:40960\n"
  "buffer_load_dwordx4 a[80:83], %[vL16], %[srdO], s62 offen offset:2048 sc1\n"
  "v_mfma_f32_16x16x32_bf16 v[36:39], v[68:71], a[36:39], v[36:39]\n"
  "s_waitcnt lgkmcnt(13)\n"
  "ds_read_b64_tr_b16 v[102:103], v129 offset:45056\n"
  "v_mfma_f32_16x16x32_bf16 v[8:11], v[72:75], a[32:35], v[8:11]\n"
  "ds_read_b64_tr_b16 v[104:105], v128 offset:41472\n"
  "v_mfma_f32_16x16x32_bf16 v[40:43], v[72:75], a[36:39], v[40:43]\n"
  "s_waitcnt lgkmcnt(13)\n"
  "ds_read_b64_tr_b16 v[106:107], v128 offset:45568\n"
  "v_mfma_f32_16x16x32_bf16 v[12:15], v[76:79], a[32:35], v[12:15]\n"
  "ds_read_b64_tr_b16 v[108:109], v129 offset:41472\n"
  "buffer_load_dwordx4 a[84:87], %[vL16], %[srdO], s63 offen offset:2048 sc1\n"
  "v_mfma_f32_16x16x32_bf16 v[44:47], v[76:79], a[36:39], v[44:47]\n"
  "s_waitcnt lgkmcnt(13)\n"
  "ds_read_b64_tr_b16 v[110:111], v129 offset:45568\n"
  "v_mfma_f32_16x16x32_bf16 v[16:19], v[80:83], a[32:35], v[16:19]\n"
  "ds_read_b64_tr_b16 v[112:113], v128 offset:41984\n"
  "v_mfma_f32_16x16x32_bf16 v[48:51], v[80:83], a[36:39], v[48:51]\n"
  "s_waitcnt lgkmcnt(13)\n"
  "ds_read_b64_tr_b16 v[114:115], v128 offset:46080\n"
  "v_mfma_f32_16x16x32_bf16 v[20:23], v[84:87], a[32:35], v[20:23]\n"
  "ds_read_b64_tr_b16 v[116:117], v129 offset:41984\n"
  "buffer_load_dwordx4 a[88:91], %[vL16], %[srdO], s62 offen offset:3072 sc1\n"
  "v_mfma_f32_16x16x32_bf16 v[52:55], v[84:87], a[36:39], v[52:55]\n"
  "s_waitcnt lgkmcnt(13)\n"
  "ds_read_b64_tr_b16 v[118:119], v129 offset:46080\n"
  "v_mfma_f32_16x16x32_bf16 v[24:27], v[88:91], a[32:35], v[24:27]\n"
  "ds_read_b64_tr_b16 v[120:121], v128 offset:42496\n"
  "v_mfma_f32_16x16x32_bf16 v[56:59], v[88:91], a[36:39], v[56:59]\n"
  "s_waitcnt lgkmcnt(13)\n"
  "ds_read_b64_tr_b16 v[122:123], v128 offset:46592\n"
  "v_mfma_f32_16x16x32_bf16 v[28:31], v[92:95], a[32:35], v[28:31]\n"
  "ds_read_b64_tr_b16 v[124:125], v129 offset:42496\n"
  "buffer_load_dwordx4 a[92:95], %[vL16], %[srdO], s63 offen offset:3072 sc1\n"
  "v_mfma_f32_16x16x32_bf16 v[60:63], v[92:95], a[36:39], v[60:63]\n"
  "s_waitcnt lgkmcnt(13)\n"
  "s_waitcnt vmcnt(20)\n"
  "ds_read_b64_tr_b16 v[126:127], v129 offset:46592\n"
  "s_waitcnt vmcnt(40)\n"
  "s_barrier\n"
  "v_mfma_f32_16x16x32_bf16 v[0:3], v[96:99], a[40:43], v[0:3]\n"
  "s_add_u32 m0, %[sM0], 131072\n"
  "ds_read_b64_tr_b16 v[64:65], v128 offset:49152\n"
  "v_mfma_f32_16x16x32_bf16 v[32:35], v[96:99], a[44:47], v[32:35]\n"
  "s_waitcnt lgkmcnt(13)\n"
  "ds_read_b64_tr_b16 v[66:67], v128 offset:53248\n"
  "v_mfma_f32_16x16x32_bf16 v[4:7], v[100:103], a[40:43], v[4:7]\n"
  "s_add_u32 s60, %[sWoff], 524288\n"
  "ds_read_b64_tr_b16 v[68:69], v129 offset:49152\n"
  "buffer_load_dwordx4 %[vWd], %[srdW], s60 offen lds\n"
  "v_mfma_f32_16x16x32_bf16 v[36:39], v[100:103], a[44:47], v[36:39]\n"
  "s_waitcnt lgkmcnt(13)\n"
  "ds_read_b64_tr_b16 v[70:71], v129 offset:53248\n"
  "v_mfma_f32_16x16x32_bf16 v[8:11], v[104:107], a[40:43], v[8:11]\n"
  "s_add_u32 m0, %[sM0], 132096\n"
  "ds_read_b64_tr_b16 v[72:73], v128 offset:49664\n"
  "v_mfma_f32_16x16x32_bf16 v[40:43], v[104:107], a[44:47], v[40:43]\n"
  "s_waitcnt lgkmcnt(13)\n"
  "ds_read_b64_tr_b16 v[74:75], v128 offset:53760\n"
  "v_mfma_f32_16x16x32_bf16 v[12:15], v[108:111], a[40:43], v[12:15]\n"
  "s_add_u32 s61, %[sWoff], 524416\n"
  "ds_read_b64_tr_b16 v[76:77], v129 offset:49664\n"
  "buffer_load_dwordx4 %[vWd], %[srdW], s61 offen lds\n"
  "v_mfma_f32_16x16x32_bf16 v[44:47], v[108:111], a[44:47], v[44:47]\n"
  "s_waitcnt lgkmcnt(13)\n"
  "ds_read_b64_tr_b16 v[78:79], v129 offset:53760\n"
  "v_mfma_f32_16x16x32_bf16 v[16:19], v[112:115], a[40:43], v[16:19]\n"
  "s_add_u32 m0, %[sM0], 133120\n"
  "ds_read_b64_tr_b16 v[80:81], v128 offset:50176\n"
  "v_mfma_f32_16x16x32_bf16 v[48:51], v[112:115], a[44:47], v[48:51]\n"
  "s_waitcnt lgkmcnt(13)\n"
  "ds_read_b64_tr_b16 v[82:83], v128 offset:54272\n"
  "v_mfma_f32_16x16x32_bf16 v[20:23], v[116:119], a[40:43], v[20:23]\n"
  "s_add_u32 s60, %[sWoff], 532480\n"
  "ds_read_b64_tr_b16 v[84:85], v129 offset:50176\n"
  "buffer_load_dwordx4 %[vWd], %[srdW], s60 offen lds\n"
  "v_mfma_f32_16x16x32_bf16 v[52:55], v[116:119], a[44:47], v[52:55]\n"
  "s_waitcnt lgkmcnt(13)\n"
  "ds_read_b64_tr_b16 v[86:87], v129 offset:54272\n"
  "v_mfma_f32_16x16x32_bf16 v[24:27], v[120:123], a[40:43], v[24:27]\n"
  "s_add_u32 m0, %[sM0], 134144\n"
  "ds_read_b64_tr_b16 v[88:89], v128 offset:50688\n"
  "v_mfma_f32_16x16x32_bf16 v[56:59], v[120:123], a[44:47], v[56:59]\n"
  "s_waitcnt lgkmcnt(13)\n"
  "ds_read_b64_tr_b16 v[90:91], v128 offset:54784\n"
  "v_mfma_f32_16x16x32_bf16 v[28:31], v[124:127], a[40:43], v[28:31]\n"
  "s_add_u32 s61, %[sWoff], 532608\n"
  "ds_read_b64_tr_b16 v[92:93], v129 offset:50688\n"
  "buffer_load_dwordx4 %[vWd], %[srdW], s61 offen lds\n"
  "v_mfma_f32_16x16x32_bf16 v[60:63], v[124:127], a[44:47], v[60:63]\n"
  "s_waitcnt lgkmcnt(13)\n"
  "s_waitcnt vmcnt(22)\n"
  "ds_read_b64_tr_b16 v[94:95], v129 offset:54784\n"
  "v_mfma_f32_16x16x32_bf16 v[0:3], v[64:67], a[48:51], v[0:3]\n"
  "ds_read_b64_tr_b16 v[96:97], v128 offset:57344\n"
  "v_mfma_f32_16x16x32_bf16 v[32:35], v[64:67], a[52:55], v[32:35]\n"
  "s_waitcnt lgkmcnt(13)\n"
  "ds_read_b64_tr_b16 v[98:99], v128 offset:61440\n"
  "v_mfma_f32_16x16x32_bf16 v[4:7], v[68:71], a[48:51], v[4:7]\n"
  "s_add_u32 s62, %[sOoff], 12288\n"
  "ds_read_b64_tr_b16 v[100:101], v129 offset:57344\n"
  "buffer_load_dwordx4 a[96:99], %[vL16], %[srdO], s62 offen offset:0 sc1\n"
  "v_mfma_f32_16x16x32_bf16 v[36:39], v[68:71], a[52:55], v[36:39]\n"
  "s_waitcnt lgkmcnt(13)\n"
  "ds_read_b64_tr_b16 v[102:103], v129 offset:61440\n"
  "v_mfma_f32_16x16x32_bf16 v[8:11], v[72:75], a[48:51], v[8:11]\n"
  "ds_read_b64_tr_b16 v[104:105], v128 offset:57856\n"
  "v_mfma_f32_16x16x32_bf16 v[40:43], v[72:75], a[52:55], v[40:43]\n"
  "s_waitcnt lgkmcnt(13)\n"
  "ds_read_b64_tr_b16 v[106:107], v128 offset:61952\n"
  "v_mfma_f32_16x16x32_bf16 v[12:15], v[76:79], a[48:51], v[12:15]\n"
  "s_add_u32 s63, %[sOoff], 143360\n"
  "ds_read_b64_tr_b16 v[108:109], v129 offset:57856\n"
  "buffer_load_dwordx4 a[100:103], %[vL16], %[srdO], s63 offen offset:0 sc1\n"
  "v_mfma_f32_16x16x32_bf16 v[44:47], v[76:79], a[52:55], v[44:47]\n"
  "s_waitcnt lgkmcnt(13)\n"
  "ds_read_b64_tr_b16 v[110:111], v129 offset:61952\n"
  "v_mfma_f32_16x16x32_bf16 v[16:19], v[80:83], a[48:51], v[16:19]\n"
  "ds_read_b64_tr_b16 v[112:113], v128 offset:58368\n"
  "v_mfma_f32_16x16x32_bf16 v[48:51], v[80:83], a[52:55], v[48:51]\n"
  "s_waitcnt lgkmcnt(13)\n"
  "ds_read_b64_tr_b16 v[114:115], v128 offset:62464\n"
  "v_mfma_f32_16x16x32_bf16 v[20:23], v[84:87], a[48:51], v[20:23]\n"
  "ds_read_b64_tr_b16 v[116:117], v129 offset:58368\n"
  "buffer_load_dwordx4 a[104:107], %[vL16], %[srdO], s62 offen offset:1024 sc1\n"
  "v_mfma_f32_16x16x32_bf16 v[52:55], v[84:87], a[52:55], v[52:55]\n"
  "s_waitcnt lgkmcnt(13)\n"
  "ds_read_b64_tr_b16 v[118:119], v129 offset:62464\n"
  "v_mfma_f32_16x16x32_bf16 v[24:27], v[88:91], a[48:51], v[24:27]\n"
  "ds_read_b64_tr_b16 v[120:121], v128 offset:58880\n"
  "v_mfma_f32_16x16x32_bf16 v[56:59], v[88:91], a[52:55], v[56:59]\n"
  "s_waitcnt lgkmcnt(13)\n"
  "ds_read_b64_tr_b16 v[122:123], v128 offset:62976\n"
  "v_mfma_f32_16x16x32_bf16 v[28:31], v[92:95], a[48:51], v[28:31]\n"
  "ds_read_b64_tr_b16 v[124:125], v129 offset:58880\n"
  "buffer_load_dwordx4 a[108:111], %[vL16], %[srdO], s63 offen offset:1024 sc1\n"
  "v_mfma_f32_16x16x32_bf16 v[60:63], v[92:95], a[52:55], v[60:63]\n"
  "s_waitcnt lgkmcnt(13)\n"
  "s_waitcnt vmcnt(24)\n"
  "ds_read_b64_tr_b16 v[126:127], v129 offset:62976\n"
  "s_waitcnt vmcnt(44)\n"
  "s_barrier\n"
  "v_mfma_f32_16x16x32_bf16 v[0:3], v[96:99], a[56:59], v[0:3]\n"
  "s_add_u32 m0, %[sM0], 147456\n"
  "ds_read_b64_tr_b16 v[64:65], v130 offset:0\n"
  "v_mfma_f32_16x16x32_bf16 v[32:35], v[96:99], a[60:63], v[32:35]\n"
  "s_waitcnt lgkmcnt(13)\n"
  "ds_read_b64_tr_b16 v[66:67], v130 offset:4096\n"
  "v_mfma_f32_16x16x32_bf16 v[4:7], v[100:103], a[56:59], v[4:7]\n"
  "s_add_u32 s60, %[sWoff], 589824\n"
  "ds_read_b64_tr_b16 v[68:69], v131 offset:0\n"
  "buffer_load_dwordx4 %[vWd], %[srdW], s60 offen lds\n"
  "v_mfma_f32_16x16x32_bf16 v[36:39], v[100:103], a[60:63], v[36:39]\n"
  "s_waitcnt lgkmcnt(13)\n"
  "ds_read_b64_tr_b16 v[70:71], v131 offset:4096\n"
  "v_mfma_f32_16x16x32_bf16 v[8:11], v[104:107], a[56:59], v[8:11]\n"
  "s_add_u32 m0, %[sM0], 148480\n"
  "ds_read_b64_tr_b16 v[72:73], v130 offset:512\n"
  "v_mfma_f32_16x16x32_bf16 v[40:43], v[104:107], a[60:63], v[40:43]\n"
  "s_waitcnt lgkmcnt(13)\n"
  "ds_read_b64_tr_b16 v[74:75], v130 offset:4608\n"
  "v_mfma_f32_16x16x32_bf16 v[12:15], v[108:111], a[56:59], v[12:15]\n"
  "s_add_u32 s61, %[sWoff], 589952\n"
  "ds_read_b64_tr_b16 v[76:77], v131 offset:512\n"
  "buffer_load_dwordx4 %[vWd], %[srdW], s61 offen lds\n"
  "v_mfma_f32_16x16x32_bf16 v[44:47], v[108:111], a[60:63], v[44:47]\n"
  "s_waitcnt lgkmcnt(13)\n"
  "ds_read_b64_tr_b16 v[78:79], v131 offset:4608\n"
  "v_mfma_f32_16x16x32_bf16 v[16:19], v[112:115], a[56:59], v[16:19]\n"
  "s_add_u32 m0, %[sM0], 149504\n"
  "ds_read_b64_tr_b16 v[80:81], v130 offset:1024\n"
  "v_mfma_f32_16x16x32_bf16 v[48:51], v[112:115], a[60:63], v[48:51]\n"
  "s_waitcnt lgkmcnt(13)\n"
  "ds_read_b64_tr_b16 v[82:83], v130 offset:5120\n"
  "v_mfma_f32_16x16x32_bf16 v[20:23], v[116:119], a[56:59], v[20:23]\n"
  "s_add_u32 s60, %[sWoff], 598016\n"
  "ds_read_b64_tr_b16 v[84:85], v131 offset:1024\n"
  "buffer_load_dwordx4 %[vWd], %[srdW], s60 offen lds\n"
  "v_mfma_f32_16x16x32_bf16 v[52:55], v[116:119], a[60:63], v[52:55]\n"
  "s_waitcnt lgkmcnt(13)\n"
  "ds_read_b64_tr_b16 v[86:87], v131 offset:5120\n"
  "v_mfma_f32_16x16x32_bf16 v[24:27], v[120:123], a[56:59], v[24:27]\n"
  "s_add_u32 m0, %[sM0], 150528\n"
  "ds_read_b64_tr_b16 v[88:89], v130 offset:1536\n"
  "v_mfma_f32_16x16x32_bf16 v[56:59], v[120:123], a[60:63], v[56:59]\n"
  "s_waitcnt lgkmcnt(13)\n"
  "ds_read_b64_tr_b16 v[90:91], v130 offset:5632\n"
  "v_mfma_f32_16x16x32_bf16 v[28:31], v[124:127], a[56:59], v[28:31]\n"
  "s_add_u32 s61, %[sWoff], 598144\n"
  "ds_read_b64_tr_b16 v[92:93], v131 offset:1536\n"
  "buffer_load_dwordx4 %[vWd], %[srdW], s61 offen lds\n"
  "v_mfma_f32_16x16x32_bf16 v[60:63], v[124:127], a[60:63], v[60:63]\n"
  "s_waitcnt lgkmcnt(13)\n"
  "s_waitcnt vmcnt(22)\n"
  "ds_read_b64_tr_b16 v[94:95], v131 offset:5632\n"
  "v_mfma_f32_16x16x32_bf16 v[0:3], v[64:67], a[64:67], v[0:3]\n"
  "ds_read_b64_tr_b16 v[96:97], v130 offset:8192\n"
  "v_mfma_f32_16x16x32_bf16 v[32:35], v[64:67], a[68:71], v[32:35]\n"
  "s_waitcnt lgkmcnt(13)\n"
  "ds_read_b64_tr_b16 v[98:99], v130 offset:12288\n"
  "v_mfma_f32_16x16x32_bf16 v[4:7], v[68:71], a[64:67], v[4:7]\n"
  "ds_read_b64_tr_b16 v[100:101], v131 offset:8192\n"
  "buffer_load_dwordx4 a[112:115], %[vL16], %[srdO], s62 offen offset:2048 sc1\n"
  "v_mfma_f32_16x16x32_bf16 v[36:39], v[68:71], a[68:71], v[36:39]\n"
  "s_waitcnt lgkmcnt(13)\n"
  "ds_read_b64_tr_b16 v[102:103], v131 offset:12288\n"
  "v_mfma_f32_16x16x32_bf16 v[8:11], v[72:75], a[64:67], v[8:11]\n"
  "ds_read_b64_tr_b16 v[104:105], v130 offset:8704\n"
  "v_mfma_f32_16x16x32_bf16 v[40:43], v[72:75], a[68:71], v[40:43]\n"
  "s_waitcnt lgkmcnt(13)\n"
  "ds_read_b64_tr_b16 v[106:107], v130 offset:12800\n"
  "v_mfma_f32_16x16x32_bf16 v[12:15], v[76:79], a[64:67], v[12:15]\n"
  "ds_read_b64_tr_b16 v[108:109], v131 offset:8704\n"
  "buffer_load_dwordx4 a[116:119], %[vL16], %[srdO], s63 offen offset:2048 sc1\n"
  "v_mfma_f32_16x16x32_bf16 v[44:47], v[76:79], a[68:71], v[44:47]\n"
  "s_waitcnt lgkmcnt(13)\n"
  "ds_read_b64_tr_b16 v[110:111], v131 offset:12800\n"
  "v_mfma_f32_16x16x32_bf16 v[16:19], v[80:83], a[64:67], v[16:19]\n"
  "ds_read_b64_tr_b16 v[112:113], v130 offset:9216\n"
  "v_mfma_f32_16x16x32_bf16 v[48:51], v[80:83], a[68:71], v[48:51]\n"
  "s_waitcnt lgkmcnt(13)\n"
  "ds_read_b64_tr_b16 v[114:115], v130 offset:13312\n"
  "v_mfma_f32_16x16x32_bf16 v[20:23], v[84:87], a[64:67], v[20:23]\n"
  "ds_read_b64_tr_b16 v[116:117], v131 offset:9216\n"
  "buffer_load_dwordx4 a[120:123], %[vL16], %[srdO], s62 offen offset:3072 sc1\n"
  "v_mfma_f32_16x16x32_bf16 v[52:55], v[84:87], a[68:71], v[52:55]\n"
  "s_waitcnt lgkmcnt(13)\n"
  "ds_read_b64_tr_b16 v[118:119], v131 offset:13312\n"
  "v_mfma_f32_16x16x32_bf16 v[24:27], v[88:91], a[64:67], v[24:27]\n"
  "ds_read_b64_tr_b16 v[120:121], v130 offset:9728\n"
  "v_mfma_f32_16x16x32_bf16 v[56:59], v[88:91], a[68:71], v[56:59]\n"
  "s_waitcnt lgkmcnt(13)\n"
  "ds_read_b64_tr_b16 v[122:123], v130 offset:13824\n"
  "v_mfma_f32_16x16x32_bf16 v[28:31], v[92:95], a[64:67], v[28:31]\n"
  "ds_read_b64_tr_b16 v[124:125], v131 offset:9728\n"
  "buffer_load_dwordx4 a[124:127], %[vL16], %[srdO], s63 offen offset:3072 sc1\n"
  "v_mfma_f32_16x16x32_bf16 v[60:63], v[92:95], a[68:71], v[60:63]\n"
  "s_waitcnt lgkmcnt(13)\n"
  "s_waitcnt vmcnt(24)\n"
  "ds_read_b64_tr_b16 v[126:127], v131 offset:13824\n"
  "s_waitcnt vmcnt(48)\n"
  "s_barrier\n"
  "v_mfma_f32_16x16x32_bf16 v[0:3], v[96:99], a[72:75], v[0:3]\n"
  "s_add_u32 m0, %[sM0], 0\n"
  "ds_read_b64_tr_b16 v[64:65], v130 offset:16384\n"
  "v_mfma_f32_16x16x32_bf16 v[32:35], v[96:99], a[76:79], v[32:35]\n"
  "s_waitcnt lgkmcnt(13)\n"
  "ds_read_b64_tr_b16 v[66:67], v130 offset:20480\n"
  "v_mfma_f32_16x16x32_bf16 v[4:7], v[100:103], a[72:75], v[4:7]\n"
  "s_add_u32 s60, %[sWoff], 655360\n"
  "ds_read_b64_tr_b16 v[68:69], v131 offset:16384\n"
  "buffer_load_dwordx4 %[vWd], %[srdW], s60 offen lds\n"
  "v_mfma_f32_16x16x32_bf16 v[36:39], v[100:103], a[76:79], v[36:39]\n"
  "s_waitcnt lgkmcnt(13)\n"
  "ds_read_b64_tr_b16 v[70:71], v131 offset:20480\n"
  "v_mfma_f32_16x16x32_bf16 v[8:11], v[104:107], a[72:75], v[8:11]\n"
  "s_add_u32 m0, %[sM0], 1024\n"
  "ds_read_b64_tr_b16 v[72:73], v130 offset:16896\n"
  "v_mfma_f32_16x16x32_bf16 v[40:43], v[104:107], a[76:79], v[40:43]\n"
  "s_waitcnt lgkmcnt(13)\n"
  "ds_read_b64_tr_b16 v[74:75], v130 offset:20992\n"
  "v_mfma_f32_16x16x32_bf16 v[12:15], v[108:111], a[72:75], v[12:15]\n"
  "s_add_u32 s61, %[sWoff], 655488\n"
  "ds_read_b64_tr_b16 v[76:77], v131 offset:16896\n"
  "buffer_load_dwordx4 %[vWd], %[srdW], s61 offen lds\n"
  "v_mfma_f32_16x16x32_bf16 v[44:47], v[108:111], a[76:79], v[44:47]\n"
  "s_waitcnt lgkmcnt(13)\n"
  "ds_read_b64_tr_b16 v[78:79], v131 offset:20992\n"
  "v_mfma_f32_16x16x32_bf16 v[16:19], v[112:115], a[72:75], v[16:19]\n"
  "s_add_u32 m0, %[sM0], 2048\n"
  "ds_read_b64_tr_b16 v[80:81], v130 offset:17408\n"
  "v_mfma_f32_16x16x32_bf16 v[48:51], v[112:115], a[76:79], v[48:51]\n"
  "s_waitcnt lgkmcnt(13)\n"
  "ds_read_b64_tr_b16 v[82:83], v130 offset:21504\n"
  "v_mfma_f32_16x16x32_bf16 v[20:23], v[116:119], a[72:75], v[20:23]\n"
  "s_add_u32 s60, %[sWoff], 663552\n"
  "ds_read_b64_tr_b16 v[84:85], v131 offset:17408\n"
  "buffer_load_dwordx4 %[vWd], %[srdW], s60 offen lds\n"
  "v_mfma_f32_16x16x32_bf16 v[52:55], v[116:119], a[76:79], v[52:55]\n"
  "s_waitcnt lgkmcnt(13)\n"
  "ds_read_b64_tr_b16 v[86:87], v131 offset:21504\n"
  "v_mfma_f32_16x16x32_bf16 v[24:27], v[120:123], a[72:75], v[24:27]\n"
  "s_add_u32 m0, %[sM0], 3072\n"
  "ds_read_b64_tr_b16 v[88:89], v130 offset:17920\n"
  "v_mfma_f32_16x16x32_bf16 v[56:59], v[120:123], a[76:79], v[56:59]\n"
  "s_waitcnt lgkmcnt(13)\n"
  "ds_read_b64_tr_b16 v[90:91], v130 offset:22016\n"
  "v_mfma_f32_16x16x32_bf16 v[28:31], v[124:127], a[72:75], v[28:31]\n"
  "s_add_u32 s61, %[sWoff], 663680\n"
  "ds_read_b64_tr_b16 v[92:93], v131 offset:17920\n"
  "buffer_load_dwordx4 %[vWd], %[srdW], s61 offen lds\n"
  "v_mfma_f32_16x16x32_bf16 v[60:63], v[124:127], a[76:79], v[60:63]\n"
  "s_waitcnt lgkmcnt(13)\n"
  "s_waitcnt vmcnt(22)\n"
  "ds_read_b64_tr_b16 v[94:95], v131 offset:22016\n"
  "v_mfma_f32_16x16x32_bf16 v[0:3], v[64:67], a[80:83], v[0:3]\n"
  "ds_read_b64_tr_b16 v[96:97], v130 offset:24576\n"
  "v_mfma_f32_16x16x32_bf16 v[32:35], v[64:67], a[84:87], v[32:35]\n"
  "s_waitcnt lgkmcnt(13)\n"
  "ds_read_b64_tr_b16 v[98:99], v130 offset:28672\n"
  "v_mfma_f32_16x16x32_bf16 v[4:7], v[68:71], a[80:83], v[4:7]\n"
  "s_add_u32 s62, %[sOoff], 16384\n"
  "ds_read_b64_tr_b16 v[100:101], v131 offset:24576\n"
  "buffer_load_dwordx4 a[128:131], %[vL16], %[srdO], s62 offen offset:0 sc1\n"
  "v_mfma_f32_16x16x32_bf16 v[36:39], v[68:71], a[84:87], v[36:39]\n"
  "s_waitcnt lgkmcnt(13)\n"
  "ds_read_b64_tr_b16 v[102:103], v131 offset:28672\n"
  "v_mfma_f32_16x16x32_bf16 v[8:11], v[72:75], a[80:83], v[8:11]\n"
  "ds_read_b64_tr_b16 v[104:105], v130 offset:25088\n"
  "v_mfma_f32_16x16x32_bf16 v[40:43], v[72:75], a[84:87], v[40:43]\n"
  "s_waitcnt lgkmcnt(13)\n"
  "ds_read_b64_tr_b16 v[106:107], v130 offset:29184\n"
  "v_mfma_f32_16x16x32_bf16 v[12:15], v[76:79], a[80:83], v[12:15]\n"
  "s_add_u32 s63, %[sOoff], 147456\n"
  "ds_read_b64_tr_b16 v[108:109], v131 offset:25088\n"
  "buffer_load_dwordx4 a[132:135], %[vL16], %[srdO], s63 offen offset:0 sc1\n"
  "v_mfma_f32_16x16x32_bf16 v[44:47], v[76:79], a[84:87], v[44:47]\n"
  "s_waitcnt lgkmcnt(13)\n"
  "ds_read_b64_tr_b16 v[110:111], v131 offset:29184\n"
  "v_mfma_f32_16x16x32_bf16 v[16:19], v[80:83], a[80:83], v[16:19]\n"
  "ds_read_b64_tr_b16 v[112:113], v130 offset:25600\n"
  "v_mfma_f32_16x16x32_bf16 v[48:51], v[80:83], a[84:87], v[48:51]\n"
  "s_waitcnt lgkmcnt(13)\n"
  "ds_read_b64_tr_b16 v[114:115], v130 offset:29696\n"
  "v_mfma_f32_16x16x32_bf16 v[20:23], v[84:87], a[80:83], v[20:23]\n"
  "ds_read_b64_tr_b16 v[116:117], v131 offset:25600\n"
  "buffer_load_dwordx4 a[136:139], %[vL16], %[srdO], s62 offen offset:1024 sc1\n"
  "v_mfma_f32_16x16x32_bf16 v[52:55], v[84:87], a[84:87], v[52:55]\n"
  "s_waitcnt lgkmcnt(13)\n"
  "ds_read_b64_tr_b16 v[118:119], v131 offset:29696\n"
  "v_mfma_f32_16x16x32_bf16 v[24:27], v[88:91], a[80:83], v[24:27]\n"
  "ds_read_b64_tr_b16 v[120:121], v130 offset:26112\n"
  "v_mfma_f32_16x16x32_bf16 v[56:59], v[88:91], a[84:87], v[56:59]\n"
  "s_waitcnt lgkmcnt(13)\n"
  "ds_read_b64_tr_b16 v[122:123], v130 offset:30208\n"
  "v_mfma_f32_16x16x32_bf16 v[28:31], v[92:95], a[80:83], v[28:31]\n"
  "ds_read_b64_tr_b16 v[124:125], v131 offset:26112\n"
  "buffer_load_dwordx4 a[140:143], %[vL16], %[srdO], s63 offen offset:1024 sc1\n"
  "v_mfma_f32_16x16x32_bf16 v[60:63], v[92:95], a[84:87], v[60:63]\n"
  "s_waitcnt lgkmcnt(13)\n"
  "s_waitcnt vmcnt(24)\n"
  "ds_read_b64_tr_b16 v[126:127], v131 offset:30208\n"
  "s_waitcnt vmcnt(36)\n"
  "s_barrier\n"
  "v_mfma_f32_16x16x32_bf16 v[0:3], v[96:99], a[88:91], v[0:3]\n"
  "s_add_u32 m0, %[sM0], 16384\n"
  "ds_read_b64_tr_b16 v[64:65], v130 offset:32768\n"
  "v_mfma_f32_16x16x32_bf16 v[32:35], v[96:99], a[92:95], v[32:35]\n"
  "s_waitcnt lgkmcnt(13)\n"
  "ds_read_b64_tr_b16 v[66:67], v130 offset:36864\n"
  "v_mfma_f32_16x16x32_bf16 v[4:7], v[100:103], a[88:91], v[4:7]\n"
  "s_add_u32 s60, %[sWoff], 720896\n"
  "ds_read_b64_tr_b16 v[68:69], v131 offset:32768\n"
  "buffer_load_dwordx4 %[vWd], %[srdW], s60 offen lds\n"
  "v_mfma_f32_16x16x32_bf16 v[36:39], v[100:103], a[92:95], v[36:39]\n"
  "s_waitcnt lgkmcnt(13)\n"
  "ds_read_b64_tr_b16 v[70:71], v131 offset:36864\n"
  "v_mfma_f32_16x16x32_bf16 v[8:11], v[104:107], a[88:91], v[8:11]\n"
  "s_add_u32 m0, %[sM0], 17408\n"
  "ds_read_b64_tr_b16 v[72:73], v130 offset:33280\n"
  "v_mfma_f32_16x16x32_bf16 v[40:43], v[104:107], a[92:95], v[40:43]\n"
  "s_waitcnt lgkmcnt(13)\n"
  "ds_read_b64_tr_b16 v[74:75], v130 offset:37376\n"
  "v_mfma_f32_16x16x32_bf16 v[12:15], v[108:111], a[88:91], v[12:15]\n"
  "s_add_u32 s61, %[sWoff], 721024\n"
  "ds_read_b64_tr_b16 v[76:77], v131 offset:33280\n"
  "buffer_load_dwordx4 %[vWd], %[srdW], s61 offen lds\n"
  "v_mfma_f32_16x16x32_bf16 v[44:47], v[108:111], a[92:95], v[44:47]\n"
  "s_waitcnt lgkmcnt(13)\n"
  "ds_read_b64_tr_b16 v[78:79], v131 offset:37376\n"
  "v_mfma_f32_16x16x32_bf16 v[16:19], v[112:115], a[88:91], v[16:19]\n"
  "s_add_u32 m0, %[sM0], 18432\n"
  "ds_read_b64_tr_b16 v[80:81], v130 offset:33792\n"
  "v_mfma_f32_16x16x32_bf16 v[48:51], v[112:115], a[92:95], v[48:51]\n"
  "s_waitcnt lgkmcnt(13)\n"
  "ds_read_b64_tr_b16 v[82:83], v130 offset:37888\n"
  "v_mfma_f32_16x16x32_bf16 v[20:23], v[116:119], a[88:91], v[20:23]\n"
  "s_add_u32 s60, %[sWoff], 729088\n"
  "ds_read_b64_tr_b16 v[84:85], v131 offset:33792\n"
  "buffer_load_dwordx4 %[vWd], %[srdW], s60 offen lds\n"
  "v_mfma_f32_16x16x32_bf16 v[52:55], v[116:119], a[92:95], v[52:55]\n"
  "s_waitcnt lgkmcnt(13)\n"
  "ds_read_b64_tr_b16 v[86:87], v131 offset:37888\n"
  "v_mfma_f32_16x16x32_bf16 v[24:27], v[120:123], a[88:91], v[24:27]\n"
  "s_add_u32 m0, %[sM0], 19456\n"
  "ds_read_b64_tr_b16 v[88:89], v130 offset:34304\n"
  "v_mfma_f32_16x16x32_bf16 v[56:59], v[120:123], a[92:95], v[56:59]\n"
  "s_waitcnt lgkmcnt(13)\n"
  "ds_read_b64_tr_b16 v[90:91], v130 offset:38400\n"
  "v_mfma_f32_16x16x32_bf16 v[28:31], v[124:127], a[88:91], v[28:31]\n"
  "s_add_u32 s61, %[sWoff], 729216\n"
  "ds_read_b64_tr_b16 v[92:93], v131 offset:34304\n"
  "buffer_load_dwordx4 %[vWd], %[srdW], s61 offen lds\n"
  "v_mfma_f32_16x16x32_bf16 v[60:63], v[124:127], a[92:95], v[60:63]\n"
  "s_waitcnt lgkmcnt(13)\n"
  "s_waitcnt vmcnt(22)\n"
  "ds_read_b64_tr_b16 v[94:95], v131 offset:38400\n"
  "v_mfma_f32_16x16x32_bf16 v[0:3], v[64:67], a[96:99], v[0:3]\n"
  "ds_read_b64_tr_b16 v[96:97], v130 offset:40960\n"
  "v_mfma_f32_16x16x32_bf16 v[32:35], v[64:67], a[100:103], v[32:35]\n"
  "s_waitcnt lgkmcnt(13)\n"
  "ds_read_b64_tr_b16 v[98:99], v130 offset:45056\n"
  "v_mfma_f32_16x16x32_bf16 v[4:7], v[68:71], a[96:99], v[4:7]\n"
  "ds_read_b64_tr_b16 v[100:101], v131 offset:40960\n"
  "buffer_load_dwordx4 a[144:147], %[vL16], %[srdO], s62 offen offset:2048 sc1\n"
  "v_mfma_f32_16x16x32_bf16 v[36:39], v[68:71], a[100:103], v[36:39]\n"
  "s_waitcnt lgkmcnt(13)\n"
  "ds_read_b64_tr_b16 v[102:103], v131 offset:45056\n"
  "v_mfma_f32_16x16x32_bf16 v[8:11], v[72:75], a[96:99], v[8:11]\n"
  "ds_read_b64_tr_b16 v[104:105], v130 offset:41472\n"
  "v_mfma_f32_16x16x32_bf16 v[40:43], v[72:75], a[100:103], v[40:43]\n"
  "s_waitcnt lgkmcnt(13)\n"
  "ds_read_b64_tr_b16 v[106:107], v130 offset:45568\n"
  "v_mfma_f32_16x16x32_bf16 v[12:15], v[76:79], a[96:99], v[12:15]\n"
  "ds_read_b64_tr_b16 v[108:109], v131 offset:41472\n"
  "buffer_load_dwordx4 a[148:151], %[vL16], %[srdO], s63 offen offset:2048 sc1\n"
  "v_mfma_f32_16x16x32_bf16 v[44:47], v[76:79], a[100:103], v[44:47]\n"
  "s_waitcnt lgkmcnt(13)\n"
  "ds_read_b64_tr_b16 v[110:111], v131 offset:45568\n"
  "v_mfma_f32_16x16x32_bf16 v[16:19], v[80:83], a[96:99], v[16:19]\n"
  "ds_read_b64_tr_b16 v[112:113], v130 offset:41984\n"
  "v_mfma_f32_16x16x32_bf16 v[48:51], v[80:83], a[100:103], v[48:51]\n"
  "s_waitcnt lgkmcnt(13)\n"
  "ds_read_b64_tr_b16 v[114:115], v130 offset:46080\n"
  "v_mfma_f32_16x16x32_bf16 v[20:23], v[84:87], a[96:99], v[20:23]\n"
  "ds_read_b64_tr_b16 v[116:117], v131 offset:41984\n"
  "buffer_load_dwordx4 a[152:155], %[vL16], %[srdO], s62 offen offset:3072 sc1\n"
  "v_mfma_f32_16x16x32_bf16 v[52:55], v[84:87], a[100:103], v[52:55]\n"
  "s_waitcnt lgkmcnt(13)\n"
  "ds_read_b64_tr_b16 v[118:119], v131 offset:46080\n"
  "v_mfma_f32_16x16x32_bf16 v[24:27], v[88:91], a[96:99], v[24:27]\n"
  "ds_read_b64_tr_b16 v[120:121], v130 offset:42496\n"
  "v_mfma_f32_16x16x32_bf16 v[56:59], v[88:91], a[100:103], v[56:59]\n"
  "s_waitcnt lgkmcnt(13)\n"
  "ds_read_b64_tr_b16 v[122:123], v130 offset:46592\n"
  "v_mfma_f32_16x16x32_bf16 v[28:31], v[92:95], a[96:99], v[28:31]\n"
  "ds_read_b64_tr_b16 v[124:125], v131 offset:42496\n"
  "buffer_load_dwordx4 a[156:159], %[vL16], %[srdO], s63 offen offset:3072 sc1\n"
  "v_mfma_f32_16x16x32_bf16 v[60:63], v[92:95], a[100:103], v[60:63]\n"
  "s_waitcnt lgkmcnt(13)\n"
  "s_waitcnt vmcnt(24)\n"
  "ds_read_b64_tr_b16 v[126:127], v131 offset:46592\n"
  "s_waitcnt vmcnt(36)\n"
  "s_barrier\n"
  "v_mfma_f32_16x16x32_bf16 v[0:3], v[96:99], a[104:107], v[0:3]\n"
  "s_add_u32 m0, %[sM0], 32768\n"
  "ds_read_b64_tr_b16 v[64:65], v130 offset:49152\n"
  "v_mfma_f32_16x16x32_bf16 v[32:35], v[96:99], a[108:111], v[32:35]\n"
  "s_waitcnt lgkmcnt(13)\n"
  "ds_read_b64_tr_b16 v[66:67], v130 offset:53248\n"
  "v_mfma_f32_16x16x32_bf16 v[4:7], v[100:103], a[104:107], v[4:7]\n"
  "s_add_u32 s60, %[sWoff], 786432\n"
  "ds_read_b64_tr_b16 v[68:69], v131 offset:49152\n"
  "buffer_load_dwordx4 %[vWd], %[srdW], s60 offen lds\n"
  "v_mfma_f32_16x16x32_bf16 v[36:39], v[100:103], a[108:111], v[36:39]\n"
  "s_waitcnt lgkmcnt(13)\n"
  "ds_read_b64_tr_b16 v[70:71], v131 offset:53248\n"
  "v_mfma_f32_16x16x32_bf16 v[8:11], v[104:107], a[104:107], v[8:11]\n"
  "s_add_u32 m0, %[sM0], 33792\n"
  "ds_read_b64_tr_b16 v[72:73], v130 offset:49664\n"
  "v_mfma_f32_16x16x32_bf16 v[40:43], v[104:107], a[108:111], v[40:43]\n"
  "s_waitcnt lgkmcnt(13)\n"
  "ds_read_b64_tr_b16 v[74:75], v130 offset:53760\n"
  "v_mfma_f32_16x16x32_bf16 v[12:15], v[108:111], a[104:107], v[12:15]\n"
  "s_add_u32 s61, %[sWoff], 786560\n"
  "ds_read_b64_tr_b16 v[76:77], v131 offset:49664\n"
  "buffer_load_dwordx4 %[vWd], %[srdW], s61 offen lds\n"
  "v_mfma_f32_16x16x32_bf16 v[44:47], v[108:111], a[108:111], v[44:47]\n"
  "s_waitcnt lgkmcnt(13)\n"
  "ds_read_b64_tr_b16 v[78:79], v131 offset:53760\n"
  "v_mfma_f32_16x16x32_bf16 v[16:19], v[112:115], a[104:107], v[16:19]\n"
  "s_add_u32 m0, %[sM0], 34816\n"
  "ds_read_b64_tr_b16 v[80:81], v130 offset:50176\n"
  "v_mfma_f32_16x16x32_bf16 v[48:51], v[112:115], a[108:111], v[48:51]\n"
  "s_waitcnt lgkmcnt(13)\n"
  "ds_read_b64_tr_b16 v[82:83], v130 offset:54272\n"
  "v_mfma_f32_16x16x32_bf16 v[20:23], v[116:119], a[104:107], v[20:23]\n"
  "s_add_u32 s60, %[sWoff], 794624\n"
  "ds_read_b64_tr_b16 v[84:85], v131 offset:50176\n"
  "buffer_load_dwordx4 %[vWd], %[srdW], s60 offen lds\n"
  "v_mfma_f32_16x16x32_bf16 v[52:55], v[116:119], a[108:111], v[52:55]\n"
  "s_waitcnt lgkmcnt(13)\n"
  "ds_read_b64_tr_b16 v[86:87], v131 offset:54272\n"
  "v_mfma_f32_16x16x32_bf16 v[24:27], v[120:123], a[104:107], v[24:27]\n"
  "s_add_u32 m0, %[sM0], 35840\n"
  "ds_read_b64_tr_b16 v[88:89], v130 offset:50688\n"
  "v_mfma_f32_16x16x32_bf16 v[56:59], v[120:123], a[108:111], v[56:59]\n"
  "s_waitcnt lgkmcnt(13)\n"
  "ds_read_b64_tr_b16 v[90:91], v130 offset:54784\n"
  "v_mfma_f32_16x16x32_bf16 v[28:31], v[124:127], a[104:107], v[28:31]\n"
  "s_add_u32 s61, %[sWoff], 794752\n"
  "ds_read_b64_tr_b16 v[92:93], v131 offset:50688\n"
  "buffer_load_dwordx4 %[vWd], %[srdW], s61 offen lds\n"
  "v_mfma_f32_16x16x32_bf16 v[60:63], v[124:127], a[108:111], v[60:63]\n"
  "s_waitcnt lgkmcnt(13)\n"
  "s_waitcnt vmcnt(22)\n"
  "ds_read_b64_tr_b16 v[94:95], v131 offset:54784\n"
  "v_mfma_f32_16x16x32_bf16 v[0:3], v[64:67], a[112:115], v[0:3]\n"
  "ds_read_b64_tr_b16 v[96:97], v130 offset:57344\n"
  "v_mfma_f32_16x16x32_bf16 v[32:35], v[64:67], a[116:119], v[32:35]\n"
  "s_waitcnt lgkmcnt(13)\n"
  "ds_read_b64_tr_b16 v[98:99], v130 offset:61440\n"
  "v_mfma_f32_16x16x32_bf16 v[4:7], v[68:71], a[112:115], v[4:7]\n"
  "s_add_u32 s62, %[sOoff], 20480\n"
  "ds_read_b64_tr_b16 v[100:101], v131 offset:57344\n"
  "buffer_load_dwordx4 a[160:163], %[vL16], %[srdO], s62 offen offset:0 sc1\n"
  "v_mfma_f32_16x16x32_bf16 v[36:39], v[68:71], a[116:119], v[36:39]\n"
  "s_waitcnt lgkmcnt(13)\n"
  "ds_read_b64_tr_b16 v[102:103], v131 offset:61440\n"
  "v_mfma_f32_16x16x32_bf16 v[8:11], v[72:75], a[112:115], v[8:11]\n"
  "ds_read_b64_tr_b16 v[104:105], v130 offset:57856\n"
  "v_mfma_f32_16x16x32_bf16 v[40:43], v[72:75], a[116:119], v[40:43]\n"
  "s_waitcnt lgkmcnt(13)\n"
  "ds_read_b64_tr_b16 v[106:107], v130 offset:61952\n"
  "v_mfma_f32_16x16x32_bf16 v[12:15], v[76:79], a[112:115], v[12:15]\n"
  "s_add_u32 s63, %[sOoff], 151552\n"
  "ds_read_b64_tr_b16 v[108:109], v131 offset:57856\n"
  "buffer_load_dwordx4 a[164:167], %[vL16], %[srdO], s63 offen offset:0 sc1\n"
  "v_mfma_f32_16x16x32_bf16 v[44:47], v[76:79], a[116:119], v[44:47]\n"
  "s_waitcnt lgkmcnt(13)\n"
  "ds_read_b64_tr_b16 v[110:111], v131 offset:61952\n"
  "v_mfma_f32_16x16x32_bf16 v[16:19], v[80:83], a[112:115], v[16:19]\n"
  "ds_read_b64_tr_b16 v[112:113], v130 offset:58368\n"
  "v_mfma_f32_16x16x32_bf16 v[48:51], v[80:83], a[116:119], v[48:51]\n"
  "s_waitcnt lgkmcnt(13)\n"
  "ds_read_b64_tr_b16 v[114:115], v130 offset:62464\n"
  "v_mfma_f32_16x16x32_bf16 v[20:23], v[84:87], a[112:115], v[20:23]\n"
  "ds_read_b64_tr_b16 v[116:117], v131 offset:58368\n"
  "buffer_load_dwordx4 a[168:171], %[vL16], %[srdO], s62 offen offset:1024 sc1\n"
  "v_mfma_f32_16x16x32_bf16 v[52:55], v[84:87], a[116:119], v[52:55]\n"
  "s_waitcnt lgkmcnt(13)\n"
  "ds_read_b64_tr_b16 v[118:119], v131 offset:62464\n"
  "v_mfma_f32_16x16x32_bf16 v[24:27], v[88:91], a[112:115], v[24:27]\n"
  "ds_read_b64_tr_b16 v[120:121], v130 offset:58880\n"
  "v_mfma_f32_16x16x32_bf16 v[56:59], v[88:91], a[116:119], v[56:59]\n"
  "s_waitcnt lgkmcnt(13)\n"
  "ds_read_b64_tr_b16 v[122:123], v130 offset:62976\n"
  "v_mfma_f32_16x16x32_bf16 v[28:31], v[92:95], a[112:115], v[28:31]\n"
  "ds_read_b64_tr_b16 v[124:125], v131 offset:58880\n"
  "buffer_load_dwordx4 a[172:175], %[vL16], %[srdO], s63 offen offset:1024 sc1\n"
  "v_mfma_f32_16x16x32_bf16 v[60:63], v[92:95], a[116:119], v[60:63]\n"
  "s_waitcnt lgkmcnt(13)\n"
  "s_waitcnt vmcnt(24)\n"
  "ds_read_b64_tr_b16 v[126:127], v131 offset:62976\n"
  "s_waitcnt vmcnt(36)\n"
  "s_barrier\n"
  "v_mfma_f32_16x16x32_bf16 v[0:3], v[96:99], a[120:123], v[0:3]\n"
  "s_add_u32 m0, %[sM0], 49152\n"
  "ds_read_b64_tr_b16 v[64:65], v132 offset:0\n"
  "v_mfma_f32_16x16x32_bf16 v[32:35], v[96:99], a[124:127], v[32:35]\n"
  "s_waitcnt lgkmcnt(13)\n"
  "ds_read_b64_tr_b16 v[66:67], v132 offset:4096\n"
  "v_mfma_f32_16x16x32_bf16 v[4:7], v[100:103], a[120:123], v[4:7]\n"
  "s_add_u32 s60, %[sWoff], 851968\n"
  "ds_read_b64_tr_b16 v[68:69], v133 offset:0\n"
  "buffer_load_dwordx4 %[vWd], %[srdW], s60 offen lds\n"
  "v_mfma_f32_16x16x32_bf16 v[36:39], v[100:103], a[124:127], v[36:39]\n"
  "s_waitcnt lgkmcnt(13)\n"
  "ds_read_b64_tr_b16 v[70:71], v133 offset:4096\n"
  "v_mfma_f32_16x16x32_bf16 v[8:11], v[104:107], a[120:123], v[8:11]\n"
  "s_add_u32 m0, %[sM0], 50176\n"
  "ds_read_b64_tr_b16 v[72:73], v132 offset:512\n"
  "v_mfma_f32_16x16x32_bf16 v[40:43], v[104:107], a[124:127], v[40:43]\n"
  "s_waitcnt lgkmcnt(13)\n"
  "ds_read_b64_tr_b16 v[74:75], v132 offset:4608\n"
  "v_mfma_f32_16x16x32_bf16 v[12:15], v[108:111], a[120:123], v[12:15]\n"
  "s_add_u32 s61, %[sWoff], 852096\n"
  "ds_read_b64_tr_b16 v[76:77], v133 offset:512\n"
  "buffer_load_dwordx4 %[vWd], %[srdW], s61 offen lds\n"
  "v_mfma_f32_16x16x32_bf16 v[44:47], v[108:111], a[124:127], v[44:47]\n"
  "s_waitcnt lgkmcnt(13)\n"
  "ds_read_b64_tr_b16 v[78:79], v133 offset:4608\n"
  "v_mfma_f32_16x16x32_bf16 v[16:19], v[112:115], a[120:123], v[16:19]\n"
  "s_add_u32 m0, %[sM0], 51200\n"
  "ds_read_b64_tr_b16 v[80:81], v132 offset:1024\n"
  "v_mfma_f32_16x16x32_bf16 v[48:51], v[112:115], a[124:127], v[48:51]\n"
  "s_waitcnt lgkmcnt(13)\n"
  "ds_read_b64_tr_b16 v[82:83], v132 offset:5120\n"
  "v_mfma_f32_16x16x32_bf16 v[20:23], v[116:119], a[120:123], v[20:23]\n"
  "s_add_u32 s60, %[sWoff], 860160\n"
  "ds_read_b64_tr_b16 v[84:85], v133 offset:1024\n"
  "buffer_load_dwordx4 %[vWd], %[srdW], s60 offen lds\n"
  "v_mfma_f32_16x16x32_bf16 v[52:55], v[116:119], a[124:127], v[52:55]\n"
  "s_waitcnt lgkmcnt(13)\n"
  "ds_read_b64_tr_b16 v[86:87], v133 offset:5120\n"
  "v_mfma_f32_16x16x32_bf16 v[24:27], v[120:123], a[120:123], v[24:27]\n"
  "s_add_u32 m0, %[sM0], 52224\n"
  "ds_read_b64_tr_b16 v[88:89], v132 offset:1536\n"
  "v_mfma_f32_16x16x32_bf16 v[56:59], v[120:123], a[124:127], v[56:59]\n"
  "s_waitcnt lgkmcnt(13)\n"
  "ds_read_b64_tr_b16 v[90:91], v132 offset:5632\n"
  "v_mfma_f32_16x16x32_bf16 v[28:31], v[124:127], a[120:123], v[28:31]\n"
  "s_add_u32 s61, %[sWoff], 860288\n"
  "ds_read_b64_tr_b16 v[92:93], v133 offset:1536\n"
  "buffer_load_dwordx4 %[vWd], %[srdW], s61 offen lds\n"
  "v_mfma_f32_16x16x32_bf16 v[60:63], v[124:127], a[124:127], v[60:63]\n"
  "s_waitcnt lgkmcnt(13)\n"
  "s_waitcnt vmcnt(22)\n"
  "ds_read_b64_tr_b16 v[94:95], v133 offset:5632\n"
  "v_mfma_f32_16x16x32_bf16 v[0:3], v[64:67], a[128:131], v[0:3]\n"
  "ds_read_b64_tr_b16 v[96:97], v132 offset:8192\n"
  "v_mfma_f32_16x16x32_bf16 v[32:35], v[64:67], a[132:135], v[32:35]\n"
  "s_waitcnt lgkmcnt(13)\n"
  "ds_read_b64_tr_b16 v[98:99], v132 offset:12288\n"
  "v_mfma_f32_16x16x32_bf16 v[4:7], v[68:71], a[128:131], v[4:7]\n"
  "ds_read_b64_tr_b16 v[100:101], v133 offset:8192\n"
  "buffer_load_dwordx4 a[176:179], %[vL16], %[srdO], s62 offen offset:2048 sc1\n"
  "v_mfma_f32_16x16x32_bf16 v[36:39], v[68:71], a[132:135], v[36:39]\n"
  "s_waitcnt lgkmcnt(13)\n"
  "ds_read_b64_tr_b16 v[102:103], v133 offset:12288\n"
  "v_mfma_f32_16x16x32_bf16 v[8:11], v[72:75], a[128:131], v[8:11]\n"
  "ds_read_b64_tr_b16 v[104:105], v132 offset:8704\n"
  "v_mfma_f32_16x16x32_bf16 v[40:43], v[72:75], a[132:135], v[40:43]\n"
  "s_waitcnt lgkmcnt(13)\n"
  "ds_read_b64_tr_b16 v[106:107], v132 offset:12800\n"
  "v_mfma_f32_16x16x32_bf16 v[12:15], v[76:79], a[128:131], v[12:15]\n"
  "ds_read_b64_tr_b16 v[108:109], v133 offset:8704\n"
  "buffer_load_dwordx4 a[180:183], %[vL16], %[srdO], s63 offen offset:2048 sc1\n"
  "v_mfma_f32_16x16x32_bf16 v[44:47], v[76:79], a[132:135], v[44:47]\n"
  "s_waitcnt lgkmcnt(13)\n"
  "ds_read_b64_tr_b16 v[110:111], v133 offset:12800\n"
  "v_mfma_f32_16x16x32_bf16 v[16:19], v[80:83], a[128:131], v[16:19]\n"
  "ds_read_b64_tr_b16 v[112:113], v132 offset:9216\n"
  "v_mfma_f32_16x16x32_bf16 v[48:51], v[80:83], a[132:135], v[48:51]\n"
  "s_waitcnt lgkmcnt(13)\n"
  "ds_read_b64_tr_b16 v[114:115], v132 offset:13312\n"
  "v_mfma_f32_16x16x32_bf16 v[20:23], v[84:87], a[128:131], v[20:23]\n"
  "ds_read_b64_tr_b16 v[116:117], v133 offset:9216\n"
  "buffer_load_dwordx4 a[184:187], %[vL16], %[srdO], s62 offen offset:3072 sc1\n"
  "v_mfma_f32_16x16x32_bf16 v[52:55], v[84:87], a[132:135], v[52:55]\n"
  "s_waitcnt lgkmcnt(13)\n"
  "ds_read_b64_tr_b16 v[118:119], v133 offset:13312\n"
  "v_mfma_f32_16x16x32_bf16 v[24:27], v[88:91], a[128:131], v[24:27]\n"
  "ds_read_b64_tr_b16 v[120:121], v132 offset:9728\n"
  "v_mfma_f32_16x16x32_bf16 v[56:59], v[88:91], a[132:135], v[56:59]\n"
  "s_waitcnt lgkmcnt(13)\n"
  "ds_read_b64_tr_b16 v[122:123], v132 offset:13824\n"
  "v_mfma_f32_16x16x32_bf16 v[28:31], v[92:95], a[128:131], v[28:31]\n"
  "ds_read_b64_tr_b16 v[124:125], v133 offset:9728\n"
  "buffer_load_dwordx4 a[188:191], %[vL16], %[srdO], s63 offen offset:3072 sc1\n"
  "v_mfma_f32_16x16x32_bf16 v[60:63], v[92:95], a[132:135], v[60:63]\n"
  "s_waitcnt lgkmcnt(13)\n"
  "s_waitcnt vmcnt(24)\n"
  "ds_read_b64_tr_b16 v[126:127], v133 offset:13824\n"
  "s_waitcnt vmcnt(36)\n"
  "s_barrier\n"
  "v_mfma_f32_16x16x32_bf16 v[0:3], v[96:99], a[136:139], v[0:3]\n"
  "s_add_u32 m0, %[sM0], 65536\n"
  "ds_read_b64_tr_b16 v[64:65], v132 offset:16384\n"
  "v_mfma_f32_16x16x32_bf16 v[32:35], v[96:99], a[140:143], v[32:35]\n"
  "s_waitcnt lgkmcnt(13)\n"
  "ds_read_b64_tr_b16 v[66:67], v132 offset:20480\n"
  "v_mfma_f32_16x16x32_bf16 v[4:7], v[100:103], a[136:139], v[4:7]\n"
  "s_add_u32 s60, %[sWoff], 917504\n"
  "ds_read_b64_tr_b16 v[68:69], v133 offset:16384\n"
  "buffer_load_dwordx4 %[vWd], %[srdW], s60 offen lds\n"
  "v_mfma_f32_16x16x32_bf16 v[36:39], v[100:103], a[140:143], v[36:39]\n"
  "s_waitcnt lgkmcnt(13)\n"
  "ds_read_b64_tr_b16 v[70:71], v133 offset:20480\n"
  "v_mfma_f32_16x16x32_bf16 v[8:11], v[104:107], a[136:139], v[8:11]\n"
  "s_add_u32 m0, %[sM0], 66560\n"
  "ds_read_b64_tr_b16 v[72:73], v132 offset:16896\n"
  "v_mfma_f32_16x16x32_bf16 v[40:43], v[104:107], a[140:143], v[40:43]\n"
  "s_waitcnt lgkmcnt(13)\n"
  "ds_read_b64_tr_b16 v[74:75], v132 offset:20992\n"
  "v_mfma_f32_16x16x32_bf16 v[12:15], v[108:111], a[136:139], v[12:15]\n"
  "s_add_u32 s61, %[sWoff], 917632\n"
  "ds_read_b64_tr_b16 v[76:77], v133 offset:16896\n"
  "buffer_load_dwordx4 %[vWd], %[srdW], s61 offen lds\n"
  "v_mfma_f32_16x16x32_bf16 v[44:47], v[108:111], a[140:143], v[44:47]\n"
  "s_waitcnt lgkmcnt(13)\n"
  "ds_read_b64_tr_b16 v[78:79], v133 offset:20992\n"
  "v_mfma_f32_16x16x32_bf16 v[16:19], v[112:115], a[136:139], v[16:19]\n"
  "s_add_u32 m0, %[sM0], 67584\n"
  "ds_read_b64_tr_b16 v[80:81], v132 offset:17408\n"
  "v_mfma_f32_16x16x32_bf16 v[48:51], v[112:115], a[140:143], v[48:51]\n"
  "s_waitcnt lgkmcnt(13)\n"
  "ds_read_b64_tr_b16 v[82:83], v132 offset:21504\n"
  "v_mfma_f32_16x16x32_bf16 v[20:23], v[116:119], a[136:139], v[20:23]\n"
  "s_add_u32 s60, %[sWoff], 925696\n"
  "ds_read_b64_tr_b16 v[84:85], v133 offset:17408\n"
  "buffer_load_dwordx4 %[vWd], %[srdW], s60 offen lds\n"
  "v_mfma_f32_16x16x32_bf16 v[52:55], v[116:119], a[140:143], v[52:55]\n"
  "s_waitcnt lgkmcnt(13)\n"
  "ds_read_b64_tr_b16 v[86:87], v133 offset:21504\n"
  "v_mfma_f32_16x16x32_bf16 v[24:27], v[120:123], a[136:139], v[24:27]\n"
  "s_add_u32 m0, %[sM0], 68608\n"
  "ds_read_b64_tr_b16 v[88:89], v132 offset:17920\n"
  "v_mfma_f32_16x16x32_bf16 v[56:59], v[120:123], a[140:143], v[56:59]\n"
  "s_waitcnt lgkmcnt(13)\n"
  "ds_read_b64_tr_b16 v[90:91], v132 offset:22016\n"
  "v_mfma_f32_16x16x32_bf16 v[28:31], v[124:127], a[136:139], v[28:31]\n"
  "s_add_u32 s61, %[sWoff], 925824\n"
  "ds_read_b64_tr_b16 v[92:93], v133 offset:17920\n"
  "buffer_load_dwordx4 %[vWd], %[srdW], s61 offen lds\n"
  "v_mfma_f32_16x16x32_bf16 v[60:63], v[124:127], a[140:143], v[60:63]\n"
  "s_waitcnt lgkmcnt(13)\n"
  "s_waitcnt vmcnt(22)\n"
  "ds_read_b64_tr_b16 v[94:95], v133 offset:22016\n"
  "v_mfma_f32_16x16x32_bf16 v[0:3], v[64:67], a[144:147], v[0:3]\n"
  "ds_read_b64_tr_b16 v[96:97], v132 offset:24576\n"
  "v_mfma_f32_16x16x32_bf16 v[32:35], v[64:67], a[148:151], v[32:35]\n"
  "s_waitcnt lgkmcnt(13)\n"
  "ds_read_b64_tr_b16 v[98:99], v132 offset:28672\n"
  "v_mfma_f32_16x16x32_bf16 v[4:7], v[68:71], a[144:147], v[4:7]\n"
  "s_add_u32 s62, %[sOoff], 24576\n"
  "ds_read_b64_tr_b16 v[100:101], v133 offset:24576\n"
  "buffer_load_dwordx4 a[192:195], %[vL16], %[srdO], s62 offen offset:0 sc1\n"
  "v_mfma_f32_16x16x32_bf16 v[36:39], v[68:71], a[148:151], v[36:39]\n"
  "s_waitcnt lgkmcnt(13)\n"
  "ds_read_b64_tr_b16 v[102:103], v133 offset:28672\n"
  "v_mfma_f32_16x16x32_bf16 v[8:11], v[72:75], a[144:147], v[8:11]\n"
  "ds_read_b64_tr_b16 v[104:105], v132 offset:25088\n"
  "v_mfma_f32_16x16x32_bf16 v[40:43], v[72:75], a[148:151], v[40:43]\n"
  "s_waitcnt lgkmcnt(13)\n"
  "ds_read_b64_tr_b16 v[106:107], v132 offset:29184\n"
  "v_mfma_f32_16x16x32_bf16 v[12:15], v[76:79], a[144:147], v[12:15]\n"
  "s_add_u32 s63, %[sOoff], 155648\n"
  "ds_read_b64_tr_b16 v[108:109], v133 offset:25088\n"
  "buffer_load_dwordx4 a[196:199], %[vL16], %[srdO], s63 offen offset:0 sc1\n"
  "v_mfma_f32_16x16x32_bf16 v[44:47], v[76:79], a[148:151], v[44:47]\n"
  "s_waitcnt lgkmcnt(13)\n"
  "ds_read_b64_tr_b16 v[110:111], v133 offset:29184\n"
  "v_mfma_f32_16x16x32_bf16 v[16:19], v[80:83], a[144:147], v[16:19]\n"
  "ds_read_b64_tr_b16 v[112:113], v132 offset:25600\n"
  "v_mfma_f32_16x16x32_bf16 v[48:51], v[80:83], a[148:151], v[48:51]\n"
  "s_waitcnt lgkmcnt(13)\n"
  "ds_read_b64_tr_b16 v[114:115], v132 offset:29696\n"
  "v_mfma_f32_16x16x32_bf16 v[20:23], v[84:87], a[144:147], v[20:23]\n"
  "ds_read_b64_tr_b16 v[116:117], v133 offset:25600\n"
  "buffer_load_dwordx4 a[200:203], %[vL16], %[srdO], s62 offen offset:1024 sc1\n"
  "v_mfma_f32_16x16x32_bf16 v[52:55], v[84:87], a[148:151], v[52:55]\n"
  "s_waitcnt lgkmcnt(13)\n"
  "ds_read_b64_tr_b16 v[118:119], v133 offset:29696\n"
  "v_mfma_f32_16x16x32_bf16 v[24:27], v[88:91], a[144:147], v[24:27]\n"
  "ds_read_b64_tr_b16 v[120:121], v132 offset:26112\n"
  "v_mfma_f32_16x16x32_bf16 v[56:59], v[88:91], a[148:151], v[56:59]\n"
  "s_waitcnt lgkmcnt(13)\n"
  "ds_read_b64_tr_b16 v[122:123], v132 offset:30208\n"
  "v_mfma_f32_16x16x32_bf16 v[28:31], v[92:95], a[144:147], v[28:31]\n"
  "ds_read_b64_tr_b16 v[124:125], v133 offset:26112\n"
  "buffer_load_dwordx4 a[204:207], %[vL16], %[srdO], s63 offen offset:1024 sc1\n"
  "v_mfma_f32_16x16x32_bf16 v[60:63], v[92:95], a[148:151], v[60:63]\n"
  "s_waitcnt lgkmcnt(13)\n"
  "s_waitcnt vmcnt(24)\n"
  "ds_read_b64_tr_b16 v[126:127], v133 offset:30208\n"
  "s_waitcnt vmcnt(36)\n"
  "s_barrier\n"
  "v_mfma_f32_16x16x32_bf16 v[0:3], v[96:99], a[152:155], v[0:3]\n"
  "s_add_u32 m0, %[sM0], 81920\n"
  "ds_read_b64_tr_b16 v[64:65], v128 offset:0\n"
  "v_mfma_f32_16x16x32_bf16 v[32:35], v[96:99], a[156:159], v[32:35]\n"
  "s_waitcnt lgkmcnt(13)\n"
  "ds_read_b64_tr_b16 v[66:67], v128 offset:4096\n"
  "v_mfma_f32_16x16x32_bf16 v[4:7], v[100:103], a[152:155], v[4:7]\n"
  "s_add_u32 s60, %[sWoff], 983040\n"
  "ds_read_b64_tr_b16 v[68:69], v129 offset:0\n"
  "buffer_load_dwordx4 %[vWd], %[srdW], s60 offen lds\n"
  "v_mfma_f32_16x16x32_bf16 v[36:39], v[100:103], a[156:159], v[36:39]\n"
  "s_waitcnt lgkmcnt(13)\n"
  "ds_read_b64_tr_b16 v[70:71], v129 offset:4096\n"
  "v_mfma_f32_16x16x32_bf16 v[8:11], v[104:107], a[152:155], v[8:11]\n"
  "s_add_u32 m0, %[sM0], 82944\n"
  "ds_read_b64_tr_b16 v[72:73], v128 offset:512\n"
  "v_mfma_f32_16x16x32_bf16 v[40:43], v[104:107], a[156:159], v[40:43]\n"
  "s_waitcnt lgkmcnt(13)\n"
  "ds_read_b64_tr_b16 v[74:75], v128 offset:4608\n"
  "v_mfma_f32_16x16x32_bf16 v[12:15], v[108:111], a[152:155], v[12:15]\n"
  "s_add_u32 s61, %[sWoff], 983168\n"
  "ds_read_b64_tr_b16 v[76:77], v129 offset:512\n"
  "buffer_load_dwordx4 %[vWd], %[srdW], s61 offen lds\n"
  "v_mfma_f32_16x16x32_bf16 v[44:47], v[108:111], a[156:159], v[44:47]\n"
  "s_waitcnt lgkmcnt(13)\n"
  "ds_read_b64_tr_b16 v[78:79], v129 offset:4608\n"
  "v_mfma_f32_16x16x32_bf16 v[16:19], v[112:115], a[152:155], v[16:19]\n"
  "s_add_u32 m0, %[sM0], 83968\n"
  "ds_read_b64_tr_b16 v[80:81], v128 offset:1024\n"
  "v_mfma_f32_16x16x32_bf16 v[48:51], v[112:115], a[156:159], v[48:51]\n"
  "s_waitcnt lgkmcnt(13)\n"
  "ds_read_b64_tr_b16 v[82:83], v128 offset:5120\n"
  "v_mfma_f32_16x16x32_bf16 v[20:23], v[116:119], a[152:155], v[20:23]\n"
  "s_add_u32 s60, %[sWoff], 991232\n"
  "ds_read_b64_tr_b16 v[84:85], v129 offset:1024\n"
  "buffer_load_dwordx4 %[vWd], %[srdW], s60 offen lds\n"
  "v_mfma_f32_16x16x32_bf16 v[52:55], v[116:119], a[156:159], v[52:55]\n"
  "s_waitcnt lgkmcnt(13)\n"
  "ds_read_b64_tr_b16 v[86:87], v129 offset:5120\n"
  "v_mfma_f32_16x16x32_bf16 v[24:27], v[120:123], a[152:155], v[24:27]\n"
  "s_add_u32 m0, %[sM0], 84992\n"
  "ds_read_b64_tr_b16 v[88:89], v128 offset:1536\n"
  "v_mfma_f32_16x16x32_bf16 v[56:59], v[120:123], a[156:159], v[56:59]\n"
  "s_waitcnt lgkmcnt(13)\n"
  "ds_read_b64_tr_b16 v[90:91], v128 offset:5632\n"
  "v_mfma_f32_16x16x32_bf16 v[28:31], v[124:127], a[152:155], v[28:31]\n"
  "s_add_u32 s61, %[sWoff], 991360\n"
  "ds_read_b64_tr_b16 v[92:93], v129 offset:1536\n"
  "buffer_load_dwordx4 %[vWd], %[srdW], s61 offen lds\n"
  "v_mfma_f32_16x16x32_bf16 v[60:63], v[124:127], a[156:159], v[60:63]\n"
  "s_waitcnt lgkmcnt(13)\n"
  "s_waitcnt vmcnt(22)\n"
  "ds_read_b64_tr_b16 v[94:95], v129 offset:5632\n"
  "v_mfma_f32_16x16x32_bf16 v[0:3], v[64:67], a[160:163], v[0:3]\n"
  "ds_read_b64_tr_b16 v[96:97], v128 offset:8192\n"
  "v_mfma_f32_16x16x32_bf16 v[32:35], v[64:67], a[164:167], v[32:35]\n"
  "s_waitcnt lgkmcnt(13)\n"
  "ds_read_b64_tr_b16 v[98:99], v128 offset:12288\n"
  "v_mfma_f32_16x16x32_bf16 v[4:7], v[68:71], a[160:163], v[4:7]\n"
  "ds_read_b64_tr_b16 v[100:101], v129 offset:8192\n"
  "buffer_load_dwordx4 a[208:211], %[vL16], %[srdO], s62 offen offset:2048 sc1\n"
  "v_mfma_f32_16x16x32_bf16 v[36:39], v[68:71], a[164:167], v[36:39]\n"
  "s_waitcnt lgkmcnt(13)\n"
  "ds_read_b64_tr_b16 v[102:103], v129 offset:12288\n"
  "v_mfma_f32_16x16x32_bf16 v[8:11], v[72:75], a[160:163], v[8:11]\n"
  "ds_read_b64_tr_b16 v[104:105], v128 offset:8704\n"
  "v_mfma_f32_16x16x32_bf16 v[40:43], v[72:75], a[164:167], v[40:43]\n"
  "s_waitcnt lgkmcnt(13)\n"
  "ds_read_b64_tr_b16 v[106:107], v128 offset:12800\n"
  "v_mfma_f32_16x16x32_bf16 v[12:15], v[76:79], a[160:163], v[12:15]\n"
  "ds_read_b64_tr_b16 v[108:109], v129 offset:8704\n"
  "buffer_load_dwordx4 a[212:215], %[vL16], %[srdO], s63 offen offset:2048 sc1\n"
  "v_mfma_f32_16x16x32_bf16 v[44:47], v[76:79], a[164:167], v[44:47]\n"
  "s_waitcnt lgkmcnt(13)\n"
  "ds_read_b64_tr_b16 v[110:111], v129 offset:12800\n"
  "v_mfma_f32_16x16x32_bf16 v[16:19], v[80:83], a[160:163], v[16:19]\n"
  "ds_read_b64_tr_b16 v[112:113], v128 offset:9216\n"
  "v_mfma_f32_16x16x32_bf16 v[48:51], v[80:83], a[164:167], v[48:51]\n"
  "s_waitcnt lgkmcnt(13)\n"
  "ds_read_b64_tr_b16 v[114:115], v128 offset:13312\n"
  "v_mfma_f32_16x16x32_bf16 v[20:23], v[84:87], a[160:163], v[20:23]\n"
  "ds_read_b64_tr_b16 v[116:117], v129 offset:9216\n"
  "buffer_load_dwordx4 a[216:219], %[vL16], %[srdO], s62 offen offset:3072 sc1\n"
  "v_mfma_f32_16x16x32_bf16 v[52:55], v[84:87], a[164:167], v[52:55]\n"
  "s_waitcnt lgkmcnt(13)\n"
  "ds_read_b64_tr_b16 v[118:119], v129 offset:13312\n"
  "v_mfma_f32_16x16x32_bf16 v[24:27], v[88:91], a[160:163], v[24:27]\n"
  "ds_read_b64_tr_b16 v[120:121], v128 offset:9728\n"
  "v_mfma_f32_16x16x32_bf16 v[56:59], v[88:91], a[164:167], v[56:59]\n"
  "s_waitcnt lgkmcnt(13)\n"
  "ds_read_b64_tr_b16 v[122:123], v128 offset:13824\n"
  "v_mfma_f32_16x16x32_bf16 v[28:31], v[92:95], a[160:163], v[28:31]\n"
  "ds_read_b64_tr_b16 v[124:125], v129 offset:9728\n"
  "buffer_load_dwordx4 a[220:223], %[vL16], %[srdO], s63 offen offset:3072 sc1\n"
  "v_mfma_f32_16x16x32_bf16 v[60:63], v[92:95], a[164:167], v[60:63]\n"
  "s_waitcnt lgkmcnt(13)\n"
  "s_waitcnt vmcnt(24)\n"
  "ds_read_b64_tr_b16 v[126:127], v129 offset:13824\n"
  "s_waitcnt vmcnt(36)\n"
  "s_barrier\n"
  "v_mfma_f32_16x16x32_bf16 v[0:3], v[96:99], a[168:171], v[0:3]\n"
  "ds_read_b64_tr_b16 v[64:65], v128 offset:16384\n"
  "v_mfma_f32_16x16x32_bf16 v[32:35], v[96:99], a[172:175], v[32:35]\n"
  "s_waitcnt lgkmcnt(13)\n"
  "ds_read_b64_tr_b16 v[66:67], v128 offset:20480\n"
  "v_mfma_f32_16x16x32_bf16 v[4:7], v[100:103], a[168:171], v[4:7]\n"
  "s_add_u32 s62, %[sOoff], 28672\n"
  "ds_read_b64_tr_b16 v[68:69], v129 offset:16384\n"
  "buffer_load_dwordx4 a[224:227], %[vL16], %[srdO], s62 offen offset:0 sc1\n"
  "v_mfma_f32_16x16x32_bf16 v[36:39], v[100:103], a[172:175], v[36:39]\n"
  "s_waitcnt lgkmcnt(13)\n"
  "ds_read_b64_tr_b16 v[70:71], v129 offset:20480\n"
  "v_mfma_f32_16x16x32_bf16 v[8:11], v[104:107], a[168:171], v[8:11]\n"
  "ds_read_b64_tr_b16 v[72:73], v128 offset:16896\n"
  "v_mfma_f32_16x16x32_bf16 v[40:43], v[104:107], a[172:175], v[40:43]\n"
  "s_waitcnt lgkmcnt(13)\n"
  "ds_read_b64_tr_b16 v[74:75], v128 offset:20992\n"
  "v_mfma_f32_16x16x32_bf16 v[12:15], v[108:111], a[168:171], v[12:15]\n"
  "s_add_u32 s63, %[sOoff], 159744\n"
  "ds_read_b64_tr_b16 v[76:77], v129 offset:16896\n"
  "buffer_load_dwordx4 a[228:231], %[vL16], %[srdO], s63 offen offset:0 sc1\n"
  "v_mfma_f32_16x16x32_bf16 v[44:47], v[108:111], a[172:175], v[44:47]\n"
  "s_waitcnt lgkmcnt(13)\n"
  "ds_read_b64_tr_b16 v[78:79], v129 offset:20992\n"
  "v_mfma_f32_16x16x32_bf16 v[16:19], v[112:115], a[168:171], v[16:19]\n"
  "ds_read_b64_tr_b16 v[80:81], v128 offset:17408\n"
  "v_mfma_f32_16x16x32_bf16 v[48:51], v[112:115], a[172:175], v[48:51]\n"
  "s_waitcnt lgkmcnt(13)\n"
  "ds_read_b64_tr_b16 v[82:83], v128 offset:21504\n"
  "v_mfma_f32_16x16x32_bf16 v[20:23], v[116:119], a[168:171], v[20:23]\n"
  "ds_read_b64_tr_b16 v[84:85], v129 offset:17408\n"
  "buffer_load_dwordx4 a[232:235], %[vL16], %[srdO], s62 offen offset:1024 sc1\n"
  "v_mfma_f32_16x16x32_bf16 v[52:55], v[116:119], a[172:175], v[52:55]\n"
  "s_waitcnt lgkmcnt(13)\n"
  "ds_read_b64_tr_b16 v[86:87], v129 offset:21504\n"
  "v_mfma_f32_16x16x32_bf16 v[24:27], v[120:123], a[168:171], v[24:27]\n"
  "ds_read_b64_tr_b16 v[88:89], v128 offset:17920\n"
  "v_mfma_f32_16x16x32_bf16 v[56:59], v[120:123], a[172:175], v[56:59]\n"
  "s_waitcnt lgkmcnt(13)\n"
  "ds_read_b64_tr_b16 v[90:91], v128 offset:22016\n"
  "v_mfma_f32_16x16x32_bf16 v[28:31], v[124:127], a[168:171], v[28:31]\n"
  "ds_read_b64_tr_b16 v[92:93], v129 offset:17920\n"
  "buffer_load_dwordx4 a[236:239], %[vL16], %[srdO], s63 offen offset:1024 sc1\n"
  "v_mfma_f32_16x16x32_bf16 v[60:63], v[124:127], a[172:175], v[60:63]\n"
  "s_waitcnt lgkmcnt(13)\n"
  "s_waitcnt vmcnt(22)\n"
  "ds_read_b64_tr_b16 v[94:95], v129 offset:22016\n"
  "v_mfma_f32_16x16x32_bf16 v[0:3], v[64:67], a[176:179], v[0:3]\n"
  "ds_read_b64_tr_b16 v[96:97], v128 offset:24576\n"
  "v_mfma_f32_16x16x32_bf16 v[32:35], v[64:67], a[180:183], v[32:35]\n"
  "s_waitcnt lgkmcnt(13)\n"
  "ds_read_b64_tr_b16 v[98:99], v128 offset:28672\n"
  "v_mfma_f32_16x16x32_bf16 v[4:7], v[68:71], a[176:179], v[4:7]\n"
  "ds_read_b64_tr_b16 v[100:101], v129 offset:24576\n"
  "buffer_load_dwordx4 a[240:243], %[vL16], %[srdO], s62 offen offset:2048 sc1\n"
  "v_mfma_f32_16x16x32_bf16 v[36:39], v[68:71], a[180:183], v[36:39]\n"
  "s_waitcnt lgkmcnt(13)\n"
  "ds_read_b64_tr_b16 v[102:103], v129 offset:28672\n"
  "v_mfma_f32_16x16x32_bf16 v[8:11], v[72:75], a[176:179], v[8:11]\n"
  "ds_read_b64_tr_b16 v[104:105], v128 offset:25088\n"
  "v_mfma_f32_16x16x32_bf16 v[40:43], v[72:75], a[180:183], v[40:43]\n"
  "s_waitcnt lgkmcnt(13)\n"
  "ds_read_b64_tr_b16 v[106:107], v128 offset:29184\n"
  "v_mfma_f32_16x16x32_bf16 v[12:15], v[76:79], a[176:179], v[12:15]\n"
  "ds_read_b64_tr_b16 v[108:109], v129 offset:25088\n"
  "buffer_load_dwordx4 a[244:247], %[vL16], %[srdO], s63 offen offset:2048 sc1\n"
  "v_mfma_f32_16x16x32_bf16 v[44:47], v[76:79], a[180:183], v[44:47]\n"
  "s_waitcnt lgkmcnt(13)\n"
  "ds_read_b64_tr_b16 v[110:111], v129 offset:29184\n"
  "v_mfma_f32_16x16x32_bf16 v[16:19], v[80:83], a[176:179], v[16:19]\n"
  "ds_read_b64_tr_b16 v[112:113], v128 offset:25600\n"
  "v_mfma_f32_16x16x32_bf16 v[48:51], v[80:83], a[180:183], v[48:51]\n"
  "s_waitcnt lgkmcnt(13)\n"
  "ds_read_b64_tr_b16 v[114:115], v128 offset:29696\n"
  "v_mfma_f32_16x16x32_bf16 v[20:23], v[84:87], a[176:179], v[20:23]\n"
  "ds_read_b64_tr_b16 v[116:117], v129 offset:25600\n"
  "buffer_load_dwordx4 a[248:251], %[vL16], %[srdO], s62 offen offset:3072 sc1\n"
  "v_mfma_f32_16x16x32_bf16 v[52:55], v[84:87], a[180:183], v[52:55]\n"
  "s_waitcnt lgkmcnt(13)\n"
  "ds_read_b64_tr_b16 v[118:119], v129 offset:29696\n"
  "v_mfma_f32_16x16x32_bf16 v[24:27], v[88:91], a[176:179], v[24:27]\n"
  "ds_read_b64_tr_b16 v[120:121], v128 offset:26112\n"
  "v_mfma_f32_16x16x32_bf16 v[56:59], v[88:91], a[180:183], v[56:59]\n"
  "s_waitcnt lgkmcnt(13)\n"
  "ds_read_b64_tr_b16 v[122:123], v128 offset:30208\n"
  "v_mfma_f32_16x16x32_bf16 v[28:31], v[92:95], a[176:179], v[28:31]\n"
  "ds_read_b64_tr_b16 v[124:125], v129 offset:26112\n"
  "buffer_load_dwordx4 a[252:255], %[vL16], %[srdO], s63 offen offset:3072 sc1\n"
  "v_mfma_f32_16x16x32_bf16 v[60:63], v[92:95], a[180:183], v[60:63]\n"
  "s_waitcnt lgkmcnt(13)\n"
  "s_waitcnt vmcnt(24)\n"
  "ds_read_b64_tr_b16 v[126:127], v129 offset:30208\n"
  "s_waitcnt vmcnt(36)\n"
  "s_barrier\n"
  "v_mfma_f32_16x16x32_bf16 v[0:3], v[96:99], a[184:187], v[0:3]\n"
  "ds_read_b64_tr_b16 v[64:65], v128 offset:32768\n"
  "v_mfma_f32_16x16x32_bf16 v[32:35], v[96:99], a[188:191], v[32:35]\n"
  "s_waitcnt lgkmcnt(13)\n"
  "ds_read_b64_tr_b16 v[66:67], v128 offset:36864\n"
  "v_mfma_f32_16x16x32_bf16 v[4:7], v[100:103], a[184:187], v[4:7]\n"
  "ds_read_b64_tr_b16 v[68:69], v129 offset:32768\n"
  "v_mfma_f32_16x16x32_bf16 v[36:39], v[100:103], a[188:191], v[36:39]\n"
  "s_waitcnt lgkmcnt(13)\n"
  "ds_read_b64_tr_b16 v[70:71], v129 offset:36864\n"
  "v_mfma_f32_16x16x32_bf16 v[8:11], v[104:107], a[184:187], v[8:11]\n"
  "ds_read_b64_tr_b16 v[72:73], v128 offset:33280\n"
  "v_mfma_f32_16x16x32_bf16 v[40:43], v[104:107], a[188:191], v[40:43]\n"
  "s_waitcnt lgkmcnt(13)\n"
  "ds_read_b64_tr_b16 v[74:75], v128 offset:37376\n"
  "v_mfma_f32_16x16x32_bf16 v[12:15], v[108:111], a[184:187], v[12:15]\n"
  "ds_read_b64_tr_b16 v[76:77], v129 offset:33280\n"
  "v_mfma_f32_16x16x32_bf16 v[44:47], v[108:111], a[188:191], v[44:47]\n"
  "s_waitcnt lgkmcnt(13)\n"
  "ds_read_b64_tr_b16 v[78:79], v129 offset:37376\n"
  "v_mfma_f32_16x16x32_bf16 v[16:19], v[112:115], a[184:187], v[16:19]\n"
  "ds_read_b64_tr_b16 v[80:81], v128 offset:33792\n"
  "v_mfma_f32_16x16x32_bf16 v[48:51], v[112:115], a[188:191], v[48:51]\n"
  "s_waitcnt lgkmcnt(13)\n"
  "ds_read_b64_tr_b16 v[82:83], v128 offset:37888\n"
  "v_mfma_f32_16x16x32_bf16 v[20:23], v[116:119], a[184:187], v[20:23]\n"
  "ds_read_b64_tr_b16 v[84:85], v129 offset:33792\n"
  "v_mfma_f32_16x16x32_bf16 v[52:55], v[116:119], a[188:191], v[52:55]\n"
  "s_waitcnt lgkmcnt(13)\n"
  "ds_read_b64_tr_b16 v[86:87], v129 offset:37888\n"
  "v_mfma_f32_16x16x32_bf16 v[24:27], v[120:123], a[184:187], v[24:27]\n"
  "ds_read_b64_tr_b16 v[88:89], v128 offset:34304\n"
  "v_mfma_f32_16x16x32_bf16 v[56:59], v[120:123], a[188:191], v[56:59]\n"
  "s_waitcnt lgkmcnt(13)\n"
  "ds_read_b64_tr_b16 v[90:91], v128 offset:38400\n"
  "v_mfma_f32_16x16x32_bf16 v[28:31], v[124:127], a[184:187], v[28:31]\n"
  "ds_read_b64_tr_b16 v[92:93], v129 offset:34304\n"
  "v_mfma_f32_16x16x32_bf16 v[60:63], v[124:127], a[188:191], v[60:63]\n"
  "s_waitcnt lgkmcnt(13)\n"
  "s_waitcnt vmcnt(18)\n"
  "ds_read_b64_tr_b16 v[94:95], v129 offset:38400\n"
  "v_mfma_f32_16x16x32_bf16 v[0:3], v[64:67], a[192:195], v[0:3]\n"
  "ds_read_b64_tr_b16 v[96:97], v128 offset:40960\n"
  "v_mfma_f32_16x16x32_bf16 v[32:35], v[64:67], a[196:199], v[32:35]\n"
  "s_waitcnt lgkmcnt(13)\n"
  "ds_read_b64_tr_b16 v[98:99], v128 offset:45056\n"
  "v_mfma_f32_16x16x32_bf16 v[4:7], v[68:71], a[192:195], v[4:7]\n"
  "s_add_u32 m0, %[sM0], 98304\n"
  "ds_read_b64_tr_b16 v[100:101], v129 offset:40960\n"
  "v_mfma_f32_16x16x32_bf16 v[36:39], v[68:71], a[196:199], v[36:39]\n"
  "s_waitcnt lgkmcnt(13)\n"
  "ds_read_b64_tr_b16 v[102:103], v129 offset:45056\n"
  "v_mfma_f32_16x16x32_bf16 v[8:11], v[72:75], a[192:195], v[8:11]\n"
  "s_add_u32 s60, %[sGoff], 0\n"
  "ds_read_b64_tr_b16 v[104:105], v128 offset:41472\n"
  "buffer_load_dwordx4 %[vGU], %[srdG], s60 offen lds\n"
  "v_mfma_f32_16x16x32_bf16 v[40:43], v[72:75], a[196:199], v[40:43]\n"
  "s_waitcnt lgkmcnt(13)\n"
  "ds_read_b64_tr_b16 v[106:107], v128 offset:45568\n"
  "v_mfma_f32_16x16x32_bf16 v[12:15], v[76:79], a[192:195], v[12:15]\n"
  "ds_read_b64_tr_b16 v[108:109], v129 offset:41472\n"
  "v_mfma_f32_16x16x32_bf16 v[44:47], v[76:79], a[196:199], v[44:47]\n"
  "s_waitcnt lgkmcnt(13)\n"
  "ds_read_b64_tr_b16 v[110:111], v129 offset:45568\n"
  "v_mfma_f32_16x16x32_bf16 v[16:19], v[80:83], a[192:195], v[16:19]\n"
  "ds_read_b64_tr_b16 v[112:113], v128 offset:41984\n"
  "v_mfma_f32_16x16x32_bf16 v[48:51], v[80:83], a[196:199], v[48:51]\n"
  "s_waitcnt lgkmcnt(13)\n"
  "ds_read_b64_tr_b16 v[114:115], v128 offset:46080\n"
  "v_mfma_f32_16x16x32_bf16 v[20:23], v[84:87], a[192:195], v[20:23]\n"
  "s_add_u32 m0, %[sM0], 114688\n"
  "ds_read_b64_tr_b16 v[116:117], v129 offset:41984\n"
  "v_mfma_f32_16x16x32_bf16 v[52:55], v[84:87], a[196:199], v[52:55]\n"
  "s_waitcnt lgkmcnt(13)\n"
  "ds_read_b64_tr_b16 v[118:119], v129 offset:46080\n"
  "v_mfma_f32_16x16x32_bf16 v[24:27], v[88:91], a[192:195], v[24:27]\n"
  "s_add_u32 s61, %[sGoff], 0\n"
  "ds_read_b64_tr_b16 v[120:121], v128 offset:42496\n"
  "buffer_load_dwordx4 %[vGU], %[srdU], s61 offen lds\n"
  "v_mfma_f32_16x16x32_bf16 v[56:59], v[88:91], a[196:199], v[56:59]\n"
  "s_waitcnt lgkmcnt(13)\n"
  "ds_read_b64_tr_b16 v[122:123], v128 offset:46592\n"
  "v_mfma_f32_16x16x32_bf16 v[28:31], v[92:95], a[192:195], v[28:31]\n"
  "ds_read_b64_tr_b16 v[124:125], v129 offset:42496\n"
  "v_mfma_f32_16x16x32_bf16 v[60:63], v[92:95], a[196:199], v[60:63]\n"
  "s_waitcnt lgkmcnt(13)\n"
  "s_waitcnt vmcnt(18)\n"
  "ds_read_b64_tr_b16 v[126:127], v129 offset:46592\n"
  "s_waitcnt vmcnt(30)\n"
  "s_barrier\n"
  "v_mfma_f32_16x16x32_bf16 v[0:3], v[96:99], a[200:203], v[0:3]\n"
  "ds_read_b64_tr_b16 v[64:65], v128 offset:49152\n"
  "v_mfma_f32_16x16x32_bf16 v[32:35], v[96:99], a[204:207], v[32:35]\n"
  "s_waitcnt lgkmcnt(13)\n"
  "ds_read_b64_tr_b16 v[66:67], v128 offset:53248\n"
  "v_mfma_f32_16x16x32_bf16 v[4:7], v[100:103], a[200:203], v[4:7]\n"
  "s_add_u32 m0, %[sM0], 131072\n"
  "ds_read_b64_tr_b16 v[68:69], v129 offset:49152\n"
  "v_mfma_f32_16x16x32_bf16 v[36:39], v[100:103], a[204:207], v[36:39]\n"
  "s_waitcnt lgkmcnt(13)\n"
  "ds_read_b64_tr_b16 v[70:71], v129 offset:53248\n"
  "v_mfma_f32_16x16x32_bf16 v[8:11], v[104:107], a[200:203], v[8:11]\n"
  "s_add_u32 s60, %[sDnoff], 0\n"
  "ds_read_b64_tr_b16 v[72:73], v128 offset:49664\n"
  "buffer_load_dwordx4 %[vDn], %[srdDn], s60 offen lds\n"
  "v_mfma_f32_16x16x32_bf16 v[40:43], v[104:107], a[204:207], v[40:43]\n"
  "s_waitcnt lgkmcnt(13)\n"
  "ds_read_b64_tr_b16 v[74:75], v128 offset:53760\n"
  "v_mfma_f32_16x16x32_bf16 v[12:15], v[108:111], a[200:203], v[12:15]\n"
  "ds_read_b64_tr_b16 v[76:77], v129 offset:49664\n"
  "v_mfma_f32_16x16x32_bf16 v[44:47], v[108:111], a[204:207], v[44:47]\n"
  "s_waitcnt lgkmcnt(13)\n"
  "ds_read_b64_tr_b16 v[78:79], v129 offset:53760\n"
  "v_mfma_f32_16x16x32_bf16 v[16:19], v[112:115], a[200:203], v[16:19]\n"
  "ds_read_b64_tr_b16 v[80:81], v128 offset:50176\n"
  "v_mfma_f32_16x16x32_bf16 v[48:51], v[112:115], a[204:207], v[48:51]\n"
  "s_waitcnt lgkmcnt(13)\n"
  "ds_read_b64_tr_b16 v[82:83], v128 offset:54272\n"
  "v_mfma_f32_16x16x32_bf16 v[20:23], v[116:119], a[200:203], v[20:23]\n"
  "s_add_u32 m0, %[sM0], 99328\n"
  "ds_read_b64_tr_b16 v[84:85], v129 offset:50176\n"
  "v_mfma_f32_16x16x32_bf16 v[52:55], v[116:119], a[204:207], v[52:55]\n"
  "s_waitcnt lgkmcnt(13)\n"
  "ds_read_b64_tr_b16 v[86:87], v129 offset:54272\n"
  "v_mfma_f32_16x16x32_bf16 v[24:27], v[120:123], a[200:203], v[24:27]\n"
  "s_add_u32 s61, %[sGoff], 114688\n"
  "ds_read_b64_tr_b16 v[88:89], v128 offset:50688\n"
  "buffer_load_dwordx4 %[vGU], %[srdG], s61 offen lds\n"
  "v_mfma_f32_16x16x32_bf16 v[56:59], v[120:123], a[204:207], v[56:59]\n"
  "s_waitcnt lgkmcnt(13)\n"
  "ds_read_b64_tr_b16 v[90:91], v128 offset:54784\n"
  "v_mfma_f32_16x16x32_bf16 v[28:31], v[124:127], a[200:203], v[28:31]\n"
  "ds_read_b64_tr_b16 v[92:93], v129 offset:50688\n"
  "v_mfma_f32_16x16x32_bf16 v[60:63], v[124:127], a[204:207], v[60:63]\n"
  "s_waitcnt lgkmcnt(13)\n"
  "s_waitcnt vmcnt(14)\n"
  "ds_read_b64_tr_b16 v[94:95], v129 offset:54784\n"
  "v_mfma_f32_16x16x32_bf16 v[0:3], v[64:67], a[208:211], v[0:3]\n"
  "ds_read_b64_tr_b16 v[96:97], v128 offset:57344\n"
  "v_mfma_f32_16x16x32_bf16 v[32:35], v[64:67], a[212:215], v[32:35]\n"
  "s_waitcnt lgkmcnt(13)\n"
  "ds_read_b64_tr_b16 v[98:99], v128 offset:61440\n"
  "v_mfma_f32_16x16x32_bf16 v[4:7], v[68:71], a[208:211], v[4:7]\n"
  "s_add_u32 m0, %[sM0], 115712\n"
  "ds_read_b64_tr_b16 v[100:101], v129 offset:57344\n"
  "v_mfma_f32_16x16x32_bf16 v[36:39], v[68:71], a[212:215], v[36:39]\n"
  "s_waitcnt lgkmcnt(13)\n"
  "ds_read_b64_tr_b16 v[102:103], v129 offset:61440\n"
  "v_mfma_f32_16x16x32_bf16 v[8:11], v[72:75], a[208:211], v[8:11]\n"
  "s_add_u32 s60, %[sGoff], 114688\n"
  "ds_read_b64_tr_b16 v[104:105], v128 offset:57856\n"
  "buffer_load_dwordx4 %[vGU], %[srdU], s60 offen lds\n"
  "v_mfma_f32_16x16x32_bf16 v[40:43], v[72:75], a[212:215], v[40:43]\n"
  "s_waitcnt lgkmcnt(13)\n"
  "ds_read_b64_tr_b16 v[106:107], v128 offset:61952\n"
  "v_mfma_f32_16x16x32_bf16 v[12:15], v[76:79], a[208:211], v[12:15]\n"
  "ds_read_b64_tr_b16 v[108:109], v129 offset:57856\n"
  "v_mfma_f32_16x16x32_bf16 v[44:47], v[76:79], a[212:215], v[44:47]\n"
  "s_waitcnt lgkmcnt(13)\n"
  "ds_read_b64_tr_b16 v[110:111], v129 offset:61952\n"
  "v_mfma_f32_16x16x32_bf16 v[16:19], v[80:83], a[208:211], v[16:19]\n"
  "ds_read_b64_tr_b16 v[112:113], v128 offset:58368\n"
  "v_mfma_f32_16x16x32_bf16 v[48:51], v[80:83], a[212:215], v[48:51]\n"
  "s_waitcnt lgkmcnt(13)\n"
  "ds_read_b64_tr_b16 v[114:115], v128 offset:62464\n"
  "v_mfma_f32_16x16x32_bf16 v[20:23], v[84:87], a[208:211], v[20:23]\n"
  "s_add_u32 m0, %[sM0], 132096\n"
  "ds_read_b64_tr_b16 v[116:117], v129 offset:58368\n"
  "v_mfma_f32_16x16x32_bf16 v[52:55], v[84:87], a[212:215], v[52:55]\n"
  "s_waitcnt lgkmcnt(13)\n"
  "ds_read_b64_tr_b16 v[118:119], v129 offset:62464\n"
  "v_mfma_f32_16x16x32_bf16 v[24:27], v[88:91], a[208:211], v[24:27]\n"
  "s_add_u32 s61, %[sDnoff], 64\n"
  "ds_read_b64_tr_b16 v[120:121], v128 offset:58880\n"
  "buffer_load_dwordx4 %[vDn], %[srdDn], s61 offen lds\n"
  "v_mfma_f32_16x16x32_bf16 v[56:59], v[88:91], a[212:215], v[56:59]\n"
  "s_waitcnt lgkmcnt(13)\n"
  "ds_read_b64_tr_b16 v[122:123], v128 offset:62976\n"
  "v_mfma_f32_16x16x32_bf16 v[28:31], v[92:95], a[208:211], v[28:31]\n"
  "ds_read_b64_tr_b16 v[124:125], v129 offset:58880\n"
  "v_mfma_f32_16x16x32_bf16 v[60:63], v[92:95], a[212:215], v[60:63]\n"
  "s_waitcnt lgkmcnt(13)\n"
  "s_waitcnt vmcnt(14)\n"
  "ds_read_b64_tr_b16 v[126:127], v129 offset:62976\n"
  "s_waitcnt vmcnt(26)\n"
  "s_barrier\n"
  "v_mfma_f32_16x16x32_bf16 v[0:3], v[96:99], a[216:219], v[0:3]\n"
  "ds_read_b64_tr_b16 v[64:65], v130 offset:0\n"
  "v_mfma_f32_16x16x32_bf16 v[32:35], v[96:99], a[220:223], v[32:35]\n"
  "s_waitcnt lgkmcnt(13)\n"
  "ds_read_b64_tr_b16 v[66:67], v130 offset:4096\n"
  "v_mfma_f32_16x16x32_bf16 v[4:7], v[100:103], a[216:219], v[4:7]\n"
  "s_add_u32 m0, %[sM0], 100352\n"
  "ds_read_b64_tr_b16 v[68:69], v131 offset:0\n"
  "v_mfma_f32_16x16x32_bf16 v[36:39], v[100:103], a[220:223], v[36:39]\n"
  "s_waitcnt lgkmcnt(13)\n"
  "ds_read_b64_tr_b16 v[70:71], v131 offset:4096\n"
  "v_mfma_f32_16x16x32_bf16 v[8:11], v[104:107], a[216:219], v[8:11]\n"
  "s_add_u32 s60, %[sGoff], 229376\n"
  "ds_read_b64_tr_b16 v[72:73], v130 offset:512\n"
  "buffer_load_dwordx4 %[vGU], %[srdG], s60 offen lds\n"
  "v_mfma_f32_16x16x32_bf16 v[40:43], v[104:107], a[220:223], v[40:43]\n"
  "s_waitcnt lgkmcnt(13)\n"
  "ds_read_b64_tr_b16 v[74:75], v130 offset:4608\n"
  "v_mfma_f32_16x16x32_bf16 v[12:15], v[108:111], a[216:219], v[12:15]\n"
  "ds_read_b64_tr_b16 v[76:77], v131 offset:512\n"
  "v_mfma_f32_16x16x32_bf16 v[44:47], v[108:111], a[220:223], v[44:47]\n"
  "s_waitcnt lgkmcnt(13)\n"
  "ds_read_b64_tr_b16 v[78:79], v131 offset:4608\n"
  "v_mfma_f32_16x16x32_bf16 v[16:19], v[112:115], a[216:219], v[16:19]\n"
  "ds_read_b64_tr_b16 v[80:81], v130 offset:1024\n"
  "v_mfma_f32_16x16x32_bf16 v[48:51], v[112:115], a[220:223], v[48:51]\n"
  "s_waitcnt lgkmcnt(13)\n"
  "ds_read_b64_tr_b16 v[82:83], v130 offset:5120\n"
  "v_mfma_f32_16x16x32_bf16 v[20:23], v[116:119], a[216:219], v[20:23]\n"
  "s_add_u32 m0, %[sM0], 116736\n"
  "ds_read_b64_tr_b16 v[84:85], v131 offset:1024\n"
  "v_mfma_f32_16x16x32_bf16 v[52:55], v[116:119], a[220:223], v[52:55]\n"
  "s_waitcnt lgkmcnt(13)\n"
  "ds_read_b64_tr_b16 v[86:87], v131 offset:5120\n"
  "v_mfma_f32_16x16x32_bf16 v[24:27], v[120:123], a[216:219], v[24:27]\n"
  "s_add_u32 s61, %[sGoff], 229376\n"
  "ds_read_b64_tr_b16 v[88:89], v130 offset:1536\n"
  "buffer_load_dwordx4 %[vGU], %[srdU], s61 offen lds\n"
  "v_mfma_f32_16x16x32_bf16 v[56:59], v[120:123], a[220:223], v[56:59]\n"
  "s_waitcnt lgkmcnt(13)\n"
  "ds_read_b64_tr_b16 v[90:91], v130 offset:5632\n"
  "v_mfma_f32_16x16x32_bf16 v[28:31], v[124:127], a[216:219], v[28:31]\n"
  "ds_read_b64_tr_b16 v[92:93], v131 offset:1536\n"
  "v_mfma_f32_16x16x32_bf16 v[60:63], v[124:127], a[220:223], v[60:63]\n"
  "s_waitcnt lgkmcnt(13)\n"
  "s_waitcnt vmcnt(14)\n"
  "ds_read_b64_tr_b16 v[94:95], v131 offset:5632\n"
  "v_mfma_f32_16x16x32_bf16 v[0:3], v[64:67], a[224:227], v[0:3]\n"
  "ds_read_b64_tr_b16 v[96:97], v130 offset:8192\n"
  "v_mfma_f32_16x16x32_bf16 v[32:35], v[64:67], a[228:231], v[32:35]\n"
  "s_waitcnt lgkmcnt(13)\n"
  "ds_read_b64_tr_b16 v[98:99], v130 offset:12288\n"
  "v_mfma_f32_16x16x32_bf16 v[4:7], v[68:71], a[224:227], v[4:7]\n"
  "s_add_u32 m0, %[sM0], 133120\n"
  "ds_read_b64_tr_b16 v[100:101], v131 offset:8192\n"
  "v_mfma_f32_16x16x32_bf16 v[36:39], v[68:71], a[228:231], v[36:39]\n"
  "s_waitcnt lgkmcnt(13)\n"
  "ds_read_b64_tr_b16 v[102:103], v131 offset:12288\n"
  "v_mfma_f32_16x16x32_bf16 v[8:11], v[72:75], a[224:227], v[8:11]\n"
  "s_add_u32 s60, %[sDnoff], 128\n"
  "ds_read_b64_tr_b16 v[104:105], v130 offset:8704\n"
  "buffer_load_dwordx4 %[vDn], %[srdDn], s60 offen lds\n"
  "v_mfma_f32_16x16x32_bf16 v[40:43], v[72:75], a[228:231], v[40:43]\n"
  "s_waitcnt lgkmcnt(13)\n"
  "ds_read_b64_tr_b16 v[106:107], v130 offset:12800\n"
  "v_mfma_f32_16x16x32_bf16 v[12:15], v[76:79], a[224:227], v[12:15]\n"
  "ds_read_b64_tr_b16 v[108:109], v131 offset:8704\n"
  "v_mfma_f32_16x16x32_bf16 v[44:47], v[76:79], a[228:231], v[44:47]\n"
  "s_waitcnt lgkmcnt(13)\n"
  "ds_read_b64_tr_b16 v[110:111], v131 offset:12800\n"
  "v_mfma_f32_16x16x32_bf16 v[16:19], v[80:83], a[224:227], v[16:19]\n"
  "ds_read_b64_tr_b16 v[112:113], v130 offset:9216\n"
  "v_mfma_f32_16x16x32_bf16 v[48:51], v[80:83], a[228:231], v[48:51]\n"
  "s_waitcnt lgkmcnt(13)\n"
  "ds_read_b64_tr_b16 v[114:115], v130 offset:13312\n"
  "v_mfma_f32_16x16x32_bf16 v[20:23], v[84:87], a[224:227], v[20:23]\n"
  "s_add_u32 m0, %[sM0], 101376\n"
  "ds_read_b64_tr_b16 v[116:117], v131 offset:9216\n"
  "v_mfma_f32_16x16x32_bf16 v[52:55], v[84:87], a[228:231], v[52:55]\n"
  "s_waitcnt lgkmcnt(13)\n"
  "ds_read_b64_tr_b16 v[118:119], v131 offset:13312\n"
  "v_mfma_f32_16x16x32_bf16 v[24:27], v[88:91], a[224:227], v[24:27]\n"
  "s_add_u32 s61, %[sGoff], 344064\n"
  "ds_read_b64_tr_b16 v[120:121], v130 offset:9728\n"
  "buffer_load_dwordx4 %[vGU], %[srdG], s61 offen lds\n"
  "v_mfma_f32_16x16x32_bf16 v[56:59], v[88:91], a[228:231], v[56:59]\n"
  "s_waitcnt lgkmcnt(13)\n"
  "ds_read_b64_tr_b16 v[122:123], v130 offset:13824\n"
  "v_mfma_f32_16x16x32_bf16 v[28:31], v[92:95], a[224:227], v[28:31]\n"
  "ds_read_b64_tr_b16 v[124:125], v131 offset:9728\n"
  "v_mfma_f32_16x16x32_bf16 v[60:63], v[92:95], a[228:231], v[60:63]\n"
  "s_waitcnt lgkmcnt(13)\n"
  "s_waitcnt vmcnt(14)\n"
  "ds_read_b64_tr_b16 v[126:127], v131 offset:13824\n"
  "s_waitcnt vmcnt(22)\n"
  "s_barrier\n"
  "v_mfma_f32_16x16x32_bf16 v[0:3], v[96:99], a[232:235], v[0:3]\n"
  "ds_read_b64_tr_b16 v[64:65], v130 offset:16384\n"
  "v_mfma_f32_16x16x32_bf16 v[32:35], v[96:99], a[236:239], v[32:35]\n"
  "s_waitcnt lgkmcnt(13)\n"
  "ds_read_b64_tr_b16 v[66:67], v130 offset:20480\n"
  "v_mfma_f32_16x16x32_bf16 v[4:7], v[100:103], a[232:235], v[4:7]\n"
  "s_add_u32 m0, %[sM0], 117760\n"
  "ds_read_b64_tr_b16 v[68:69], v131 offset:16384\n"
  "v_mfma_f32_16x16x32_bf16 v[36:39], v[100:103], a[236:239], v[36:39]\n"
  "s_waitcnt lgkmcnt(13)\n"
  "ds_read_b64_tr_b16 v[70:71], v131 offset:20480\n"
  "v_mfma_f32_16x16x32_bf16 v[8:11], v[104:107], a[232:235], v[8:11]\n"
  "s_add_u32 s60, %[sGoff], 344064\n"
  "ds_read_b64_tr_b16 v[72:73], v130 offset:16896\n"
  "buffer_load_dwordx4 %[vGU], %[srdU], s60 offen lds\n"
  "v_mfma_f32_16x16x32_bf16 v[40:43], v[104:107], a[236:239], v[40:43]\n"
  "s_waitcnt lgkmcnt(13)\n"
  "ds_read_b64_tr_b16 v[74:75], v130 offset:20992\n"
  "v_mfma_f32_16x16x32_bf16 v[12:15], v[108:111], a[232:235], v[12:15]\n"
  "ds_read_b64_tr_b16 v[76:77], v131 offset:16896\n"
  "v_mfma_f32_16x16x32_bf16 v[44:47], v[108:111], a[236:239], v[44:47]\n"
  "s_waitcnt lgkmcnt(13)\n"
  "ds_read_b64_tr_b16 v[78:79], v131 offset:20992\n"
  "v_mfma_f32_16x16x32_bf16 v[16:19], v[112:115], a[232:235], v[16:19]\n"
  "ds_read_b64_tr_b16 v[80:81], v130 offset:17408\n"
  "v_mfma_f32_16x16x32_bf16 v[48:51], v[112:115], a[236:239], v[48:51]\n"
  "s_waitcnt lgkmcnt(13)\n"
  "ds_read_b64_tr_b16 v[82:83], v130 offset:21504\n"
  "v_mfma_f32_16x16x32_bf16 v[20:23], v[116:119], a[232:235], v[20:23]\n"
  "s_add_u32 m0, %[sM0], 134144\n"
  "ds_read_b64_tr_b16 v[84:85], v131 offset:17408\n"
  "v_mfma_f32_16x16x32_bf16 v[52:55], v[116:119], a[236:239], v[52:55]\n"
  "s_waitcnt lgkmcnt(13)\n"
  "ds_read_b64_tr_b16 v[86:87], v131 offset:21504\n"
  "v_mfma_f32_16x16x32_bf16 v[24:27], v[120:123], a[232:235], v[24:27]\n"
  "s_add_u32 s61, %[sDnoff], 192\n"
  "ds_read_b64_tr_b16 v[88:89], v130 offset:17920\n"
  "buffer_load_dwordx4 %[vDn], %[srdDn], s61 offen lds\n"
  "v_mfma_f32_16x16x32_bf16 v[56:59], v[120:123], a[236:239], v[56:59]\n"
  "s_waitcnt lgkmcnt(13)\n"
  "ds_read_b64_tr_b16 v[90:91], v130 offset:22016\n"
  "v_mfma_f32_16x16x32_bf16 v[28:31], v[124:127], a[232:235], v[28:31]\n"
  "ds_read_b64_tr_b16 v[92:93], v131 offset:17920\n"
  "v_mfma_f32_16x16x32_bf16 v[60:63], v[124:127], a[236:239], v[60:63]\n"
  "s_waitcnt lgkmcnt(13)\n"
  "s_waitcnt vmcnt(14)\n"
  "ds_read_b64_tr_b16 v[94:95], v131 offset:22016\n"
  "v_mfma_f32_16x16x32_bf16 v[0:3], v[64:67], a[240:243], v[0:3]\n"
  "ds_read_b64_tr_b16 v[96:97], v130 offset:24576\n"
  "v_mfma_f32_16x16x32_bf16 v[32:35], v[64:67], a[244:247], v[32:35]\n"
  "s_waitcnt lgkmcnt(13)\n"
  "ds_read_b64_tr_b16 v[98:99], v130 offset:28672\n"
  "v_mfma_f32_16x16x32_bf16 v[4:7], v[68:71], a[240:243], v[4:7]\n"
  "ds_read_b64_tr_b16 v[100:101], v131 offset:24576\n"
  "v_mfma_f32_16x16x32_bf16 v[36:39], v[68:71], a[244:247], v[36:39]\n"
  "s_waitcnt lgkmcnt(13)\n"
  "ds_read_b64_tr_b16 v[102:103], v131 offset:28672\n"
  "v_mfma_f32_16x16x32_bf16 v[8:11], v[72:75], a[240:243], v[8:11]\n"
  "ds_read_b64_tr_b16 v[104:105], v130 offset:25088\n"
  "v_mfma_f32_16x16x32_bf16 v[40:43], v[72:75], a[244:247], v[40:43]\n"
  "s_waitcnt lgkmcnt(13)\n"
  "ds_read_b64_tr_b16 v[106:107], v130 offset:29184\n"
  "v_mfma_f32_16x16x32_bf16 v[12:15], v[76:79], a[240:243], v[12:15]\n"
  "ds_read_b64_tr_b16 v[108:109], v131 offset:25088\n"
  "v_mfma_f32_16x16x32_bf16 v[44:47], v[76:79], a[244:247], v[44:47]\n"
  "s_waitcnt lgkmcnt(13)\n"
  "ds_read_b64_tr_b16 v[110:111], v131 offset:29184\n"
  "v_mfma_f32_16x16x32_bf16 v[16:19], v[80:83], a[240:243], v[16:19]\n"
  "ds_read_b64_tr_b16 v[112:113], v130 offset:25600\n"
  "v_mfma_f32_16x16x32_bf16 v[48:51], v[80:83], a[244:247], v[48:51]\n"
  "s_waitcnt lgkmcnt(13)\n"
  "ds_read_b64_tr_b16 v[114:115], v130 offset:29696\n"
  "v_mfma_f32_16x16x32_bf16 v[20:23], v[84:87], a[240:243], v[20:23]\n"
  "ds_read_b64_tr_b16 v[116:117], v131 offset:25600\n"
  "v_mfma_f32_16x16x32_bf16 v[52:55], v[84:87], a[244:247], v[52:55]\n"
  "s_waitcnt lgkmcnt(13)\n"
  "ds_read_b64_tr_b16 v[118:119], v131 offset:29696\n"
  "v_mfma_f32_16x16x32_bf16 v[24:27], v[88:91], a[240:243], v[24:27]\n"
  "ds_read_b64_tr_b16 v[120:121], v130 offset:26112\n"
  "v_mfma_f32_16x16x32_bf16 v[56:59], v[88:91], a[244:247], v[56:59]\n"
  "s_waitcnt lgkmcnt(13)\n"
  "ds_read_b64_tr_b16 v[122:123], v130 offset:30208\n"
  "v_mfma_f32_16x16x32_bf16 v[28:31], v[92:95], a[240:243], v[28:31]\n"
  "ds_read_b64_tr_b16 v[124:125], v131 offset:26112\n"
  "v_mfma_f32_16x16x32_bf16 v[60:63], v[92:95], a[244:247], v[60:63]\n"
  "s_waitcnt lgkmcnt(13)\n"
  "s_waitcnt vmcnt(12)\n"
  "ds_read_b64_tr_b16 v[126:127], v131 offset:30208\n"
  "v_mfma_f32_16x16x32_bf16 v[0:3], v[96:99], a[248:251], v[0:3]\n"
  "v_mfma_f32_16x16x32_bf16 v[32:35], v[96:99], a[252:255], v[32:35]\n"
  "s_waitcnt lgkmcnt(12)\n"
  "v_mfma_f32_16x16x32_bf16 v[4:7], v[100:103], a[248:251], v[4:7]\n"
  "v_mfma_f32_16x16x32_bf16 v[36:39], v[100:103], a[252:255], v[36:39]\n"
  "s_waitcnt lgkmcnt(10)\n"
  "v_mfma_f32_16x16x32_bf16 v[8:11], v[104:107], a[248:251], v[8:11]\n"
  "v_mfma_f32_16x16x32_bf16 v[40:43], v[104:107], a[252:255], v[40:43]\n"
  "s_waitcnt lgkmcnt(8)\n"
  "v_mfma_f32_16x16x32_bf16 v[12:15], v[108:111], a[248:251], v[12:15]\n"
  "v_mfma_f32_16x16x32_bf16 v[44:47], v[108:111], a[252:255], v[44:47]\n"
  "s_waitcnt lgkmcnt(6)\n"
  "v_mfma_f32_16x16x32_bf16 v[16:19], v[112:115], a[248:251], v[16:19]\n"
  "v_mfma_f32_16x16x32_bf16 v[48:51], v[112:115], a[252:255], v[48:51]\n"
  "s_waitcnt lgkmcnt(4)\n"
  "v_mfma_f32_16x16x32_bf16 v[20:23], v[116:119], a[248:251], v[20:23]\n"
  "v_mfma_f32_16x16x32_bf16 v[52:55], v[116:119], a[252:255], v[52:55]\n"
  "s_waitcnt lgkmcnt(2)\n"
  "v_mfma_f32_16x16x32_bf16 v[24:27], v[120:123], a[248:251], v[24:27]\n"
  "v_mfma_f32_16x16x32_bf16 v[56:59], v[120:123], a[252:255], v[56:59]\n"
  "s_waitcnt lgkmcnt(0)\n"
  "v_mfma_f32_16x16x32_bf16 v[28:31], v[124:127], a[248:251], v[28:31]\n"
  "v_mfma_f32_16x16x32_bf16 v[60:63], v[124:127], a[252:255], v[60:63]\n"
  "s_add_u32 s60, %[sPoff], 0\n"
  "s_add_u32 s61, %[sPoff], 16384\n"
  "v_cvt_pk_bf16_f32 v0, v0, v1\n"
  "v_cvt_pk_bf16_f32 v1, v2, v3\n"
  "v_cvt_pk_bf16_f32 v2, v4, v5\n"
  "v_cvt_pk_bf16_f32 v3, v6, v7\n"
  "buffer_store_dwordx4 v[0:3], %[vL16], %[srdP], s60 offen offset:0 sc1\n"
  "v_cvt_pk_bf16_f32 v32, v32, v33\n"
  "v_cvt_pk_bf16_f32 v33, v34, v35\n"
  "v_cvt_pk_bf16_f32 v34, v36, v37\n"
  "v_cvt_pk_bf16_f32 v35, v38, v39\n"
  "buffer_store_dwordx4 v[32:35], %[vL16], %[srdP], s61 offen offset:0 sc1\n"
  "v_cvt_pk_bf16_f32 v8, v8, v9\n"
  "v_cvt_pk_bf16_f32 v9, v10, v11\n"
  "v_cvt_pk_bf16_f32 v10, v12, v13\n"
  "v_cvt_pk_bf16_f32 v11, v14, v15\n"
  "buffer_store_dwordx4 v[8:11], %[vL16], %[srdP], s60 offen offset:1024 sc1\n"
  "v_cvt_pk_bf16_f32 v40, v40, v41\n"
  "v_cvt_pk_bf16_f32 v41, v42, v43\n"
  "v_cvt_pk_bf16_f32 v42, v44, v45\n"
  "v_cvt_pk_bf16_f32 v43, v46, v47\n"
  "buffer_store_dwordx4 v[40:43], %[vL16], %[srdP], s61 offen offset:1024 sc1\n"
  "v_cvt_pk_bf16_f32 v16, v16, v17\n"
  "v_cvt_pk_bf16_f32 v17, v18, v19\n"
  "v_cvt_pk_bf16_f32 v18, v20, v21\n"
  "v_cvt_pk_bf16_f32 v19, v22, v23\n"
  "buffer_store_dwordx4 v[16:19], %[vL16], %[srdP], s60 offen offset:2048 sc1\n"
  "v_cvt_pk_bf16_f32 v48, v48, v49\n"
  "v_cvt_pk_bf16_f32 v49, v50, v51\n"
  "v_cvt_pk_bf16_f32 v50, v52, v53\n"
  "v_cvt_pk_bf16_f32 v51, v54, v55\n"
  "buffer_store_dwordx4 v[48:51], %[vL16], %[srdP], s61 offen offset:2048 sc1\n"
  "v_cvt_pk_bf16_f32 v24, v24, v25\n"
  "v_cvt_pk_bf16_f32 v25, v26, v27\n"
  "v_cvt_pk_bf16_f32 v26, v28, v29\n"
  "v_cvt_pk_bf16_f32 v27, v30, v31\n"
  "buffer_store_dwordx4 v[24:27], %[vL16], %[srdP], s60 offen offset:3072 sc1\n"
  "v_cvt_pk_bf16_f32 v56, v56, v57\n"
  "v_cvt_pk_bf16_f32 v57, v58, v59\n"
  "v_cvt_pk_bf16_f32 v58, v60, v61\n"
  "v_cvt_pk_bf16_f32 v59, v62, v63\n"
  "buffer_store_dwordx4 v[56:59], %[vL16], %[srdP], s61 offen offset:3072 sc1\n"
  "s_waitcnt vmcnt(0) lgkmcnt(0)\n"

    :
    : [srdO] "s"(srdO), [srdW] "s"(srdWo), [srdP] "s"(srdP), [sOoff] "s"(cOoff), [sWoff] "s"(cWoff), [sM0] "s"(sM0), [sPoff] "s"(cPoff), [sWid] "s"(wid), [pCnt] "s"(pCnt),
      [srdG] "s"(srdG), [srdU] "s"(srdU), [srdDn] "s"(srdDn), [sGoff] "s"(sGoff), [sDnoff] "s"(sDnoff), [vGU] "v"(vGU), [vDn] "v"(vDn),
      [vWb] "v"(vVb), [vWd] "v"(vCd), [vL16] "v"(vL16)
    : "memory", "scc", "s60", "s61", "s62", "s63",
      CLOB_0_99("v"), CLOB8("v", 10), CLOB8("v", 11), CLOB8("v", 12), "v130", "v131", "v132", "v133", CLOB_0_99("a"), CLOB_100_239("a"), CLOB_240_255("a"));
  if ((vL16 >> 4) == 0u) __hip_atomic_fetch_add((GAS unsigned*)cnt + (64u + (crb0 >> 1)) * 32u, 1u, __ATOMIC_RELAXED, __HIP_MEMORY_SCOPE_AGENT);
  tail(lds, b, wid, part, x, mod, wr, xsel, cnt - 8 * SEQ, cnt + 64 * 32, yp, out KS_TPASS);
  KS_END
}

extern "C" __global__ void k_end() {}

extern "C" void kernel_launch(void* const* d_in, const int* in_sizes, int n_in, void* d_out, int out_size, void* d_ws, size_t ws_size,
                              hipStream_t stream) {
  const u16* x = (const u16*)d_in[0]; const u16* kc = (const u16*)d_in[1]; const u16* vc = (const u16*)d_in[2];
  const u16* wq = (const u16*)d_in[3]; const u16* wo = (const u16*)d_in[4];
  const u16* wr = (const u16*)d_in[6]; const u16* wg = (const u16*)d_in[7]; const u16* wu = (const u16*)d_in[8]; const u16* wd = (const u16*)d_in[9];
  const u16* c = (const u16*)d_in[10]; const u16* wmod = (const u16*)d_in[11];
  char* ws = (char*)d_ws; u16* out = (u16*)d_out;
  float* mod = (float*)(ws + WS_MOD); unsigned* keys = (unsigned*)(ws + WS_R);
  u16* hfrag = (u16*)(ws + WS_HFRAG); u16* xsel = (u16*)(ws + WS_XSEL);
  float* yp = (float*)(ws + WS_YP); u16* ofrag = (u16*)(ws + WS_OFRAG); u16* part = (u16*)(ws + WS_PART);
  k_pre<<<480, 256, 0, stream>>>(c, wmod, mod, x, hfrag, wq, kc, vc, keys, yp, wo, out, wg, wu, wd, wr KS_PASS(0));
  k_attn<<<256, 256, 0, stream>>>(hfrag, wq, kc, vc, ofrag, wo, wg, wu, wd, out, keys + 8 * SEQ, part, yp, x, mod, wr, xsel KS_PASS(2));
}
```
